```python
import torch

def _softmax(x, axis=-1):
    return torch.softmax(x, dim=axis)

def _gelu(x):
    return torch.nn.functional.gelu(x, approximate='tanh')

def _silu(x):
    return torch.nn.functional.silu(x)

def _sigmoid(x):
    return torch.sigmoid(x)

def _layernorm(x, axis=-1, eps=1e-05):
    m = x.mean(dim=axis, keepdim=True)
    v = x.var(dim=axis, keepdim=True, unbiased=False)
    return (x - m) / torch.sqrt(v + eps)

def _scatter_add(tgt, idx, src, axis=0):
    return tgt.clone().index_add_(axis, idx, src)

def _segment_sum(src, seg, n):
    out = torch.zeros((n,) + src.shape[1:], dtype=src.dtype)
    return out.index_add_(0, seg, src)

def _pad(x, pw):
    flat = tuple((v for pair in reversed(pw) for v in pair))
    return torch.nn.functional.pad(x, flat)
kernel_name = 'gated_mix__memory_aug_recurrent_mem_tr__ssm_conv_s4__prompt'

def setup_inputs():

    _w = lambda a: (lambda t: t.to(torch.bfloat16) if t.is_floating_point() else t)(torch.as_tensor(a))

    def _A_setup():
        B, S, Dm = (1, 2048, 640)
        H, Dh = (32, 128)
        M, seg = (8, 32)
        seg = min(seg, S)
        S = S // seg * seg
        return {'x': _w((lambda *s: torch.randn(*s, dtype=torch.bfloat16) * 0.05)(B, S, Dm)), 'mem0': _w((lambda *s: torch.randn(*s, dtype=torch.bfloat16) * 0.4)(B, M, Dm)), 'Wq': _w((lambda *s: torch.randn(*s, dtype=torch.bfloat16) * 6.4)(Dm, H, Dh) * Dm ** (-0.5)), 'Wk': _w((lambda *s: torch.randn(*s, dtype=torch.bfloat16) * 6.4)(Dm, H, Dh) * Dm ** (-0.5)), 'Wv': _w((lambda *s: torch.randn(*s, dtype=torch.bfloat16) * 0.4)(Dm, H, Dh) * Dm ** (-0.5)), 'Wo': _w((lambda *s: torch.randn(*s, dtype=torch.bfloat16) * 0.4)(H, Dh, Dm) * (H * Dh) ** (-0.5)), 'seg_len': seg}
    _A_in = _A_setup()

    def _B_setup():
        B, L, D, N = (1, 2048, 640, 128)
        A_re = -(lambda _x: torch.exp(torch.as_tensor(_x)))((lambda *s: torch.randn(*s, dtype=torch.bfloat16) * 6.4)(D, N))
        A_im = torch.pi * (lambda *s: torch.randn(*s, dtype=torch.bfloat16) * 0.05)(D, N)
        B_re = (lambda *s: torch.randn(*s, dtype=torch.bfloat16) * 0.1)(D, N)
        B_im = (lambda *s: torch.randn(*s, dtype=torch.bfloat16) * 0.1)(D, N)
        C_re = (lambda *s: torch.randn(*s, dtype=torch.bfloat16) * 0.1)(D, N)
        C_im = (lambda *s: torch.randn(*s, dtype=torch.bfloat16) * 0.1)(D, N)
        log_dt = (lambda *s: torch.randn(*s, dtype=torch.bfloat16) * 0.05)(D) * 0.5 - 4.0
        Dskip = (lambda *s: torch.randn(*s, dtype=torch.bfloat16) * 0.05)(D)
        return {'u': _w((lambda *s: torch.randn(*s, dtype=torch.bfloat16) * 0.05)(B, L, D)), 'A_re': _w(A_re), 'A_im': _w(A_im), 'B_re': _w(B_re), 'B_im': _w(B_im), 'C_re': _w(C_re), 'C_im': _w(C_im), 'log_dt': _w(log_dt), 'Dskip': _w(Dskip)}
    _B_in = _B_setup()
    return {'A_x': _A_in['x'], 'A_mem0': _A_in['mem0'], 'A_Wq': _A_in['Wq'], 'A_Wk': _A_in['Wk'], 'A_Wv': _A_in['Wv'], 'A_Wo': _A_in['Wo'], 'A_seg_len': _A_in['seg_len'], 'B_u': _B_in['u'], 'B_A_re': _B_in['A_re'], 'B_A_im': _B_in['A_im'], 'B_B_re': _B_in['B_re'], 'B_B_im': _B_in['B_im'], 'B_C_re': _B_in['C_re'], 'B_C_im': _B_in['C_im'], 'B_log_dt': _B_in['log_dt'], 'B_Dskip': _B_in['Dskip'], 'gate_w': _w((lambda *s: torch.randn(*s, dtype=torch.bfloat16) * 0.05)(640) * 16.0)}

def reference(A_x, A_mem0, A_Wq, A_Wk, A_Wv, A_Wo, A_seg_len, B_u, B_A_re, B_A_im, B_B_re, B_B_im, B_C_re, B_C_im, B_log_dt, B_Dskip, gate_w):

    def _A_ref(x, mem0, Wq, Wk, Wv, Wo, seg_len):
        B, S, Dm = x.shape
        H, Dh = (Wq.shape[1], Wq.shape[2])
        M = mem0.shape[1]
        scale = Dh ** (-0.5)
        n_seg = S // seg_len
        L = M + seg_len
        idx = torch.arange(L)
        mask = (idx[:, None] < M) | (idx[:, None] >= idx[None, :])
        mem = mem0
        outs = []
        for s in range(n_seg):
            xs = x[:, s * seg_len:(s + 1) * seg_len, :].clone()
            h = torch.concatenate([mem, xs], axis=1)
            q = torch.einsum('btd,dhe->bhte', h, Wq)
            k = torch.einsum('btd,dhe->bhte', h, Wk)
            v = torch.einsum('btd,dhe->bhte', h, Wv)
            scores = torch.einsum('bhqe,bhke->bhqk', q, k) * scale
            scores = torch.where(mask[None, None], scores, -1000000000.0)
            p = _softmax(scores, axis=-1)
            o = torch.einsum('bhqk,bhke->bhqe', p, v)
            o = torch.einsum('bhte,hed->btd', o, Wo)
            mem = o[:, :M, :].clone()
            outs.append(o[:, M:, :])
        return torch.concatenate(outs, axis=1)

    def _B_ref(u, A_re, A_im, B_re, B_im, C_re, C_im, log_dt, Dskip):
        Bsz, L, D = u.shape
        dt = torch.exp(log_dt)[:, None]
        A = A_re + 1j * A_im
        Bc = B_re + 1j * B_im
        Cc = C_re + 1j * C_im
        logdA = dt * A
        dB = dt * Bc
        l = (lambda x, dt: x.to(dt))(torch.arange(L), A_re.dtype)
        P = torch.exp(logdA[:, :, None] * l[None, None, :])
        K = torch.einsum('dn,dnl,dn->dl', Cc, P, dB).real
        L2 = 2 * L
        Kp = _pad(K, ((0, 0), (0, L)))
        up = (lambda x, *d: x.permute(*d))(u, 0, 2, 1)
        up = _pad(up, ((0, 0), (0, 0), (0, L)))
        Kf = (lambda x, axes=(-2, -1): torch.fft.rfft2(x.float(), dim=axes))(Kp[:, None, :], axes=(-2, -1))
        Uf = (lambda x, axes=(-2, -1): torch.fft.rfft2(x.float(), dim=axes))(up[:, :, None, :], axes=(-2, -1))
        Yf = Uf * Kf[None, :, :, :]
        y = (lambda x, s, axes=(-2, -1): torch.fft.irfft2(x, s=s, dim=axes).to(torch.bfloat16))(Yf, (1, L2), axes=(-2, -1))[:, :, 0, :L]
        y = (lambda x, *d: x.permute(*d))(y, 0, 2, 1) + u * Dskip[None, None, :]
        return y
    x = A_x
    g = _sigmoid((x * gate_w).sum(-1, keepdims=True))
    a = _A_ref(x, A_mem0, A_Wq, A_Wk, A_Wv, A_Wo, A_seg_len)
    b = _B_ref(x, B_A_re, B_A_im, B_B_re, B_B_im, B_C_re, B_C_im, B_log_dt, B_Dskip)
    return g * a + (1 - g) * b
```

```cpp
#include <hip/hip_runtime.h>
#include <hip/hip_bf16.h>
#include <stdint.h>

typedef unsigned short u16;
__device__ __forceinline__ float bf2f(u16 b) { return __uint_as_float(((unsigned)b) << 16); }
__device__ __forceinline__ u16 f2bf(float f) { __hip_bfloat16 h = __float2bfloat16(f); return *(u16*)&h; }
__device__ __forceinline__ float rbf(float f) { return bf2f(f2bf(f)); }

constexpr int S = 2048, DM = 640, H = 32, DH = 128, M = 8, SEG = 32, NSEG = 64, HD = H * DH, NS = 128;

__global__ void gemm_naive(const u16* A, const u16* W, u16* C, int R, int K, int N) {
    int n = blockIdx.x * blockDim.x + threadIdx.x;
    int r0 = blockIdx.y * 8;
    if (n >= N) return;
    float acc[8];
    for (int i = 0; i < 8; ++i) acc[i] = 0.f;
    for (int k = 0; k < K; ++k) {
        float w = bf2f(W[(size_t)k * N + n]);
#pragma unroll
        for (int i = 0; i < 8; ++i) acc[i] += bf2f(A[(size_t)(r0 + i) * K + k]) * w;
    }
#pragma unroll
    for (int i = 0; i < 8; ++i) C[(size_t)(r0 + i) * N + n] = f2bf(acc[i]);
}

__global__ void attn_naive(const u16* qm, const u16* km, const u16* vm, const u16* qx, const u16* kx, const u16* vx, u16* o) {
    int h = blockIdx.x, r = blockIdx.y, t = threadIdx.x;
    __shared__ float sc[40];
    __shared__ float qs[128];
    const u16* q = (r < M) ? qm + (size_t)r * HD + h * DH : qx + (size_t)(r - M) * HD + h * DH;
    qs[t] = bf2f(q[t]);
    __syncthreads();
    if (t < 40) {
        const u16* k = (t < M) ? km + (size_t)t * HD + h * DH : kx + (size_t)(t - M) * HD + h * DH;
        float s = 0.f;
        for (int e = 0; e < DH; ++e) s += qs[e] * bf2f(k[e]);
        s = rbf(rbf(s) * rbf(0.08838834764831845f));
        bool ok = (r < M) || (r >= t);
        sc[t] = ok ? s : -1e9f;
    }
    __syncthreads();
    float mx = -3e38f;
    for (int j = 0; j < 40; ++j) mx = fmaxf(mx, sc[j]);
    float sum = 0.f;
    for (int j = 0; j < 40; ++j) sum += expf(sc[j] - mx);
    float acc = 0.f;
    for (int j = 0; j < 40; ++j) {
        float p = rbf(expf(sc[j] - mx) / sum);
        const u16* v = (j < M) ? vm + (size_t)j * HD + h * DH : vx + (size_t)(j - M) * HD + h * DH;
        acc += p * bf2f(v[t]);
    }
    o[(size_t)r * HD + h * DH + t] = f2bf(acc);
}

__global__ void ssm_k(const u16* Are, const u16* Aim, const u16* Bre, const u16* Bim, const u16* Cre, const u16* Cim, const u16* logdt, float* K) {
    int d = blockIdx.x;
    __shared__ float a[NS], b[NS], wr[NS], wi[NS];
    float dt = rbf(expf(bf2f(logdt[d])));
    if (threadIdx.x < NS) {
        int n = threadIdx.x;
        a[n] = dt * bf2f(Are[d * NS + n]);
        b[n] = dt * bf2f(Aim[d * NS + n]);
        float br = dt * bf2f(Bre[d * NS + n]), bi = dt * bf2f(Bim[d * NS + n]);
        float cr = bf2f(Cre[d * NS + n]), ci = bf2f(Cim[d * NS + n]);
        wr[n] = cr * br - ci * bi;
        wi[n] = cr * bi + ci * br;
    }
    __syncthreads();
    for (int l = threadIdx.x; l < S; l += blockDim.x) {
        float lf = rbf((float)l);
        float acc = 0.f;
        for (int n = 0; n < NS; ++n) {
            float e = expf(a[n] * lf);
            float s, c;
            sincosf(b[n] * lf, &s, &c);
            acc += e * (wr[n] * c - wi[n] * s);
        }
        K[(size_t)d * S + l] = acc;
    }
}

__global__ void ssm_conv(const u16* x, const float* K, float* y) {
    int d = blockIdx.x;
    __shared__ float ks[S], us[S];
    for (int l = threadIdx.x; l < S; l += blockDim.x) { ks[l] = K[(size_t)d * S + l]; us[l] = bf2f(x[(size_t)l * DM + d]); }
    __syncthreads();
    for (int l = threadIdx.x; l < S; l += blockDim.x) {
        float acc = 0.f;
        for (int j = 0; j <= l; ++j) acc += ks[j] * us[l - j];
        y[(size_t)l * DM + d] = acc;
    }
}

__global__ void combine(const u16* x, const u16* gw, const u16* dskip, const u16* a, const float* y, u16* out) {
    int l = blockIdx.x;
    __shared__ float red[256];
    float s = 0.f;
    for (int d = threadIdx.x; d < DM; d += 256) s += rbf(bf2f(x[(size_t)l * DM + d]) * bf2f(gw[d]));
    red[threadIdx.x] = s;
    __syncthreads();
    for (int o = 128; o > 0; o >>= 1) { if (threadIdx.x < o) red[threadIdx.x] += red[threadIdx.x + o]; __syncthreads(); }
    float z = rbf(red[0]);
    float g = rbf(1.f / (1.f + expf(-z)));
    float omg = rbf(1.f - g);
    for (int d = threadIdx.x; d < DM; d += 256) {
        float u = bf2f(x[(size_t)l * DM + d]);
        float b = rbf(rbf(y[(size_t)l * DM + d]) + rbf(u * bf2f(dskip[d])));
        float av = bf2f(a[(size_t)l * DM + d]);
        out[(size_t)l * DM + d] = f2bf(rbf(g * av) + rbf(omg * b));
    }
}

extern "C" void kernel_launch(void* const* d_in, const int* in_sizes, int n_in,
                              void* d_out, int out_size, void* d_ws, size_t ws_size,
                              hipStream_t stream) {
    const u16* x = (const u16*)d_in[0];
    const u16* mem0 = (const u16*)d_in[1];
    const u16* Wq = (const u16*)d_in[2];
    const u16* Wk = (const u16*)d_in[3];
    const u16* Wv = (const u16*)d_in[4];
    const u16* Wo = (const u16*)d_in[5];
    const u16* Are = (const u16*)d_in[8];
    const u16* Aim = (const u16*)d_in[9];
    const u16* Bre = (const u16*)d_in[10];
    const u16* Bim = (const u16*)d_in[11];
    const u16* Cre = (const u16*)d_in[12];
    const u16* Cim = (const u16*)d_in[13];
    const u16* logdt = (const u16*)d_in[14];
    const u16* dskip = (const u16*)d_in[15];
    const u16* gw = (const u16*)d_in[16];
    u16* out = (u16*)d_out;
    char* ws = (char*)d_ws;
    u16* qx = (u16*)ws; ws += (size_t)S * HD * 2;
    u16* kx = (u16*)ws; ws += (size_t)S * HD * 2;
    u16* vx = (u16*)ws; ws += (size_t)S * HD * 2;
    u16* qm = (u16*)ws; ws += (size_t)M * HD * 2;
    u16* km = (u16*)ws; ws += (size_t)M * HD * 2;
    u16* vm = (u16*)ws; ws += (size_t)M * HD * 2;
    u16* o = (u16*)ws; ws += (size_t)40 * HD * 2;
    u16* mem = (u16*)ws; ws += (size_t)M * DM * 2;
    u16* a = (u16*)ws; ws += (size_t)S * DM * 2;
    float* K = (float*)ws; ws += (size_t)DM * S * 4;
    float* y = (float*)ws; ws += (size_t)DM * S * 4;
    gemm_naive<<<dim3(HD / 256, S / 8), 256, 0, stream>>>(x, Wq, qx, S, DM, HD);
    gemm_naive<<<dim3(HD / 256, S / 8), 256, 0, stream>>>(x, Wk, kx, S, DM, HD);
    gemm_naive<<<dim3(HD / 256, S / 8), 256, 0, stream>>>(x, Wv, vx, S, DM, HD);
    for (int s = 0; s < NSEG; ++s) {
        const u16* m = s == 0 ? mem0 : mem;
        gemm_naive<<<dim3(HD / 64, 1), 64, 0, stream>>>(m, Wq, qm, M, DM, HD);
        gemm_naive<<<dim3(HD / 64, 1), 64, 0, stream>>>(m, Wk, km, M, DM, HD);
        gemm_naive<<<dim3(HD / 64, 1), 64, 0, stream>>>(m, Wv, vm, M, DM, HD);
        attn_naive<<<dim3(H, 40), 128, 0, stream>>>(qm, km, vm, qx + (size_t)s * SEG * HD, kx + (size_t)s * SEG * HD, vx + (size_t)s * SEG * HD, o);
        gemm_naive<<<dim3(DM / 64, 1), 64, 0, stream>>>(o, Wo, mem, M, HD, DM);
        gemm_naive<<<dim3(DM / 64, 4), 64, 0, stream>>>(o + (size_t)M * HD, Wo, a + (size_t)s * SEG * DM, SEG, HD, DM);
    }
    ssm_k<<<DM, 256, 0, stream>>>(Are, Aim, Bre, Bim, Cre, Cim, logdt, K);
    ssm_conv<<<DM, 256, 0, stream>>>(x, K, y);
    combine<<<S, 256, 0, stream>>>(x, gw, dskip, a, y, out);
}
```

```cpp
#include <hip/hip_runtime.h>
#include <hip/hip_bf16.h>
#include <stdint.h>

typedef unsigned short u16;
typedef unsigned int u32;
typedef u32 v4u __attribute__((ext_vector_type(4)));
typedef u32 v2u __attribute__((ext_vector_type(2)));
typedef u32 v8u __attribute__((ext_vector_type(8)));
typedef float f32x4 __attribute__((ext_vector_type(4)));
typedef float f32x2 __attribute__((ext_vector_type(2)));
typedef __bf16 bf16x8 __attribute__((ext_vector_type(8)));
typedef __bf16 bf16x2 __attribute__((ext_vector_type(2)));
typedef short s16x4 __attribute__((ext_vector_type(4)));
typedef __amdgpu_buffer_rsrc_t rsrc_t;
#define GAS __attribute__((address_space(1)))
#define LAS __attribute__((address_space(3)))
#define RLX_AGENT __ATOMIC_RELAXED, __HIP_MEMORY_SCOPE_AGENT
#define DEV __device__ __forceinline__

constexpr int S = 2048, DM = 640, H = 32, DH = 128, NSEG = 64, HD = H * DH, NS = 128;
constexpr int SC1 = 16;
constexpr u32 SENT = 0xFFFFFFFFu;

DEV float bf2f(u32 b) { return __uint_as_float(b << 16); }
DEV float bfhi(u32 b) { return __uint_as_float(b & 0xFFFF0000u); }
DEV float rbf(float f) { return (float)(__bf16)f; }
DEV u32 pk2(float lo, float hi) { bf16x2 v = {(__bf16)lo, (__bf16)hi}; return __builtin_bit_cast(u32, v); }
DEV u32 pk2s(float lo, float hi) { return min(pk2(lo, hi), 0xFFFFFFFEu); }
DEV int rfl(int v) { return __builtin_amdgcn_readfirstlane(v); }
DEV int lane_id() { int l; asm volatile("v_mbcnt_lo_u32_b32 %0, -1, 0\n\tv_mbcnt_hi_u32_b32 %0, -1, %0" : "=v"(l)); return l; }

constexpr size_t CTRL_OFF = 0, CTRL_BYTES = 16384;
constexpr size_t HAND_OFF = 2u << 20;
constexpr int SLOT_PHYS = 65536;
constexpr int MEM_O = 0, PX_O = 10240, PM_O = PX_O + 16384, PROBE_O = PM_O + 4096;
constexpr size_t QX_OFF = 6u << 20, QX_BYTES = (size_t)NSEG * H * 8192;
constexpr size_t ZX_OFF = QX_OFF + 2 * QX_BYTES, ZX_BYTES = (size_t)NSEG * 80 * 16384;
constexpr size_t ZM_OFF = ZX_OFF + ZX_BYTES, ZM_BYTES = (size_t)NSEG * 80 * 4096;
constexpr size_t PT_OFF = ZM_OFF + ZM_BYTES, PT_BYTES = (size_t)NSEG * 81920;
constexpr size_t VO_OFF = PT_OFF + PT_BYTES, VO_BYTES = (size_t)80 * 327680;
constexpr size_t BS_OFF = VO_OFF + VO_BYTES, BS_BYTES = (size_t)S * DM * 2;
constexpr size_t WS_END = BS_OFF + BS_BYTES;
constexpr int CW_TMO = 0, CW_PAR = 64, CW_READY = 128, CW_VO = 192, CW_SSM = 256, CW_L = 1024, CW_S = 2048;

DEV int amap(int o) { return (o >> 13) * 16384 + (o & 8191); }
DEV u32 max4(v4u v) { return max(max(v.x, v.y), max(v.z, v.w)); }
DEV v8u cat8(v4u a, v4u b) { return __builtin_shufflevector(a, b, 0, 1, 2, 3, 4, 5, 6, 7); }
DEV rsrc_t mkrs(const void* p, size_t bytes) { return __builtin_amdgcn_make_buffer_rsrc((void*)p, 0, (int)bytes, 0x00020000); }
DEV v4u ld128(rsrc_t r, int off) { return __builtin_amdgcn_raw_buffer_load_b128(r, off, 0, SC1); }
DEV void st128(v4u v, rsrc_t r, int off) { __builtin_amdgcn_raw_buffer_store_b128(v, r, off, 0, SC1); }
DEV void st32(u32 v, rsrc_t r, int off) { __builtin_amdgcn_raw_buffer_store_b32(v, r, off, 0, SC1); }
#define VMCNT(n) asm volatile("s_waitcnt vmcnt(%0)" :: "i"(n) : "memory")
#define BAR() do { asm volatile("s_waitcnt lgkmcnt(0)" ::: "memory"); __builtin_amdgcn_s_barrier(); asm volatile("" ::: "memory"); } while (0)
template <int CTRL> DEV float dppf(float v) { return __builtin_bit_cast(float, __builtin_amdgcn_update_dpp(0, __builtin_bit_cast(int, v), CTRL, 0xf, 0xf, true)); }
DEV float rowsum16(float v) { v += dppf<0x128>(v); v += dppf<0x124>(v); v += dppf<0x122>(v); v += dppf<0x121>(v); return v; }
DEV void dma16(u32 ldsaddr, int voff, rsrc_t rs, int soff) {
    u32 keep;
    asm volatile("s_nop 4\n\ts_mov_b32 %0, m0\n\ts_mov_b32 m0, %1\n\ts_nop 0\n\tbuffer_load_dwordx4 %2, %3, %4 offen lds\n\ts_mov_b32 m0, %0"
                 : "=&s"(keep) : "s"(ldsaddr), "v"(voff), "s"(rs), "s"(soff) : "memory");
}

constexpr u32 SPINCAP = 1u << 20;
#define STAMP(i)
#define STAMP_DECL
#define STAMP_FLUSH(s)
#define STAMP_SET(i, v)
#define STAMP_END()
#define EVT(i)

struct Params {
    const u16 *x, *mem0, *Wq, *Wk, *Wv, *Wo, *Are, *Aim, *Bre, *Bim, *Cre, *Cim, *logdt, *dskip, *gw;
    u16* out;
    unsigned char* ws;
    int dA, dB, dR, pad;
};

constexpr int L_MEMF = 0, L_QF = 10240, L_KMF = 12288, L_KXF = 16384, L_RED = 10240, L_ZT = 16384, L_STAGE = 24576, L_TOTAL = L_STAGE + 131072;

DEV bool spin_fail(u32& spins, GAS u32* ctl) {
    if (++spins > SPINCAP) { __hip_atomic_store(ctl + CW_TMO, 1u, RLX_AGENT); return true; }
    return (spins & 1023u) == 0 && rfl(__hip_atomic_load(ctl + CW_TMO, RLX_AGENT)) != 0;
}
DEV u32 wait_ge(GAS u32* w, u32 want, GAS u32* ctl) {
    u32 spins = 0, v;
    for (;;) { v = rfl(__hip_atomic_load(w, RLX_AGENT)); if (v >= want) break; __builtin_amdgcn_s_sleep(4); if (spin_fail(spins, ctl)) break; }
    asm volatile("" ::: "memory");
    return v;
}
DEV int wait_upto(GAS u32* base, u32 want, int need, int lane, GAS u32* ctl) {
    u32 spins = 0; int r;
    for (;;) {
        const u32 v = __hip_atomic_load(base + 16 * lane, RLX_AGENT);
        const unsigned long long nr = __ballot(v < want);
        r = nr ? __builtin_ctzll(nr) : 64;
        if (r > need) break;
        __builtin_amdgcn_s_sleep(4);
        if (spin_fail(spins, ctl)) { r = 64; break; }
    }
    asm volatile("" ::: "memory");
    return r;
}
DEV u32 now32() { unsigned long long t; asm volatile("s_memtime %0\n s_waitcnt lgkmcnt(0)" : "=s"(t) :: "memory"); return (u32)t; }
DEV void wait_until(u32 t) {
    for (;;) { const int d = (int)(t - now32()); if (d <= 0) break; if (d > 192) __builtin_amdgcn_s_sleep(1); }
}
#define SMF_A(acc, a, w) asm volatile("v_smfmac_f32_16x16x64_bf16 %0, %1, %2, %3" : "+v"(acc) : "v"(a), "a"(w), "v"(sidx))
#define SMF_V(acc, a, w) asm volatile("v_smfmac_f32_16x16x64_bf16 %0, %1, %2, %3" : "+v"(acc) : "v"(a), "v"(w), "v"(sidx))
#define SMF_W(acc, a, i) do { if ((i) < 16) SMF_A(acc, a, wr[i]); else SMF_V(acc, a, wr[i]); } while (0)
#define MF_WA(acc, i, b) do { if ((i) < 32) asm volatile("v_mfma_f32_16x16x32_bf16 %0, %1, %2, %0" : "+v"(acc) : "a"(w4[i]), "v"(b)); \
                              else asm volatile("v_mfma_f32_16x16x32_bf16 %0, %1, %2, %0" : "+v"(acc) : "v"(w4[i]), "v"(b)); } while (0)
#define MF_WB(acc, a, i) do { if ((i) < 32) asm volatile("v_mfma_f32_16x16x32_bf16 %0, %1, %2, %0" : "+v"(acc) : "v"(a), "a"(w4[i])); \
                              else asm volatile("v_mfma_f32_16x16x32_bf16 %0, %1, %2, %0" : "+v"(acc) : "v"(a), "v"(w4[i])); } while (0)
#define LDS_RD128(dst, addr, off) asm volatile("ds_read_b128 %0, %1 offset:%2" : "=v"(dst) : "v"(addr), "i"(off) : "memory")
#define LGKM_WAIT(n, x) asm volatile("s_waitcnt lgkmcnt(%1)" : "+v"(x) : "i"(n) : "memory")
#define LGKM_WAIT2(n, x, y) asm volatile("s_waitcnt lgkmcnt(%2)" : "+v"(x), "+v"(y) : "i"(n) : "memory")
DEV float dmax16(float v) {
    asm volatile("s_nop 1\n v_max_f32_dpp %0, %0, %0 row_ror:8 row_mask:0xf bank_mask:0xf\n s_nop 1\n v_max_f32_dpp %0, %0, %0 row_ror:4 row_mask:0xf bank_mask:0xf\n"
                 "s_nop 1\n v_max_f32_dpp %0, %0, %0 row_ror:2 row_mask:0xf bank_mask:0xf\n s_nop 1\n v_max_f32_dpp %0, %0, %0 row_ror:1 row_mask:0xf bank_mask:0xf" : "+v"(v));
    return v;
}
DEV float max3f(float a, float b, float c) { float r; asm("v_max3_f32 %0, %1, %2, %3" : "=v"(r) : "v"(a), "v"(b), "v"(c)); return r; }
DEV f32x4 mfma16(v4u a, v4u b, f32x4 c) { return __builtin_amdgcn_mfma_f32_16x16x32_bf16(__builtin_bit_cast(bf16x8, a), __builtin_bit_cast(bf16x8, b), c, 0, 0, 0); }

template <int MODE, class Sink>
DEV void load_units(const Params& p, int h, unsigned char* lds, int wave, int lane, Sink sink) {
    constexpr int NU = MODE == 3 ? 10 : 20, NR = (NU + 3) / 4;
    const u32 lds0 = (u32)(size_t)(LAS unsigned char*)lds;
    const int tp = lane >> 3, q4 = (lane >> 1) & 3, hf = lane & 1;
    const rsrc_t r0 = mkrs(MODE <= 1 ? p.Wq : p.Wv, (size_t)DM * HD * 2), r1 = mkrs(MODE <= 1 ? p.Wk : p.Wo, (size_t)DM * HD * 2);
    auto issue = [&](int r) {
        const int u = r * 4 + (wave >> 1), hh = wave & 1;
        if (u >= NU) return;
        bool second; int base, stride, map;
        if (MODE <= 1) { second = u & 1; base = (64 * (u >> 1) * HD + h * DH) * 2; stride = HD * 2; map = MODE; }
        else if (MODE == 2 && u < 10) { second = false; base = (64 * u * HD + h * DH) * 2; stride = HD * 2; map = 1; }
        else { const int uo = MODE == 2 ? u - 10 : u; second = true; base = ((h * DH + 64 * (uo & 1)) * DM + (uo >> 1) * 128) * 2; stride = DM * 2; map = MODE == 2 ? 2 : 1; }
        const int gq = map == 0 ? 8 * (q4 >> 1) + (q4 & 1) : q4;
#pragma unroll
        for (int i = 0; i < 8; ++i) {
            const int gB = i >> 1, i4 = i & 1;
            const int f = 32 * hh + (map == 0 ? 16 * (gB >> 1) + 4 * (gB & 1) + 2 * i4 : map == 1 ? 8 * gB + 4 * i4 : 16 * i4 + 4 * gB);
            const int voff = gq * stride + ((tp ^ (gB & 1)) * 16 + hf * 8) * 2;
            const u32 la = rfl(lds0 + L_STAGE + (r & 1) * 65536 + (wave >> 1) * 16384 + (hh * 8 + i) * 1024);
            const int so = rfl(base + f * stride);
            if (second) dma16(la, voff, r1, so); else dma16(la, voff, r0, so);
        }
    };
    const int gB = lane >> 4;
    const int rd0 = (2 * gB) * 1024 + ((lane & 15) >> 2) * 32 + (lane & 3) * 8;
    issue(0);
#pragma unroll
    for (int r = 0; r < NR; ++r) {
        if (r + 1 < NR) { issue(r + 1); if ((r + 1) * 4 + 3 < NU) VMCNT(8); else VMCNT(0); } else VMCNT(0);
        BAR();
#pragma unroll
        for (int k = 0; k < 4; ++k) {
            int uu, tl;
            if (MODE == 1) { uu = (k >> 1) * 2 + (wave >> 2); tl = 2 * (wave & 3) + (k & 1); } else { uu = k; tl = wave; }
            if (MODE != 1 && r * 4 + k >= NU) continue;
            v2u t[4];
#pragma unroll
            for (int j = 0; j < 4; ++j) {
                s16x4 v = __builtin_amdgcn_ds_read_tr16_b64_v4i16((LAS s16x4*)(lds + L_STAGE + (r & 1) * 65536 + uu * 16384 + ((j >> 1) * 8 + (j & 1)) * 1024 + rd0 + (tl ^ (gB & 1)) * 128));
                t[j] = __builtin_bit_cast(v2u, v);
            }
            sink(r * 4 + k, t);
        }
        BAR();
    }
}

DEV u32 gather_mem(rsrc_t rh, int ad0, int ad1, unsigned char* lds, int wave, int lane, GAS u32* ctl) {
    u32 spins = 0;
    v4u* dst = (v4u*)(lds + L_MEMF + wave * 2048 + lane * 16);
    for (;;) {
        asm volatile("" ::: "memory");
        v4u v0 = ld128(rh, ad0), v1 = ld128(rh, ad1);
        dst[0] = v0; dst[64] = v1;
        if (__builtin_expect(!__any(max(max4(v0), max4(v1)) == SENT), 1)) break;
        if (spin_fail(spins, ctl)) break;
    }
    return spins;
}
DEV void load_mem0(const u16* mem0, unsigned char* lds, int tid) {
    for (int i = tid; i < 640; i += 512) { const int m = i / 80, c8 = i % 80; *(v4u*)(lds + L_MEMF + (c8 >> 1) * 256 + (2 * m + (c8 & 1)) * 16) = *(const v4u*)(mem0 + m * DM + c8 * 8); }
}
#define PROJ2(acc0, acc1) do { v4u a_[5]; \
    _Pragma("unroll") for (int ks = 0; ks < 5; ++ks) LDS_RD128(a_[ks], lbase, L_MEMF + ks * 1024); \
    _Pragma("unroll") for (int ks = 0; ks < 5; ++ks) { LGKM_WAIT(4, a_[ks]); SMF_W(acc0, a_[ks], 2 * ks); SMF_W(acc1, a_[ks], 2 * ks + 1); LDS_RD128(a_[ks], lbase, L_MEMF + (ks + 5) * 1024); } \
    _Pragma("unroll") for (int ks = 5; ks < 10; ++ks) { LGKM_WAIT(9 - ks, a_[ks - 5]); SMF_W(acc0, a_[ks - 5], 2 * ks); SMF_W(acc1, a_[ks - 5], 2 * ks + 1); } } while (0)

template <int CLS>
DEV void a_loop(const Params& p, v8u (&wr)[20], unsigned char* lds, int w, int wave, int lane, u32 par2, GAS u32* ctl) {
    const int h = w, n = lane & 15, gq = lane >> 4;
    const int sidx = (lane & 1) ? 0xEEEE : 0x4444;
    const u32 lbase = (u32)(size_t)(LAS unsigned char*)lds + lane * 16;
    STAMP_DECL;
    const rsrc_t rh = mkrs(p.ws + HAND_OFF, (size_t)NSEG * SLOT_PHYS), rq = mkrs(p.ws + QX_OFF, 2 * QX_BYTES), rp = mkrs(p.ws + PT_OFF, PT_BYTES);
    const int wg = wave < 5 ? wave : 0;
    const int gl = gq * 256 + (n & 1) * 128 + (n >> 1) * 16;
    const int ad0 = amap(MEM_O + wg * 2048 + gl), ad1 = amap(MEM_O + wg * 2048 + 1024 + gl);
    const int qw = L_QF + (wave >> 2) * 1024 + (2 * ((wave >> 1) & 1) + (n >> 3)) * 256 + (2 * gq) * 32 + ((n >> 1) & 1) * 16 + (4 * ((n >> 2) & 1) + 2 * (wave & 1) + (n & 1)) * 2;
    const int kw = L_KMF + (wave >> 1) * 1024 + (16 * (n >> 2) + gq) * 16 + (4 * (wave & 1) + (n & 3)) * 2;
    const int pair = 8 * (n >> 3) + 2 * (n & 3) + ((n >> 2) & 1);
    const int tk0 = 2 * pair, tk1 = 2 * pair + 1;
    const u32 lq = (u32)(size_t)(LAS unsigned char*)lds + gq * 256 + (4 * (wave & 1) + (n >> 2)) * 32 + (n & 1) * 16;
    const int adpx = amap(PX_O + h * 512 + (wave & 1) * 256 + lane * 4);
    const int adpm = amap(PM_O + h * 128 + (4 * (wave & 1) + gq) * 16 + (n & 3) * 4);
    const int g8 = (wave - 2) & 3;
    const int t0 = 8 * g8 + 2 * gq;
    const int adtx = (g8 * 32 + h) * 512 + (2 * gq) * 64 + n * 4;
    const int adtm = 65536 + (g8 * 16 + (h >> 1)) * 256 + (2 * gq) * 32 + (n & 1) * 16 + (h & 1) * 8 + ((n >> 1) & 1) * 4;
    const float SC = 0.08838834764831845f * 1.4426950408889634f;
    const float NEGR = -1.0e9f / 0.08838834764831845f;
    const float pad0 = (n < 8) ? 0.f : -__builtin_inff();
    const bool pmlane = n < 4;
    int ready = 0;
    v4u qx0, qx1;
    f32x4 s1, s2;
    auto tok_prep = [&](int sn) {
        if (sn >= ready) ready = wait_upto(ctl + CW_L, 64u, sn, lane, ctl);
        const int qb = (sn * H + h) * 8192 + lane * 16;
        qx0 = ld128(rq, qb + g8 * 2048);
        qx1 = ld128(rq, qb + g8 * 2048 + 1024);
        v4u kx[8];
#pragma unroll
        for (int i = 0; i < 8; ++i) kx[i] = ld128(rq, (int)QX_BYTES + qb + i * 1024);
        s1 = (f32x4){0, 0, 0, 0}; s2 = s1;
        asm volatile("s_nop 1" : "+v"(s1), "+v"(s2));
        SMF_V(s1, qx0, cat8(kx[0], kx[1]));
        SMF_V(s2, qx0, cat8(kx[4], kx[5]));
        SMF_V(s1, qx1, cat8(kx[2], kx[3]));
        SMF_V(s2, qx1, cat8(kx[6], kx[7]));
        asm volatile("s_nop 7" : "+v"(s1), "+v"(s2));
        s1.x = (tk0 <= t0) ? s1.x + s1.y : NEGR; s1.z = (tk0 <= t0 + 1) ? s1.z + s1.w : NEGR;
        s1.y = (tk1 <= t0) ? s2.x + s2.y : NEGR; s1.w = (tk1 <= t0 + 1) ? s2.z + s2.w : NEGR;
    };
    auto help_prep = [&](int sn) {
        if (sn >= ready) ready = wait_upto(ctl + CW_L, 64u, sn, lane, ctl);
        v4u t[4];
#pragma unroll
        for (int i = 0; i < 4; ++i) t[i] = ld128(rq, (int)QX_BYTES + (sn * H + h) * 8192 + (wave - 6) * 4096 + i * 1024 + lane * 16);
#pragma unroll
        for (int i = 0; i < 4; ++i) *(v4u*)(lds + L_KXF + (wave - 6) * 4096 + i * 1024 + lane * 16) = t[i];
    };
    if (CLS == 2) help_prep(0);
    if (CLS == 1) tok_prep(0);
    u32 tref = 0;
    for (int s = 0; s < NSEG; ++s) {
        const int so = s * SLOT_PHYS + ((par2 >> (s >> 5)) & 1) * 8192;
        f32x4 aq = {0, 0, 0, 0}, ak = {0, 0, 0, 0};
        asm volatile("" : "+v"(aq), "+v"(ak));
        if (CLS != 2 && s > 0 && wave < 5) {
            const int x0 = ad0 + so, x1 = ad1 + so;
            asm volatile("" :: "v"(x0), "v"(x1));
            wait_until(tref + p.dA);
            STAMP(0);
            const u32 sp = gather_mem(rh, x0, x1, lds, wave, lane, ctl);
            STAMP_SET(9, sp + 1);
            STAMP(1);
        }
        VMCNT(0);
        BAR();
        STAMP(2);
        PROJ2(aq, ak);
        v4u kx[8];
        if (CLS == 0) {
#pragma unroll
            for (int i = 0; i < 8; ++i) LDS_RD128(kx[i], lbase, L_KXF + i * 1024);
        } else asm volatile("s_nop 7");
        asm volatile("" : "+v"(aq), "+v"(ak));
        {
            const u32 q2 = pk2(aq.x + aq.y, aq.z + aq.w), k2 = pk2(ak.x + ak.y, ak.z + ak.w);
            *(u16*)(lds + qw) = (u16)q2; *(u16*)(lds + qw + 32) = (u16)(q2 >> 16);
            *(u16*)(lds + kw) = (u16)k2; *(u16*)(lds + kw + 64) = (u16)(k2 >> 16);
        }
        f32x4 s0 = {pad0, 0, pad0, 0};
        const int xpx = adpx + so, xpm = adpm + so;
        if (CLS == 0) { s1 = (f32x4){0, 0, 0, 0}; s2 = s1; asm volatile("" : "+v"(s0), "+v"(s1), "+v"(s2) : "v"(xpx), "v"(xpm)); }
        if (CLS == 1) asm volatile("" : "+v"(s0));
        STAMP(3);
        BAR();
        STAMP(4);
        if (CLS == 0) {
            v4u a0, a1, km[4];
            LDS_RD128(a0, lq, L_QF); LDS_RD128(a1, lq, L_QF + 1024);
#pragma unroll
            for (int i = 0; i < 4; ++i) LDS_RD128(km[i], lbase, L_KMF + i * 1024);
            LGKM_WAIT(5, a0);
            SMF_V(s1, a0, cat8(kx[0], kx[1]));
            SMF_V(s2, a0, cat8(kx[4], kx[5]));
            LGKM_WAIT(4, a1);
            SMF_V(s1, a1, cat8(kx[2], kx[3]));
            SMF_V(s2, a1, cat8(kx[6], kx[7]));
            LGKM_WAIT2(2, km[0], km[1]);
            SMF_V(s0, a0, cat8(km[0], km[1]));
            LGKM_WAIT2(0, km[2], km[3]);
            SMF_V(s0, a1, cat8(km[2], km[3]));
            asm volatile("s_nop 4" : "+v"(s1), "+v"(s2));
            float x1 = s1.x + s1.y, x2 = s2.x + s2.y;
            asm volatile("s_nop 0" : "+v"(s0), "+v"(x1), "+v"(x2));
            const float x0 = s0.x + s0.y;
            const float mx = -SC * dmax16(max3f(x0, x1, x2));
            const float e0 = __builtin_amdgcn_exp2f(__builtin_fmaf(x0, SC, mx)), e1 = __builtin_amdgcn_exp2f(__builtin_fmaf(x1, SC, mx)), e2 = __builtin_amdgcn_exp2f(__builtin_fmaf(x2, SC, mx));
            const float inv = __builtin_amdgcn_rcpf(rowsum16(e0 + e1 + e2));
            const float p0 = e0 * inv;
            st32(pk2s(e1 * inv, e2 * inv), rh, xpx);
            const u32 pm = pk2s(p0, dppf<0x104>(p0));
            if (pmlane) st32(pm, rh, xpm);
            STAMP(5);
            tref = now32() - 450;
        } else if (CLS == 1) {
            tref = now32();
            v4u km[4];
#pragma unroll
            for (int i = 0; i < 4; ++i) km[i] = *(const v4u*)(lds + L_KMF + i * 1024 + lane * 16);
            SMF_V(s0, qx0, cat8(km[0], km[1]));
            SMF_V(s0, qx1, cat8(km[2], km[3]));
            asm volatile("s_nop 7" : "+v"(s0));
#pragma unroll
            for (int rr = 0; rr < 2; ++rr) {
                const float x0 = rr ? s0.z + s0.w : s0.x + s0.y, x1 = rr ? s1.z : s1.x, x2 = rr ? s1.w : s1.y;
                const float mx = -SC * dmax16(max3f(x0, x1, x2));
                const float e0 = __builtin_amdgcn_exp2f(__builtin_fmaf(x0, SC, mx)), e1 = __builtin_amdgcn_exp2f(__builtin_fmaf(x1, SC, mx)), e2 = __builtin_amdgcn_exp2f(__builtin_fmaf(x2, SC, mx));
                const float inv = __builtin_amdgcn_rcpf(rowsum16(e0 + e1 + e2));
                const float p0 = e0 * inv;
                st32(pk2(e1 * inv, e2 * inv), rp, adtx + rr * 64 + s * 81920);
                const u32 pm = pk2(p0, dppf<0x104>(p0));
                if (pmlane) st32(pm, rp, adtm + rr * 32 + s * 81920);
            }
            STAMP(5);
            if (s + 1 < NSEG) tok_prep(s + 1);
            STAMP(6);
        } else {
            if (wave == 7 && s > 0 && lane == 0) __hip_atomic_fetch_add(ctl + CW_S + 16 * (s - 1), 1u, RLX_AGENT);
            if (s + 1 < NSEG) help_prep(s + 1);
            STAMP(5);
        }
        STAMP_FLUSH(s);
    }
    VMCNT(0);
    BAR();
    if (CLS == 2 && wave == 7 && lane == 0) __hip_atomic_fetch_add(ctl + CW_S + 16 * (NSEG - 1), 1u, RLX_AGENT);
    STAMP_END();
}

DEV void r_loop(const Params& p, v8u (&wr)[20], unsigned char* lds, int w, int wave, int lane, u32 par2, GAS u32* ctl) {
    const int c8 = w - 32, n = lane & 15, gq = lane >> 4;
    const int sidx = (lane & 1) ? 0xEEEE : 0x4444;
    const u32 lbase = (u32)(size_t)(LAS unsigned char*)lds + lane * 16;
    STAMP_DECL;
    const rsrc_t rh = mkrs(p.ws + HAND_OFF, (size_t)NSEG * SLOT_PHYS), rz = mkrs(p.ws + ZM_OFF, ZM_BYTES), rx = mkrs(p.ws + ZX_OFF, ZX_BYTES);
    const int wg = wave < 5 ? wave : 0;
    const int gl = gq * 256 + (n & 1) * 128 + (n >> 1) * 16;
    const int ad0 = amap(MEM_O + wg * 2048 + gl), ad1 = amap(MEM_O + wg * 2048 + 1024 + gl);
    const int ztw = L_ZT + wave * 512 + (n >> 3) * 128 + (n & 7) * 16 + gq * 4;
    const int ztr = L_ZT + wave * 512 + gq * 128 + (n & 7) * 16;
    const int adzm = c8 * 4096 + wave * 512 + gq * 128 + (n & 7) * 16;
    const int pxl = (4 * wave + (gq >> 1)) * 512 + (n >> 1) * 64 + (gq & 1) * 32 + (n & 1) * 16;
    const int adx0 = amap(PX_O + pxl), adx1 = amap(PX_O + pxl + 1024);
    const int adm = amap(PM_O + (4 * wave + gq) * 128 + (n >> 1) * 16);
    const int adzx = c8 * 16384 + wave * 2048 + lane * 16;
    const int redw = L_RED + ((2 * gq) * 4 + ((n & 7) >> 1)) * 144 + (2 * wave + (n >> 3)) * 8 + (n & 1) * 4;
    const int redr = L_RED + (lane & 31) * 144 + (lane >> 5) * 64;
    const int adpub = amap(MEM_O + c8 * 128 + (lane & 31) * 4);
    int ready = 0;
    u32 tref = now32() - 100000;
    for (int s = 0; s < NSEG; ++s) {
        const int so = s * SLOT_PHYS + ((par2 >> (s >> 5)) & 1) * 8192;
        f32x4 z0 = {0, 0, 0, 0}, z1 = {0, 0, 0, 0};
        asm volatile("" : "+v"(z0), "+v"(z1));
        if (s > 0 && wave < 5) {
            const int x0 = ad0 + so, x1 = ad1 + so;
            asm volatile("" :: "v"(x0), "v"(x1));
            wait_until(tref + p.dB);
            STAMP(0);
            gather_mem(rh, x0, x1, lds, wave, lane, ctl);
            STAMP(1);
        }
        BAR();
        tref = now32();
        STAMP(2);
        if (s >= ready) ready = wait_upto(ctl + CW_L, 64u, s, lane, ctl);
        const v4u zx0 = ld128(rx, adzx + s * (80 * 16384));
        const v4u zx1 = ld128(rx, adzx + 1024 + s * (80 * 16384));
        PROJ2(z0, z1);
        asm volatile("s_nop 7" : "+v"(z0), "+v"(z1));
        *(u32*)(lds + ztw) = pk2(z0.x + z0.y, z0.z + z0.w);
        *(u32*)(lds + ztw + 256) = pk2(z1.x + z1.y, z1.z + z1.w);
        asm volatile("s_waitcnt lgkmcnt(0)" ::: "memory");
        const v4u zm = *(const v4u*)(lds + ztr);
        if (n < 8) st128(zm, rz, adzm + s * (80 * 4096));
        STAMP(3);
        if (s + 1 == NSEG) break;
        v4u px0, px1, pm;
        f32x4 c1 = {0, 0, 0, 0}, c2 = c1, c3 = c1;
        {
            const int xx0 = adx0 + so, xx1 = adx1 + so, xm = adm + so;
            asm volatile("" : "+v"(c1), "+v"(c2), "+v"(c3) : "v"(xx0), "v"(xx1), "v"(xm));
            wait_until(tref + p.dR);
            STAMP(5);
            u32 spins = 0;
            for (;;) {
                asm volatile("" ::: "memory");
                px0 = ld128(rh, xx0); px1 = ld128(rh, xx1); pm = ld128(rh, xm);
                if (__builtin_expect(!__any(max(max(max4(px0), max4(px1)), max4(pm)) == SENT), 1)) break;
                if (spin_fail(spins, ctl)) break;
            }
            STAMP_SET(9, spins + 1);
        }
        STAMP(6);
        SMF_V(c1, px0, cat8(zx0, zx1));
        SMF_V(c2, px1, cat8(zx0, zx1));
        asm volatile("v_mfma_f32_16x16x32_bf16 %0, %3, %4, %0\n s_nop 7" : "+v"(c3), "+v"(c1), "+v"(c2) : "v"(pm), "v"(zm));
        float t0, t1;
        if (n < 8) { t0 = c1.x + c1.y + c3.x; t1 = c1.z + c1.w + c3.z; } else { t0 = c2.x + c2.y; t1 = c2.z + c2.w; }
        *(float*)(lds + redw) = t0;
        *(float*)(lds + redw + 576) = t1;
        const int sn = s + 1;
        const int xpub = adpub + sn * SLOT_PHYS + ((par2 >> (sn >> 5)) & 1) * 8192;
        asm volatile("" :: "v"(xpub));
        BAR();
        STAMP(7);
        if (wave == 7) {
            const unsigned char* rb = lds + redr;
            f32x4 r0 = *(const f32x4*)(rb), r1 = *(const f32x4*)(rb + 16), r2 = *(const f32x4*)(rb + 32), r3 = *(const f32x4*)(rb + 48);
            r0 = (r0 + r1) + (r2 + r3);
            const float o0 = r0.x + r0.z, o1 = r0.y + r0.w;
            auto sw0 = __builtin_amdgcn_permlane32_swap(__float_as_uint(o0), __float_as_uint(o0), false, false);
            auto sw1 = __builtin_amdgcn_permlane32_swap(__float_as_uint(o1), __float_as_uint(o1), false, false);
            const u32 pkd = pk2s(__uint_as_float(sw0[0]) + __uint_as_float(sw0[1]), __uint_as_float(sw1[0]) + __uint_as_float(sw1[1]));
            if (lane < 32) st32(pkd, rh, xpub);
        }
        tref = now32();
        if (wave == 6 && lane == 0) __hip_atomic_fetch_add(ctl + CW_S + 16 * s, 1u, RLX_AGENT);
        STAMP(8);
        STAMP_FLUSH(s);
    }
    VMCNT(0);
    BAR();
    if (wave == 6 && lane == 0) __hip_atomic_fetch_add(ctl + CW_S + 16 * (NSEG - 1), 1u, RLX_AGENT);
    STAMP_END();
}

DEV void vo_task(const Params& p, unsigned char* lds, int v, int wave, int lane, GAS u32* ctl) {
    const int h = 8 * (v & 3) + (v >> 2) / 3, part = (v >> 2) % 3;
    const int g0 = 7 * part, g1 = min(20, g0 + 7);
    v4u w4[20];
    load_units<3>(p, h, lds, wave, lane, [&](int slot, v2u (&t)[4]) {
        w4[2 * slot] = (v4u){t[0].x, t[0].y, t[1].x, t[1].y}; w4[2 * slot + 1] = (v4u){t[2].x, t[2].y, t[3].x, t[3].y}; });
    const int i = lane & 15, gq = lane >> 4;
    const rsrc_t rv = mkrs(p.Wv, (size_t)DM * HD * 2), ro = mkrs(p.ws + VO_OFF, VO_BYTES);
    const int dl = 16 * (i >> 3) + 8 * ((i >> 1) & 1) + 4 * ((i >> 2) & 1) + (i & 1);
    const int ab = (dl * HD + h * DH + 8 * gq) * 2;
    const int ob = (2 * wave + (i >> 3)) * 327680 + (h >> 1) * 20480 + (16 * gq + 8 * (h & 1) + (i & 7)) * 16;
    v4u a[8], an[8];
#pragma unroll
    for (int k = 0; k < 8; ++k) an[k] = __builtin_amdgcn_raw_buffer_load_b128(rv, ab + (k >> 2) * (2 * HD * 2) + (k & 3) * 64 + g0 * (32 * HD * 2), 0, 0);
    for (int g = g0; g < g1; ++g) {
#pragma unroll
        for (int k = 0; k < 8; ++k) a[k] = an[k];
        const int gn = g + 1 < g1 ? g + 1 : g;
#pragma unroll
        for (int k = 0; k < 8; ++k) an[k] = __builtin_amdgcn_raw_buffer_load_b128(rv, ab + (k >> 2) * (2 * HD * 2) + (k & 3) * 64 + gn * (32 * HD * 2), 0, 0);
#pragma unroll
        for (int cg = 0; cg < 5; ++cg) {
            f32x4 d0 = {0, 0, 0, 0}, d1 = d0;
            asm volatile("s_nop 1" : "+v"(d0), "+v"(d1), "+v"(a[0]), "+v"(a[1]), "+v"(a[2]), "+v"(a[3]), "+v"(a[4]), "+v"(a[5]), "+v"(a[6]), "+v"(a[7]));
#pragma unroll
            for (int ss = 0; ss < 4; ++ss) { MF_WB(d0, a[ss], cg * 4 + ss); MF_WB(d1, a[4 + ss], cg * 4 + ss); }
            asm volatile("s_nop 7" : "+v"(d0), "+v"(d1));
            st128((v4u){pk2(d0.x, d0.y), pk2(d0.z, d0.w), pk2(d1.x, d1.y), pk2(d1.z, d1.w)}, ro, ob + cg * (16 * 327680) + g * 1024);
        }
    }
    VMCNT(0);
    BAR();
    if (wave == 0 && lane == 0) __hip_atomic_fetch_add(ctl + CW_VO, 1u, RLX_AGENT);
}

constexpr int L_XT = L_STAGE, L_VI = L_STAGE + 81920;
template <int TYPE>
DEV void l_worker(const Params& p, unsigned char* lds, int h, int par, int wave, int lane, GAS u32* ctl) {
    v4u w4[40];
    if (TYPE == 0) load_units<1>(p, h, lds, wave, lane, [&](int slot, v2u (&t)[4]) {
        const int ks = slot >> 1, tau = slot & 1;
        w4[(2 * ks) * 2 + tau] = (v4u){t[0].x, t[0].y, t[1].x, t[1].y}; w4[(2 * ks + 1) * 2 + tau] = (v4u){t[2].x, t[2].y, t[3].x, t[3].y}; });
    else load_units<2>(p, h, lds, wave, lane, [&](int slot, v2u (&t)[4]) {
        w4[2 * slot] = (v4u){t[0].x, t[0].y, t[1].x, t[1].y}; w4[2 * slot + 1] = (v4u){t[2].x, t[2].y, t[3].x, t[3].y}; });
    EVT(1);
    const u32 lds0 = (u32)(size_t)(LAS unsigned char*)lds;
    const int i = lane & 15, gq = lane >> 4;
    const rsrc_t rxin = mkrs(p.x, (size_t)S * DM * 2);
    const rsrc_t ro = TYPE == 0 ? mkrs(p.ws + QX_OFF, 2 * QX_BYTES) : mkrs(p.ws + ZX_OFF, ZX_BYTES);
    auto issue_x = [&](int s, int buf) {
#pragma unroll
        for (int k = 0; k < 5; ++k) {
            const int pc = wave * 5 + k, k64 = pc >> 2, r0 = (pc & 3) * 8;
            const int row = r0 + (lane >> 3);
            const int voff = row * (DM * 2) + (((lane & 7) ^ ((row >> 1) & 7)) * 16);
            dma16(rfl(lds0 + L_XT + buf * 40960 + pc * 1024), voff, rxin, rfl(s * (32 * DM * 2) + k64 * 128));
        }
    };
    const u32 xb0 = lds0 + L_XT + i * 128 + ((gq ^ (i >> 1)) * 16), xb1 = lds0 + L_XT + i * 128 + (((4 + gq) ^ (i >> 1)) * 16);
    int ost0, ost1;
    if (TYPE == 0) {
        const int hp = wave & 3;
        if (wave < 4) {
            ost0 = h * 8192 + (i >> 3) * 2048 + (hp >> 1) * 1024 + (16 * (2 * (hp & 1) + (gq >> 1)) + 2 * (i & 7) + (gq & 1)) * 16; ost1 = ost0 + 4096;
        } else {
            const int ip = i >> 1;
            ost0 = (int)QX_BYTES + h * 8192 + (i & 1) * 4096 + hp * 1024 + (16 * gq + 4 * (ip & 1) + ((ip >> 1) & 3)) * 16; ost1 = ost0 + 128;
        }
    } else {
        ost0 = (2 * wave + (i >> 3)) * 16384 + (h >> 2) * 2048 + (h & 1) * 1024 + (16 * gq + 8 * ((h >> 1) & 1) + (i & 7)) * 16; ost1 = 0;
    }
    const int viw = L_VI + i * 256 + (((4 * (wave >> 1) + gq) ^ i) * 16) + (wave & 1) * 8;
    const int vtok = 8 * (i >> 2) + (i & 3);
    constexpr int NST = TYPE == 0 ? 2 : 5;
    issue_x(par, 0);
    for (int it = 0; it < 32; ++it) {
        const int s = 2 * it + par, buf = it & 1;
        if (it < 2) VMCNT(0); else VMCNT(NST);
        BAR();
        if (it + 1 < 32) issue_x(s + 2, buf ^ 1);
        if (wave == 0 && lane == 0) {
            if (it == 1) __hip_atomic_fetch_add(ctl + CW_L + 16 * (s - 2), 1u, RLX_AGENT);
            else if (it >= 3) __hip_atomic_fetch_add(ctl + CW_L + 16 * (s - 4), 1u, RLX_AGENT);
        }
        const u32 x0 = xb0 + buf * 40960, x1 = xb1 + buf * 40960;
        f32x4 d00 = {0, 0, 0, 0}, d01 = d00, d10 = d00, d11 = d00;
        asm volatile("" : "+v"(d00), "+v"(d01), "+v"(d10), "+v"(d11));
        v4u b[8];
#define XRD(slot, ss) do { LDS_RD128(b[2 * (slot)], ((ss) & 1) ? x1 : x0, ((ss) >> 1) * 4096); LDS_RD128(b[2 * (slot) + 1], ((ss) & 1) ? x1 : x0, ((ss) >> 1) * 4096 + 2048); } while (0)
#pragma unroll
        for (int ss = 0; ss < 4; ++ss) XRD(ss, ss);
#pragma unroll
        for (int ss = 0; ss < 20; ++ss) {
            const int sl = ss & 3;
            if (ss < 17) LGKM_WAIT2(6, b[2 * sl], b[2 * sl + 1]); else LGKM_WAIT2(2 * (19 - ss), b[2 * sl], b[2 * sl + 1]);
            if (TYPE == 0) { MF_WA(d00, 2 * ss, b[2 * sl]); MF_WA(d01, 2 * ss, b[2 * sl + 1]); MF_WA(d10, 2 * ss + 1, b[2 * sl]); MF_WA(d11, 2 * ss + 1, b[2 * sl + 1]); }
            else { MF_WA(d00, ss, b[2 * sl]); MF_WA(d01, ss, b[2 * sl + 1]); }
            if (ss + 4 < 20) XRD(sl, ss + 4);
        }
#undef XRD
        asm volatile("s_nop 7" : "+v"(d00), "+v"(d01), "+v"(d10), "+v"(d11));
        if (TYPE == 0) {
            u32 a0 = pk2(d00.x, d00.y), a1 = pk2(d00.z, d00.w), a2 = pk2(d10.x, d10.y), a3 = pk2(d10.z, d10.w);
            u32 c0 = pk2(d01.x, d01.y), c1 = pk2(d01.z, d01.w), c2 = pk2(d11.x, d11.y), c3 = pk2(d11.z, d11.w);
            if (wave < 4) {
                auto sa = __builtin_amdgcn_permlane16_swap(a0, a1, false, false), sb = __builtin_amdgcn_permlane16_swap(a2, a3, false, false);
                auto sc = __builtin_amdgcn_permlane16_swap(c0, c1, false, false), sd = __builtin_amdgcn_permlane16_swap(c2, c3, false, false);
                st128((v4u){sa[0], sb[0], sa[1], sb[1]}, ro, ost0 + s * (H * 8192));
                st128((v4u){sc[0], sd[0], sc[1], sd[1]}, ro, ost1 + s * (H * 8192));
            } else {
                st128((v4u){a0, a1, a2, a3}, ro, ost0 + s * (H * 8192));
                st128((v4u){c0, c1, c2, c3}, ro, ost1 + s * (H * 8192));
            }
        } else {
            *(v2u*)(lds + viw) = (v2u){pk2(d00.x, d00.y), pk2(d00.z, d00.w)};
            *(v2u*)(lds + viw + 4096) = (v2u){pk2(d01.x, d01.y), pk2(d01.z, d01.w)};
            BAR();
            v4u a[8];
#pragma unroll
            for (int k = 0; k < 8; ++k) { const int tok = vtok + 4 * (k >> 2); a[k] = *(const v4u*)(lds + L_VI + tok * 256 + (((4 * (k & 3) + gq) ^ (tok & 15)) * 16)); }
#pragma unroll
            for (int cg = 0; cg < 5; ++cg) {
                f32x4 d0 = {0, 0, 0, 0}, d1 = d0;
                asm volatile("s_nop 1" : "+v"(d0), "+v"(d1));
#pragma unroll
                for (int ss = 0; ss < 4; ++ss) { MF_WB(d0, a[ss], 20 + cg * 4 + ss); MF_WB(d1, a[4 + ss], 20 + cg * 4 + ss); }
                asm volatile("s_nop 7" : "+v"(d0), "+v"(d1));
                st128((v4u){pk2(d0.x, d0.y), pk2(d0.z, d0.w), pk2(d1.x, d1.y), pk2(d1.z, d1.w)}, ro, ost0 + cg * (16 * 16384) + s * (80 * 16384));
            }
        }
        if (it == 0) EVT(2);
    }
    VMCNT(0);
    BAR();
    if (wave == 0 && lane == 0) {
        __hip_atomic_fetch_add(ctl + CW_L + 16 * (60 + par), 1u, RLX_AGENT);
        __hip_atomic_fetch_add(ctl + CW_L + 16 * (62 + par), 1u, RLX_AGENT);
    }
}

constexpr int SS_K = 33 * 144, SS_U = 4288  , SS_CH = 2 * SS_K + 2 * SS_U + 32;
static_assert(SS_CH % 16 == 0 && 8 * SS_CH <= L_TOTAL, "ssm lds");
DEV void ssm_task(const Params& p, unsigned char* lds, int task, int wave, int lane, int tid, GAS u32* ctl) {
    const int d = task * 8 + wave;
    unsigned char* ch = lds + wave * SS_CH;
    unsigned char* tmpb = ch + 2 * SS_K;
    const int i = lane & 15, gq = lane >> 4;
    {
        const float dt = rbf(__expf(bf2f(p.logdt[d])));
#pragma unroll
        for (int k = 0; k < 2; ++k) {
            const int nn = lane + 64 * k, o = d * NS + nn;
            const float br = dt * bf2f(p.Bre[o]), bi = dt * bf2f(p.Bim[o]), cr = bf2f(p.Cre[o]), ci = bf2f(p.Cim[o]);
            *(f32x4*)(tmpb + nn * 16) = (f32x4){dt * bf2f(p.Are[o]) * 1.4426950408889634f, dt * bf2f(p.Aim[o]) * 0.15915494309189535f, cr * br - ci * bi, cr * bi + ci * br};
        }
        asm volatile("s_waitcnt lgkmcnt(0)" ::: "memory");
        const float lu = 16.f * i;
        const float lv0 = (float)i;
        const float lv1 = i < 8 ? 256.f + 2.f * i : 512.f + 256.f * ((i - 8) >> 2) + 4.f * (i & 3);
        const float lv2 = i < 8 ? 1024.f + 256.f * (i >> 1) + 8.f * (i & 1) : 2048.f;
        f32x4 k0 = {0, 0, 0, 0}, k1 = k0, k2 = k0;
#pragma unroll 2
        for (int it = 0; it < 32; ++it) {
            const f32x4 q = *(const f32x4*)(tmpb + (4 * it + gq) * 16);
            auto cexp = [&](float l, float& re, float& im) {
                const float e = __builtin_amdgcn_exp2f(q.x * l), th = __builtin_amdgcn_fractf(q.y * l);
                re = e * __builtin_amdgcn_cosf(th); im = e * __builtin_amdgcn_sinf(th);
            };
            float er, ei, vr, vi;
            cexp(lu, er, ei);
            const float ur = q.z * er - q.w * ei, nui = -(q.z * ei + q.w * er);
            cexp(lv0, vr, vi); k0 = __builtin_amdgcn_mfma_f32_16x16x4f32(ur, vr, k0, 0, 0, 0); k0 = __builtin_amdgcn_mfma_f32_16x16x4f32(nui, vi, k0, 0, 0, 0);
            cexp(lv1, vr, vi); k1 = __builtin_amdgcn_mfma_f32_16x16x4f32(ur, vr, k1, 0, 0, 0); k1 = __builtin_amdgcn_mfma_f32_16x16x4f32(nui, vi, k1, 0, 0, 0);
            cexp(lv2, vr, vi); k2 = __builtin_amdgcn_mfma_f32_16x16x4f32(ur, vr, k2, 0, 0, 0); k2 = __builtin_amdgcn_mfma_f32_16x16x4f32(nui, vi, k2, 0, 0, 0);
        }
        float* tab = (float*)(tmpb + 2048);
        *(f32x4*)(tab + i * 20 + 4 * gq) = k0; *(f32x4*)(tab + (16 + i) * 20 + 4 * gq) = k1; *(f32x4*)(tab + (32 + i) * 20 + 4 * gq) = k2;
        asm volatile("s_waitcnt lgkmcnt(0)" ::: "memory");
        if (lane < 36) { *(u32*)(ch + lane * 4) = 0; *(u32*)(ch + SS_K + lane * 4) = 0; }
#pragma unroll 2
        for (int it = 0; it < 16; ++it) {
            const int l0 = it * 128 + lane * 2;
            float kv[2];
#pragma unroll
            for (int e = 0; e < 2; ++e) {
                const int lr = (int)rbf((float)(l0 + e));
                const int a = (lr >> 4) & 15, j = lr & 15, hi = lr >> 8;
                int col;
                if (hi == 0) col = j; else if (hi == 1) col = 16 + (j >> 1); else if (hi < 4) col = 24 + 4 * (hi - 2) + (j >> 2); else if (hi < 8) col = 32 + 2 * (hi - 4) + (j >> 3); else col = 40;
                kv[e] = tab[col * 20 + a];
            }
            const float h0 = rbf(kv[0]), h1 = rbf(kv[1]);
            const int o = ((l0 >> 6) + 1) * 144 + ((l0 & 56) + 6 - (l0 & 6)) * 2;
            *(u32*)(ch + o) = pk2(h1, h0);
            *(u32*)(ch + SS_K + o) = pk2(kv[1] - h1, kv[0] - h0);
        }
    }
    BAR();
    {
        const rsrc_t rxin = mkrs(p.x, (size_t)S * DM * 2);
        if (lane < 24) { *(u32*)(ch + 2 * SS_K + lane * 4) = 0; *(u32*)(ch + 2 * SS_K + SS_U + lane * 4) = 0; }
        BAR();
#pragma unroll
        for (int k = 0; k < 4; ++k) {
            const int l = tid + 512 * k;
            const v4u xv = __builtin_amdgcn_raw_buffer_load_b128(rxin, (l * DM + task * 8) * 2, 0, 0);
#pragma unroll
            for (int c = 0; c < 8; ++c) {
                const u16 e = (u16)((c & 1) ? (xv[c >> 1] >> 16) : xv[c >> 1]);
                unsigned char* ub = lds + c * SS_CH + 2 * SS_K;
                *(u16*)(ub + (l + 48) * 2) = e;
                *(u16*)(ub + SS_U + (l + 47) * 2) = e;
            }
        }
    }
    BAR();
    f32x4 acc[2][4];
#pragma unroll
    for (int I = 0; I < 2; ++I)
#pragma unroll
        for (int rt = 0; rt < 4; ++rt) acc[I][rt] = (f32x4){0, 0, 0, 0};
    {
        const int st0 = i - 8 * gq - 7 + 48;
        const unsigned char* ua = ch + 2 * SS_K + ((st0 & 1) ? SS_U + (st0 - 1) * 2 : st0 * 2);
        auto afrag = [&](int o) { const u32* q = (const u32*)(ua + o * 32); return (v4u){q[0], q[1], q[2], q[3]}; };
        v4u am2 = {0, 0, 0, 0};
        v4u am1 = afrag(-1);
        const int kb = gq * 16;
        auto step = [&](int t, int I, const v4u& a0, const v4u& a1, const v4u& a2, const v4u& a3) {
            const int blk = max(16 * I + i - t + 1, 0);
            const unsigned char* kp = ch + blk * 144 + kb;
#pragma unroll
            for (int hl = 0; hl < 2; ++hl) {
                const v4u b0 = *(const v4u*)(kp + hl * SS_K), b1 = *(const v4u*)(kp + hl * SS_K + 64);
                acc[I][0] = mfma16(a0, b0, acc[I][0]); acc[I][1] = mfma16(a1, b0, acc[I][1]); acc[I][2] = mfma16(a2, b0, acc[I][2]); acc[I][3] = mfma16(a3, b0, acc[I][3]);
                acc[I][0] = mfma16(am2, b1, acc[I][0]); acc[I][1] = mfma16(am1, b1, acc[I][1]); acc[I][2] = mfma16(a0, b1, acc[I][2]); acc[I][3] = mfma16(a1, b1, acc[I][3]);
            }
        };
#pragma unroll 1
        for (int t = 0; t < 16; ++t) {
            const v4u a0 = afrag(4 * t), a1 = afrag(4 * t + 1), a2 = afrag(4 * t + 2), a3 = afrag(4 * t + 3);
            step(t, 0, a0, a1, a2, a3); step(t, 1, a0, a1, a2, a3);
            am2 = a2; am1 = a3;
        }
#pragma unroll 1
        for (int t = 16; t < 32; ++t) {
            const v4u a0 = afrag(4 * t), a1 = afrag(4 * t + 1), a2 = afrag(4 * t + 2), a3 = afrag(4 * t + 3);
            step(t, 1, a0, a1, a2, a3);
            am2 = a2; am1 = a3;
        }
    }
    {
        const float dsk = bf2f(p.dskip[d]);
        asm volatile("s_waitcnt lgkmcnt(0)" ::: "memory");
#pragma unroll
        for (int I = 0; I < 2; ++I)
#pragma unroll
            for (int rt = 0; rt < 4; ++rt) {
                const int l = 64 * (16 * I + i) + 16 * rt + 4 * gq;
                const v2u uu = *(const v2u*)(ch + 2 * SS_K + (l + 48) * 2);
                const f32x4 y = acc[I][rt];
                const float b0 = rbf(y.x) + rbf(bf2f(uu.x & 0xffffu) * dsk), b1 = rbf(y.y) + rbf(bfhi(uu.x) * dsk), b2 = rbf(y.z) + rbf(bf2f(uu.y & 0xffffu) * dsk), b3 = rbf(y.w) + rbf(bfhi(uu.y) * dsk);
                *(v2u*)(ch + l * 2) = (v2u){pk2(b0, b1), pk2(b2, b3)};
            }
    }
    BAR();
    {
        const rsrc_t rb = mkrs(p.ws + BS_OFF, BS_BYTES);
#pragma unroll
        for (int k = 0; k < 4; ++k) {
            const int l = tid + 512 * k;
            u32 e[8];
#pragma unroll
            for (int c = 0; c < 8; ++c) e[c] = *(const u16*)(lds + c * SS_CH + l * 2);
            st128((v4u){e[0] | (e[1] << 16), e[2] | (e[3] << 16), e[4] | (e[5] << 16), e[6] | (e[7] << 16)}, rb, (l * DM + task * 8) * 2);
        }
    }
    VMCNT(0);
    BAR();
    if (tid == 0) __hip_atomic_fetch_add(ctl + CW_SSM, 1u, RLX_AGENT);
}

constexpr int L_OP = 0, L_OA = 81920, L_OG = L_OA + 32 * 272;
DEV void out_task(const Params& p, unsigned char* lds, int s, int cgp, int wave, int lane, int tid, GAS u32* ctl) {
    const int n = lane & 15, gq = lane >> 4;
    const int sidx = (lane & 1) ? 0xEEEE : 0x4444;
    const rsrc_t rx = mkrs(p.ws + ZX_OFF, ZX_BYTES), rz = mkrs(p.ws + ZM_OFF, ZM_BYTES), rp = mkrs(p.ws + PT_OFF, PT_BYTES);
    const int c8 = 2 * (8 * cgp + wave) + (n >> 3);
    {
        const rsrc_t rxin = mkrs(p.x, (size_t)S * DM * 2), rg = mkrs(p.gw, DM * 2);
        const int tok = wave * 4 + gq;
        float sum = 0.f;
#pragma unroll
        for (int k = 0; k < 5; ++k) {
            const v4u xv = __builtin_amdgcn_raw_buffer_load_b128(rxin, ((s * 32 + tok) * DM + k * 128 + n * 8) * 2, 0, 0);
            const v4u gv = __builtin_amdgcn_raw_buffer_load_b128(rg, (k * 128 + n * 8) * 2, 0, 0);
#pragma unroll
            for (int e = 0; e < 4; ++e) sum += rbf(bf2f(xv[e] & 0xffffu) * bf2f(gv[e] & 0xffffu)) + rbf(bfhi(xv[e]) * bfhi(gv[e]));
        }
        sum = rbf(rowsum16(sum));
        const float g = rbf(1.f / (1.f + __expf(-sum)));
        if (n == 0) *(float*)(lds + L_OG + tok * 4) = g;
    }
    wait_ge(ctl + CW_S + 16 * s, 112u, ctl);
#pragma unroll
    for (int k = 0; k < 10; ++k) {
        const int f = wave * 10 + k, g8 = f / 20, ks = f % 20;
        int o;
        if (ks < 16) o = (g8 * 32 + 2 * ks + (gq >> 1)) * 512 + (n >> 1) * 64 + (gq & 1) * 32 + (n & 1) * 16;
        else o = 65536 + (g8 * 16 + 4 * (ks - 16) + gq) * 256 + n * 16;
        *(v4u*)(lds + L_OP + f * 1024 + lane * 16) = ld128(rp, s * 81920 + o);
    }
    BAR();
    f32x4 acc[4];
#pragma unroll
    for (int g = 0; g < 4; ++g) acc[g] = (f32x4){0, 0, 0, 0};
    asm volatile("s_nop 1" : "+v"(acc[0]), "+v"(acc[1]), "+v"(acc[2]), "+v"(acc[3]));
    const int zb = (s * 80 + c8) * 16384 + (16 * gq + (n & 7)) * 16;
    const int mb = (s * 80 + c8) * 4096 + gq * 128 + (n & 7) * 16;
#pragma unroll
    for (int kq = 0; kq < 5; ++kq) {
        v4u b[8];
#pragma unroll
        for (int k = 0; k < 4; ++k) {
            const int ks = kq * 4 + k;
            if (kq < 4) { b[2 * k] = ld128(rx, zb + (ks >> 1) * 2048 + (ks & 1) * 128); b[2 * k + 1] = ld128(rx, zb + (ks >> 1) * 2048 + (ks & 1) * 128 + 1024); }
            else { b[2 * k] = ld128(rz, mb + k * 1024); b[2 * k + 1] = ld128(rz, mb + k * 1024 + 512); }
        }
#pragma unroll
        for (int k = 0; k < 4; ++k)
#pragma unroll
            for (int g = 0; g < 4; ++g) {
                const v4u a = *(const v4u*)(lds + L_OP + (g * 20 + kq * 4 + k) * 1024 + lane * 16);
                SMF_V(acc[g], a, cat8(b[2 * k], b[2 * k + 1]));
            }
    }
    asm volatile("s_nop 7" : "+v"(acc[0]), "+v"(acc[1]), "+v"(acc[2]), "+v"(acc[3]));
#pragma unroll
    for (int g = 0; g < 4; ++g) {
        const u32 v = pk2(acc[g].x + acc[g].y, acc[g].z + acc[g].w);
        unsigned char* o = lds + L_OA + (8 * g + 2 * gq) * 272 + (wave * 16 + n) * 2;
        *(u16*)o = (u16)v; *(u16*)(o + 272) = (u16)(v >> 16);
    }
    wait_ge(ctl + CW_SSM, 80u, ctl);
    BAR();
    {
        const int tok = tid >> 4, cc = (tid & 15) * 8;
        const rsrc_t rb = mkrs(p.ws + BS_OFF, BS_BYTES);
        const int go = ((s * 32 + tok) * DM + cgp * 128 + cc) * 2;
        const v4u bv = ld128(rb, go);
        const v4u av = *(const v4u*)(lds + L_OA + tok * 272 + cc * 2);
        const float g = *(const float*)(lds + L_OG + tok * 4), og = rbf(1.f - g);
        v4u o;
#pragma unroll
        for (int e = 0; e < 4; ++e)
            o[e] = pk2(rbf(g * bf2f(av[e] & 0xffffu)) + rbf(og * bf2f(bv[e] & 0xffffu)), rbf(g * bfhi(av[e])) + rbf(og * bfhi(bv[e])));
        *(v4u*)((unsigned char*)p.out + go) = o;
    }
    BAR();
}

__global__ void __launch_bounds__(512, 2) gm_kernel(Params p) {
    extern __shared__ __attribute__((aligned(16))) unsigned char lds[];
    const int b = blockIdx.x;
    const int wave = rfl(threadIdx.x >> 6);
    GAS u32* ctl = (GAS u32*)(p.ws + CTRL_OFF);
    EVT(0);
#define lane lane_id()
#define tid (wave * 64 + lane_id())
    if ((b & 7) >= 4) {
        const int u = (b >> 3) * 4 + (b & 7) - 4, j = u >> 2;
        const int h = 8 * (u & 3) + (j >> 2);
        if ((j >> 1) & 1) l_worker<1>(p, lds, h, j & 1, wave, lane, ctl); else l_worker<0>(p, lds, h, j & 1, wave, lane, ctl);
        EVT(3);
        if (u < 80) ssm_task(p, lds, u, wave, lane, tid, ctl);
        EVT(4);
        for (int t = (u + 48) & 127; t < NSEG * 5; t += 128) out_task(p, lds, t / 5, t % 5, wave, lane, tid, ctl);
        EVT(5);
        return;
    }
    const int w = (b >> 3) * 4 + (b & 7);
    if (w >= 112) {
        const rsrc_t rh = mkrs(p.ws + HAND_OFF, (size_t)NSEG * SLOT_PHYS);
        if (w == 112 && wave == 0) {
            u32 res = 4;
            for (int pg = 0; pg < 2; ++pg) {
                u32 sum[2] = {0, 0};
                for (int it = 0; it < 4; ++it) {
#pragma unroll
                    for (int par = 0; par < 2; ++par) {
                        const int ad = (pg * 32 + it) * SLOT_PHYS + par * 8192 + amap(PROBE_O + lane * 4);
                        const u32 c0 = now32();
                        st32(0u, rh, ad);
                        VMCNT(0);
                        const u32 c1 = now32();
                        if (it > 0) sum[par] += min(c1 - c0, 2000u);
                    }
                }
                if (sum[1] < sum[0]) res |= 1u << pg;
            }
            if (lane == 0) __hip_atomic_store(ctl + CW_PAR, res, RLX_AGENT);
        }
        const v4u sv = {SENT, SENT, SENT, SENT};
        for (int sl = (w - 112) * 4; sl < (w - 112) * 4 + 4; ++sl)
            for (int i = tid; i < PROBE_O / 16; i += 512) { const int o = sl * SLOT_PHYS + amap(i * 16); st128(sv, rh, o); st128(sv, rh, o + 8192); }
        VMCNT(0);
        BAR();
        if (tid == 0) __hip_atomic_fetch_add(ctl + CW_READY, 1u, RLX_AGENT);
        vo_task(p, lds, w - 32, wave, lane, ctl);
        EVT(1);
        return;
    }
    if (w >= 32) vo_task(p, lds, w - 32, wave, lane, ctl);
    EVT(1);
    v8u wr[20];
    if (w < 32) {
        load_units<0>(p, w, lds, wave, lane, [&](int slot, v2u (&t)[4]) { wr[slot] = (v8u){t[0].x, t[0].y, t[1].x, t[1].y, t[2].x, t[2].y, t[3].x, t[3].y}; });
        load_mem0(p.mem0, lds, tid);
        for (int i = tid; i < 1024; i += 512) *(u32*)(lds + L_KMF + i * 4) = 0;
        const u32 par2 = wait_ge(ctl + CW_PAR, 1u, ctl);
        wait_ge(ctl + CW_READY, 16u, ctl);
        EVT(2);
        if (wave < 2) a_loop<0>(p, wr, lds, w, wave, lane, par2, ctl);
        else if (wave < 6) a_loop<1>(p, wr, lds, w, wave, lane, par2, ctl);
        else a_loop<2>(p, wr, lds, w, wave, lane, par2, ctl);
    } else {
        wait_ge(ctl + CW_VO, 96u, ctl);
        EVT(2);
        const rsrc_t rv = mkrs(p.ws + VO_OFF, VO_BYTES);
        const int vb = (w - 32) * 327680 + wave * 40960 + lane * 16;
#pragma unroll
        for (int i = 0; i < 20; ++i) {
            const int o = vb + (i & 1) * 20480 + (i >> 1) * 2048;
            wr[i] = cat8(ld128(rv, o), ld128(rv, o + 1024));
        }
        load_mem0(p.mem0, lds, tid);
        const u32 par2 = wait_ge(ctl + CW_PAR, 1u, ctl);
        wait_ge(ctl + CW_READY, 16u, ctl);
        EVT(3);
        r_loop(p, wr, lds, w, wave, lane, par2, ctl);
    }
    EVT(5);
#undef lane
#undef tid
}

int g_dA = 2850, g_dB = 700, g_dR = 1800, g_dZ = 0;
int g_only_chain = 0, g_dbg = 0;

extern "C" void kernel_launch(void* const* d_in, const int* in_sizes, int n_in,
                              void* d_out, int out_size, void* d_ws, size_t ws_size,
                              hipStream_t stream) {
    unsigned char* ws = (unsigned char*)(((size_t)d_ws + (2u << 20) - 1) & ~(size_t)((2u << 20) - 1));
    Params p;
    p.x = (const u16*)d_in[0]; p.mem0 = (const u16*)d_in[1]; p.Wq = (const u16*)d_in[2]; p.Wk = (const u16*)d_in[3]; p.Wv = (const u16*)d_in[4]; p.Wo = (const u16*)d_in[5];
    p.Are = (const u16*)d_in[8]; p.Aim = (const u16*)d_in[9]; p.Bre = (const u16*)d_in[10]; p.Bim = (const u16*)d_in[11]; p.Cre = (const u16*)d_in[12]; p.Cim = (const u16*)d_in[13];
    p.logdt = (const u16*)d_in[14]; p.dskip = (const u16*)d_in[15]; p.gw = (const u16*)d_in[16];
    p.out = (u16*)d_out; p.ws = ws;
    p.dA = g_dA; p.dB = g_dB; p.dR = g_dR; p.pad = 0;
    (void)hipMemsetAsync(ws + CTRL_OFF, 0, CTRL_BYTES, stream);
    (void)hipFuncSetAttribute((const void*)gm_kernel, hipFuncAttributeMaxDynamicSharedMemorySize, L_TOTAL);
    gm_kernel<<<256, 512, L_TOTAL, stream>>>(p);
}
```

```cpp
#include <hip/hip_runtime.h>
#include <hip/hip_bf16.h>
#include <stdint.h>

typedef unsigned short u16;
typedef unsigned int u32;
typedef u32 v4u __attribute__((ext_vector_type(4)));
typedef u32 v2u __attribute__((ext_vector_type(2)));
typedef u32 v8u __attribute__((ext_vector_type(8)));
typedef float f32x4 __attribute__((ext_vector_type(4)));
typedef __bf16 bf16x8 __attribute__((ext_vector_type(8)));
typedef __bf16 bf16x2 __attribute__((ext_vector_type(2)));
typedef short s16x4 __attribute__((ext_vector_type(4)));
typedef __amdgpu_buffer_rsrc_t rsrc_t;
#define GAS __attribute__((address_space(1)))
#define LAS __attribute__((address_space(3)))
#define RLX_AGENT __ATOMIC_RELAXED, __HIP_MEMORY_SCOPE_AGENT
#define DEV __device__ __forceinline__

constexpr int S = 2048, DM = 640, H = 32, DH = 128, NSEG = 64, HD = H * DH, NS = 128;
constexpr int SC1 = 16;
constexpr u32 SENT = 0xFFFFFFFFu;

DEV float bf2f(u32 b) { return __uint_as_float(b << 16); }
DEV float bfhi(u32 b) { return __uint_as_float(b & 0xFFFF0000u); }
DEV float rbf(float f) { return (float)(__bf16)f; }
DEV u32 pk2(float lo, float hi) { bf16x2 v = {(__bf16)lo, (__bf16)hi}; return __builtin_bit_cast(u32, v); }
DEV u32 pk2s(float lo, float hi) { return min(pk2(lo, hi), 0xFFFFFFFEu); }
DEV int rfl(int v) { return __builtin_amdgcn_readfirstlane(v); }
DEV int lane_id() { int l; asm volatile("v_mbcnt_lo_u32_b32 %0, -1, 0\n\tv_mbcnt_hi_u32_b32 %0, -1, %0" : "=v"(l)); return l; }

constexpr int CTRL_BYTES = 27648;
constexpr int ARENA_BYTES = 240 << 20;
constexpr int SLOT = 32768, MEM_O = 0, PX_O = 10240, PM_O = PX_O + 16384, TS_O = PM_O + 4096;
constexpr int VO_L = 2 << 20, QX_L = 28 << 20, KX_L = 44 << 20, ZM_L = 60 << 20, ZM_STEP = 80 * 4096;
constexpr int PT_L = 3 << 20, PT_STEP = 81920, BS_L = 8 << 20;
constexpr int ZX_P = 160 << 20, ZX_STEP = 80 * 16384;
constexpr int CW_TMO = 0, CW_PAR = 64, CW_SSM = 128  , CW_S = 512, CW_FIN = 1536, CW_P = 3328, CW_V = 5376;

DEV int amap(int o, int sel) { return ((o >> 13) << 14) | (o & 8191) | ((((o >> 20) ^ sel) & 1) << 13); }
DEV u32 max4(v4u v) { return max(max(v.x, v.y), max(v.z, v.w)); }
DEV v8u cat8(v4u a, v4u b) { return __builtin_shufflevector(a, b, 0, 1, 2, 3, 4, 5, 6, 7); }
DEV rsrc_t mkrs(const void* p, int bytes) { return __builtin_amdgcn_make_buffer_rsrc((void*)p, 0, bytes, 0x00020000); }
DEV v4u ld128(rsrc_t r, int off, int so) { return __builtin_amdgcn_raw_buffer_load_b128(r, off, so, SC1); }
DEV u32 ld32(rsrc_t r, int off, int so) { return __builtin_amdgcn_raw_buffer_load_b32(r, off, so, SC1); }
DEV void st128(v4u v, rsrc_t r, int off, int so) { __builtin_amdgcn_raw_buffer_store_b128(v, r, off, so, SC1); }
DEV void st32(u32 v, rsrc_t r, int off, int so) { __builtin_amdgcn_raw_buffer_store_b32(v, r, off, so, SC1); }
#define VMCNT(n) asm volatile("s_waitcnt vmcnt(%0)" :: "i"(n) : "memory")
#define BAR() do { asm volatile("s_waitcnt lgkmcnt(0)" ::: "memory"); __builtin_amdgcn_s_barrier(); asm volatile("" ::: "memory"); } while (0)
template <int CTRL> DEV float dppf(float v) { return __builtin_bit_cast(float, __builtin_amdgcn_update_dpp(0, __builtin_bit_cast(int, v), CTRL, 0xf, 0xf, true)); }
DEV float rowsum16(float v) { v += dppf<0x128>(v); v += dppf<0x124>(v); v += dppf<0x122>(v); v += dppf<0x121>(v); return v; }
DEV void dma16(u32 ldsaddr, int voff, rsrc_t rs, int soff) {
    u32 keep;
    asm volatile("s_nop 4\n\ts_mov_b32 %0, m0\n\ts_mov_b32 m0, %1\n\ts_nop 0\n\tbuffer_load_dwordx4 %2, %3, %4 offen lds\n\ts_mov_b32 m0, %0"
                 : "=&s"(keep) : "s"(ldsaddr), "v"(voff), "s"(rs), "s"(soff) : "memory");
}

constexpr u32 SPINCAP = 1u << 20;
#define EVT(i)

#define DV(slot, v)
#define DFLUSH()
#define DT(name)
struct Params {
    const u16 *x, *mem0, *Wq, *Wk, *Wv, *Wo, *Are, *Aim, *Bre, *Bim, *Cre, *Cim, *logdt, *dskip, *gw;
    u16* out;
    unsigned char* ws;
    int per, c2, gmax, cp, cmL, maxrel;
};
struct Ctx { GAS u32* ctl; rsrc_t ar; int par; };

constexpr int L_MEMF = 0, L_QF = 10240, L_KMF = 12288, L_KXF = 16384  , L_RED = 10240, L_ZT = 16384, L_STAGE = 24576, L_TOTAL = L_STAGE + 131072;

DEV bool spin_fail(u32& spins, GAS u32* ctl) {
    if (++spins > SPINCAP) { __hip_atomic_store(ctl + CW_TMO, 1u, RLX_AGENT); return true; }
    return (spins & 1023u) == 0 && rfl(__hip_atomic_load(ctl + CW_TMO, RLX_AGENT)) != 0;
}
DEV u32 wait_ge(GAS u32* w, u32 want, GAS u32* ctl) {
    u32 spins = 0, v;
    for (;;) { v = rfl(__hip_atomic_load(w, RLX_AGENT)); if (v >= want) break; __builtin_amdgcn_s_sleep(8); if (spin_fail(spins, ctl)) break; }
    asm volatile("" ::: "memory");
    return v;
}
template <int MODE> DEV int prog_load(int h, int lane, GAS u32* ctl) { return (int)__hip_atomic_load(ctl + CW_P + 16 * (MODE == 0 ? 2 * h + (lane & 1) : 64 + lane), RLX_AGENT); }
template <int MODE>
DEV int wait_upto(int v, int need, int h, int lane, GAS u32* ctl) {
    u32 spins = 0; int r;
    for (;;) {
        v = max(v, lane & 1);
        v = min(v, __builtin_amdgcn_update_dpp(64, v, 0x128, 0xf, 0xf, false)); v = min(v, __builtin_amdgcn_update_dpp(64, v, 0x124, 0xf, 0xf, false));
        v = min(v, __builtin_amdgcn_update_dpp(64, v, 0x122, 0xf, 0xf, false)); v = min(v, __builtin_amdgcn_update_dpp(64, v, 0x121, 0xf, 0xf, false));
        r = min(min(__builtin_amdgcn_readlane(v, 0), __builtin_amdgcn_readlane(v, 16)), min(__builtin_amdgcn_readlane(v, 32), __builtin_amdgcn_readlane(v, 48)));
        if (r > need) break;
        __builtin_amdgcn_s_sleep(8);
        if (spin_fail(spins, ctl)) { r = 64; break; }
        v = prog_load<MODE>(h, lane, ctl);
    }
    asm volatile("" ::: "memory");
    return r;
}
DEV u32 now32() { unsigned long long t; asm volatile("s_memtime %0\n s_waitcnt lgkmcnt(0)" : "=s"(t) :: "memory"); return (u32)t; }
DEV u32 gnow() { unsigned long long t; asm volatile("s_memrealtime %0\n s_waitcnt lgkmcnt(0)" : "=s"(t) :: "memory"); return (u32)t * 24u + 48u; }
DEV u32 wait_global(u32 T) {
    u32 off, e, d;
    asm volatile(
        "s_memrealtime vcc\n s_memtime s[92:93]\n s_waitcnt lgkmcnt(0)\n"
        "s_mul_i32 %0, vcc_lo, 24\n s_sub_u32 %0, %0, s92\n"
        "1:\n"
        "s_memrealtime vcc\n s_memtime s[92:93]\n s_waitcnt lgkmcnt(0)\n"
        "s_mul_i32 %1, vcc_lo, 24\n s_sub_u32 %1, %1, s92\n"
        "s_sub_u32 %2, %1, %0\n s_cmp_gt_i32 %2, 0\n s_cselect_b32 %0, %1, %0\n"
        "s_sub_u32 %2, %3, s92\n s_sub_u32 %2, %2, %0\n"
        "s_cmp_gt_i32 %2, 700\n s_cbranch_scc0 2f\n s_cmp_gt_i32 %2, 0x40000\n s_cbranch_scc1 4f\n s_sleep 2\n s_branch 1b\n"
        "2:\n s_cmp_gt_i32 %2, 230\n s_cbranch_scc1 1b\n"
        "3:\n s_sub_u32 %2, %2, 32\n s_cmp_gt_i32 %2, 120\n s_cbranch_scc1 3b\n"
        "4:\n"
        : "=&s"(off), "=&s"(e), "=&s"(d) : "s"(T) : "memory", "scc", "vcc", "s92", "s93");
    return off;
}
#define RAWBAR() do { __builtin_amdgcn_s_barrier(); asm volatile("" ::: "memory"); } while (0)
DEV u32 read_ref(u32 v, const Ctx& cx, int ad, int so) {
    u32 spins = 0;
    while (__builtin_expect(__any(v == SENT), 0)) {
        if (spin_fail(spins, cx.ctl)) break;
        __builtin_amdgcn_s_sleep(2);
        asm volatile("" ::: "memory");
        v = ld32(cx.ar, ad, so);
    }
    const u32 b = rfl(v);
    int d = (int)(v - b);
    d = max(d, __builtin_amdgcn_update_dpp(0, d, 0x128, 0xf, 0xf, true)); d = max(d, __builtin_amdgcn_update_dpp(0, d, 0x124, 0xf, 0xf, true));
    d = max(d, __builtin_amdgcn_update_dpp(0, d, 0x122, 0xf, 0xf, true)); d = max(d, __builtin_amdgcn_update_dpp(0, d, 0x121, 0xf, 0xf, true));
    return b + (u32)max(__builtin_amdgcn_readlane(d, 0), __builtin_amdgcn_readlane(d, 16));
}

struct Pace { u32 p1; int c2, per, relax; bool sched; };
DEV void pace_next(Pace& q, u32 tref, bool first, const Params& p) {
    const int g = (int)(tref - q.p1);
    const bool late = first || g > p.gmax;
    if (late && q.sched && q.relax < p.maxrel) { if (g > p.gmax + 800) q.c2 += 48; q.per += 56; ++q.relax; }
    q.p1 = late ? tref + q.per - p.cmL : q.p1 + q.per;
    q.sched = !late;
}

#define SMF_A(acc, a, w) asm volatile("v_smfmac_f32_16x16x64_bf16 %0, %1, %2, %3" : "+v"(acc) : "v"(a), "a"(w), "v"(sidx))
#define SMF_V(acc, a, w) asm volatile("v_smfmac_f32_16x16x64_bf16 %0, %1, %2, %3" : "+v"(acc) : "v"(a), "v"(w), "v"(sidx))
#define SMF_W(acc, a, i) do { if ((i) < 16) SMF_A(acc, a, wr[i]); else SMF_V(acc, a, wr[i]); } while (0)
#define MF_WA(acc, i, b) do { if ((i) < 32) asm volatile("v_mfma_f32_16x16x32_bf16 %0, %1, %2, %0" : "+v"(acc) : "a"(w4[i]), "v"(b)); \
                              else asm volatile("v_mfma_f32_16x16x32_bf16 %0, %1, %2, %0" : "+v"(acc) : "v"(w4[i]), "v"(b)); } while (0)
#define MF_WB(acc, a, i) do { if ((i) < 32) asm volatile("v_mfma_f32_16x16x32_bf16 %0, %1, %2, %0" : "+v"(acc) : "v"(a), "a"(w4[i])); \
                              else asm volatile("v_mfma_f32_16x16x32_bf16 %0, %1, %2, %0" : "+v"(acc) : "v"(a), "v"(w4[i])); } while (0)
#define LDS_RD128(dst, addr, off) asm volatile("ds_read_b128 %0, %1 offset:%2" : "=v"(dst) : "v"(addr), "i"(off) : "memory")
#define LGKM_WAIT(n, x) asm volatile("s_waitcnt lgkmcnt(%1)" : "+v"(x) : "i"(n) : "memory")
#define LGKM_WAIT2(n, x, y) asm volatile("s_waitcnt lgkmcnt(%2)" : "+v"(x), "+v"(y) : "i"(n) : "memory")
#define SIDX_DECL int sidx = (lane & 1) ? 0xEEEE : 0x4444; asm volatile("s_nop 1" : "+v"(sidx))
DEV float dmax16(float v) {
    asm volatile("s_nop 1\n v_max_f32_dpp %0, %0, %0 row_ror:8 row_mask:0xf bank_mask:0xf\n s_nop 1\n v_max_f32_dpp %0, %0, %0 row_ror:4 row_mask:0xf bank_mask:0xf\n"
                 "s_nop 1\n v_max_f32_dpp %0, %0, %0 row_ror:2 row_mask:0xf bank_mask:0xf\n s_nop 1\n v_max_f32_dpp %0, %0, %0 row_ror:1 row_mask:0xf bank_mask:0xf" : "+v"(v));
    return v;
}
DEV float max3f(float a, float b, float c) { float r; asm("v_max3_f32 %0, %1, %2, %3" : "=v"(r) : "v"(a), "v"(b), "v"(c)); return r; }
DEV f32x4 mfma16(v4u a, v4u b, f32x4 c) { return __builtin_amdgcn_mfma_f32_16x16x32_bf16(__builtin_bit_cast(bf16x8, a), __builtin_bit_cast(bf16x8, b), c, 0, 0, 0); }

template <int MODE, class Sink>
DEV void load_units(const Params& p, int h, unsigned char* lds, int wave, int lane, Sink sink) {
    constexpr int NU = MODE == 3 ? 10 : 20, NR = (NU + 3) / 4;
    const u32 lds0 = (u32)(size_t)(LAS unsigned char*)lds;
    const int tp = lane >> 3, q4 = (lane >> 1) & 3, hf = lane & 1;
    auto issue = [&](int r) {
        const int u = r * 4 + (wave >> 1), hh = wave & 1;
        if (u >= NU) return;
        bool second; int base, stride, map;
        if (MODE <= 1) { second = u & 1; base = (64 * (u >> 1) * HD + h * DH) * 2; stride = HD * 2; map = MODE; }
        else if (MODE == 2 && u < 10) { second = false; base = (64 * u * HD + h * DH) * 2; stride = HD * 2; map = 1; }
        else { const int uo = MODE == 2 ? u - 10 : u; second = true; base = ((h * DH + 64 * (uo & 1)) * DM + (uo >> 1) * 128) * 2; stride = DM * 2; map = MODE == 2 ? 2 : 1; }
        const int gq = map == 0 ? 8 * (q4 >> 1) + (q4 & 1) : q4;
        const rsrc_t rs = mkrs(second ? (MODE <= 1 ? p.Wk : p.Wo) : (MODE <= 1 ? p.Wq : p.Wv), DM * HD * 2);
#pragma unroll
        for (int i = 0; i < 8; ++i) {
            const int gB = i >> 1, i4 = i & 1;
            const int f = 32 * hh + (map == 0 ? 16 * (gB >> 1) + 4 * (gB & 1) + 2 * i4 : map == 1 ? 8 * gB + 4 * i4 : 16 * i4 + 4 * gB);
            const int voff = gq * stride + ((tp ^ (gB & 1)) * 16 + hf * 8) * 2;
            const u32 la = rfl(lds0 + L_STAGE + (r & 1) * 65536 + (wave >> 1) * 16384 + (hh * 8 + i) * 1024);
            dma16(la, voff, rs, rfl(base + f * stride));
        }
    };
    const int gB = lane >> 4;
    const int rd0 = (2 * gB) * 1024 + ((lane & 15) >> 2) * 32 + (lane & 3) * 8;
    issue(0);
#pragma unroll
    for (int r = 0; r < NR; ++r) {
        if (r + 1 < NR) { issue(r + 1); if ((r + 1) * 4 + 3 < NU) VMCNT(8); else VMCNT(0); } else if (MODE != 3) VMCNT(0);
        BAR();
#pragma unroll
        for (int k = 0; k < 4; ++k) {
            int uu, tl;
            if (MODE == 1) { uu = (k >> 1) * 2 + (wave >> 2); tl = 2 * (wave & 3) + (k & 1); } else { uu = k; tl = wave; }
            if (MODE != 1 && r * 4 + k >= NU) continue;
            v2u t[4];
#pragma unroll
            for (int j = 0; j < 4; ++j) {
                s16x4 v = __builtin_amdgcn_ds_read_tr16_b64_v4i16((LAS s16x4*)(lds + L_STAGE + (r & 1) * 65536 + uu * 16384 + ((j >> 1) * 8 + (j & 1)) * 1024 + rd0 + (tl ^ (gB & 1)) * 128));
                t[j] = __builtin_bit_cast(v2u, v);
            }
            sink(r * 4 + k, t);
        }
        BAR();
    }
}

constexpr u32 POLLCAP = 1u << 17;
#define POLL_COUNT "s_add_u32 %[cnt], %[cnt], 1\n s_cmp_lt_u32 %[cnt], %[cap]\n s_cselect_b32 %[key], %[key], -1\n"
DEV void gather_mem(const Ctx& cx, int gl, int s0, int s1, u32 ldst, u32& key) {
    u32 cnt = 0;
    asm volatile(
        "s_barrier\n"
        "1:\n"
        "buffer_load_dwordx4 v[116:119], %[gl], %[rs], %[s0] offen sc1\n"
        "buffer_load_dwordx4 v[120:123], %[gl], %[rs], %[s1] offen sc1\n"
        POLL_COUNT
        "s_waitcnt vmcnt(1)\n"
        "ds_write_b128 %[ld], v[116:119]\n"
        "v_max3_u32 v124, v116, v117, v118\n"
        "s_waitcnt vmcnt(0)\n"
        "ds_write_b128 %[ld], v[120:123] offset:1024\n"
        "v_max3_u32 v124, v124, v119, v120\n"
        "v_max3_u32 v124, v124, v121, v122\n"
        "v_max_u32 v124, v124, v123\n"
        "v_cmp_lt_u32 vcc, %[key], v124\n"
        "s_waitcnt lgkmcnt(0)\n"
        "s_cbranch_vccnz 1b\n"
        "s_barrier\n"
        : [cnt] "+s"(cnt), [key] "+s"(key)
        : [gl] "v"(gl), [rs] "s"(cx.ar), [s0] "s"(s0), [s1] "s"(s1), [ld] "v"(ldst), [cap] "s"(POLLCAP)
        : "memory", "scc", "vcc", "v116", "v117", "v118", "v119", "v120", "v121", "v122", "v123", "v124");
}
DEV void load_mem0(const u16* mem0, unsigned char* lds, int tid) {
    for (int i = tid; i < 640; i += 512) { const int m = i / 80, c8 = i % 80; *(v4u*)(lds + L_MEMF + (c8 >> 1) * 256 + (2 * m + (c8 & 1)) * 16) = *(const v4u*)(mem0 + m * DM + c8 * 8); }
}

template <int CLS>
DEV void a_role(const Params& p, const Ctx& cx, unsigned char* lds, int h, int wave) {
    v8u wr[20];
    load_units<0>(p, h, lds, wave, lane_id(), [&](int slot, v2u (&t)[4]) { wr[slot] = (v8u){t[0].x, t[0].y, t[1].x, t[1].y, t[2].x, t[2].y, t[3].x, t[3].y}; });
    load_mem0(p.mem0, lds, wave * 64 + lane_id());
    for (int i = wave * 64 + lane_id(); i < 1024; i += 512) *(u32*)(lds + L_KMF + i * 4) = 0;
    const int par = cx.par, far = par ^ 1;
    EVT(2);
    const int lane = lane_id(), n = lane & 15, gq = lane >> 4;
    SIDX_DECL;
    const u32 lbase = (u32)(size_t)(LAS unsigned char*)lds + lane * 16;
    const int gl = lane * 16;
    const int qw = L_QF + (wave >> 2) * 1024 + (2 * ((wave >> 1) & 1) + (n >> 3)) * 256 + (2 * gq) * 32 + ((n >> 1) & 1) * 16 + (4 * ((n >> 2) & 1) + 2 * (wave & 1) + (n & 1)) * 2;
    const int kw = L_KMF + (wave >> 1) * 1024 + (16 * (n >> 2) + gq) * 16 + (4 * (wave & 1) + (n & 3)) * 2;
    const int pair = 8 * (n >> 3) + 2 * (n & 3) + ((n >> 2) & 1);
    const int tk0 = 2 * pair, tk1 = 2 * pair + 1;
    const u32 lq = (u32)(size_t)(LAS unsigned char*)lds + gq * 256 + (4 * (wave & 1) + (n >> 2)) * 32 + (n & 1) * 16;
    const int adpx = (n >> 3) * 256 + gq * 32 + (n & 7) * 4, upx = PX_O + h * 512 + (wave & 1) * 128;
    const int adpm = gq * 16 + (n & 3) * 4, upm = PM_O + h * 128 + (wave & 1) * 64;
    const int g8 = (wave - 2) & 3;
    const int t0 = 8 * g8 + 2 * gq;
    const int adtx = (n >> 3) * 256 + (2 * gq) * 32 + (n & 7) * 4, utx = (g8 * 32 + h) * 512;
    const int adtm = (2 * gq) * 32 + (n & 1) * 16 + ((n >> 1) & 1) * 4, utm = 65536 + (g8 * 16 + (h >> 1)) * 256 + (h & 1) * 8;
    const float SC = 0.08838834764831845f * 1.4426950408889634f;
    const float NEGR = -1.0e9f / 0.08838834764831845f;
    const float pad0 = (n < 8) ? 0.f : -__builtin_inff();
    const bool pmlane = n < 4;
    const int adts = (lane & 31) * 64;
    int ready = 0;
    v4u qx0, qx1, qn0, qn1;
    f32x4 s1, s2;
    auto tok_load = [&](int sn) {
        const int qb = rfl(amap(QX_L + (sn * H + h) * 8192 + g8 * 2048, par));
        qn0 = ld128(cx.ar, gl, qb); qn1 = ld128(cx.ar, gl + 1024, qb);
    };
    auto tok_prep = [&](int sn) {
        qx0 = qn0; qx1 = qn1;
        v4u kx[8];
#pragma unroll
        for (int i = 0; i < 8; ++i) kx[i] = *(const v4u*)(lds + L_KXF + (sn & 1) * 8192 + i * 1024 + lane * 16);
        s1 = (f32x4){0, 0, 0, 0}; s2 = s1;
        asm volatile("s_nop 1" : "+v"(s1), "+v"(s2), "+v"(qx0), "+v"(qx1));
        SMF_V(s1, qx0, cat8(kx[0], kx[1]));
        SMF_V(s2, qx0, cat8(kx[4], kx[5]));
        SMF_V(s1, qx1, cat8(kx[2], kx[3]));
        SMF_V(s2, qx1, cat8(kx[6], kx[7]));
        asm volatile("s_nop 7" : "+v"(s1), "+v"(s2));
        s1.x = (tk0 <= t0) ? s1.x + s1.y : NEGR; s1.z = (tk0 <= t0 + 1) ? s1.z + s1.w : NEGR;
        s1.y = (tk1 <= t0) ? s2.x + s2.y : NEGR; s1.w = (tk1 <= t0 + 1) ? s2.z + s2.w : NEGR;
    };
    auto help_prep = [&](int sn) {
        if (sn >= ready) ready = wait_upto<0>(prog_load<0>(h, lane, cx.ctl), sn, h, lane, cx.ctl);
        const int kb = rfl(amap(KX_L + (sn * H + h) * 8192, par));
        v4u t[8];
#pragma unroll
        for (int i = 0; i < 8; ++i) t[i] = ld128(cx.ar, gl + (i & 3) * 1024, kb + (i >> 2) * 4096);
#pragma unroll
        for (int i = 0; i < 8; ++i) *(v4u*)(lds + L_KXF + (sn & 1) * 8192 + i * 1024 + lane * 16) = t[i];
    };
    if (CLS == 2) help_prep(wave - 6);
    BAR();
    if (CLS == 1) { tok_load(0); tok_prep(0); }
    Pace pc = {0u, p.c2, p.per, 0, false};
    u32 off = gnow() - now32(), key = 0xFFFFFFFEu;
    const u32 gdst = (u32)(size_t)(LAS unsigned char*)lds + L_MEMF + wave * 2048 + gq * 256 + (n & 7) * 32 + (n >> 3) * 16;
    for (int s = 0; s < NSEG; ++s) {
        const int sb = s * SLOT;
        f32x4 aq = {0, 0, 0, 0}, ak = {0, 0, 0, 0};
        asm volatile("" : "+v"(aq), "+v"(ak));
        const int xpx = rfl(amap(sb + upx, par)), xpm = rfl(amap(sb + upm, par)), xts = rfl(amap(sb + TS_O, par));
        VMCNT(0);
        if (s > 0) {
            const int x0 = rfl(amap(sb + MEM_O + wave * 2048, par)), x1 = rfl(amap(sb + MEM_O + wave * 2048 + 1024, par));
            asm volatile("" :: "s"(x0), "s"(x1), "s"(xpx), "s"(xpm), "s"(xts));
            asm volatile("s_waitcnt lgkmcnt(0)" ::: "memory");
            if (CLS == 2 && wave == 7) off = wait_global(pc.p1);
            if (CLS == 0 || (CLS == 1 && wave < 5)) gather_mem(cx, gl, x0, x1, gdst, key);
            else { RAWBAR(); RAWBAR(); }
        } else BAR();
        if (CLS == 2 && wave == 7) { const u32 ts = min(now32() + off, 0xFFFFFFFEu); if (lane == 0) st32(ts, cx.ar, h * 64, xts); }
        {
            v4u a_[5];
#pragma unroll
            for (int ks = 0; ks < 5; ++ks) LDS_RD128(a_[ks], lbase, L_MEMF + ks * 1024);
#pragma unroll
            for (int ks = 0; ks < 5; ++ks) { LGKM_WAIT(4, a_[ks]); SMF_W(aq, a_[ks], 2 * ks); SMF_W(ak, a_[ks], 2 * ks + 1); LDS_RD128(a_[ks], lbase, L_MEMF + (ks + 5) * 1024); }
#pragma unroll
            for (int ks = 5; ks < 10; ++ks) { LGKM_WAIT(9 - ks, a_[ks - 5]); SMF_W(aq, a_[ks - 5], 2 * ks); SMF_W(ak, a_[ks - 5], 2 * ks + 1); }
        }
        v4u kx[8];
        if (CLS == 0) {
            const u32 lk = lbase + (s & 1) * 8192;
#pragma unroll
            for (int i = 0; i < 8; ++i) LDS_RD128(kx[i], lk, L_KXF + i * 1024);
        } else asm volatile("s_nop 7");
        asm volatile("" : "+v"(aq), "+v"(ak));
        {
            const u32 q2 = pk2(aq.x + aq.y, aq.z + aq.w), k2 = pk2(ak.x + ak.y, ak.z + ak.w);
            *(u16*)(lds + qw) = (u16)q2; *(u16*)(lds + qw + 32) = (u16)(q2 >> 16);
            *(u16*)(lds + kw) = (u16)k2; *(u16*)(lds + kw + 64) = (u16)(k2 >> 16);
        }
        f32x4 s0 = {pad0, 0, pad0, 0};
        if (CLS == 0) { s1 = (f32x4){0, 0, 0, 0}; s2 = s1; asm volatile("" : "+v"(s0), "+v"(s1), "+v"(s2) : "s"(xpx), "s"(xpm)); }
        if (CLS == 1) asm volatile("" : "+v"(s0));
        BAR();
        if (CLS == 0) {
            v4u a0, a1, km[4];
            LDS_RD128(a0, lq, L_QF); LDS_RD128(a1, lq, L_QF + 1024);
#pragma unroll
            for (int i = 0; i < 4; ++i) LDS_RD128(km[i], lbase, L_KMF + i * 1024);
            LGKM_WAIT(5, a0);
            SMF_V(s1, a0, cat8(kx[0], kx[1]));
            SMF_V(s2, a0, cat8(kx[4], kx[5]));
            LGKM_WAIT(4, a1);
            SMF_V(s1, a1, cat8(kx[2], kx[3]));
            SMF_V(s2, a1, cat8(kx[6], kx[7]));
            LGKM_WAIT2(2, km[0], km[1]);
            SMF_V(s0, a0, cat8(km[0], km[1]));
            LGKM_WAIT2(0, km[2], km[3]);
            SMF_V(s0, a1, cat8(km[2], km[3]));
            asm volatile("s_nop 4" : "+v"(s1), "+v"(s2));
            float x1 = s1.x + s1.y, x2 = s2.x + s2.y;
            asm volatile("s_nop 0" : "+v"(s0), "+v"(x1), "+v"(x2));
            const float x0 = s0.x + s0.y;
            const float mx = -SC * dmax16(max3f(x0, x1, x2));
            const float e0 = __builtin_amdgcn_exp2f(__builtin_fmaf(x0, SC, mx)), e1 = __builtin_amdgcn_exp2f(__builtin_fmaf(x1, SC, mx)), e2 = __builtin_amdgcn_exp2f(__builtin_fmaf(x2, SC, mx));
            const float inv = __builtin_amdgcn_rcpf(rowsum16(e0 + e1 + e2));
            const float p0 = e0 * inv;
            st32(pk2s(e1 * inv, e2 * inv), cx.ar, adpx, xpx);
            const u32 pm = pk2s(p0, dppf<0x104>(p0));
            if (pmlane) st32(pm, cx.ar, adpm, xpm);
            DT(d_pub); DV(3, d_pub);
        } else if (CLS == 1) {
            __builtin_amdgcn_s_sleep(4);
            if (s + 1 < NSEG) tok_load(s + 1);
            v4u km[4];
#pragma unroll
            for (int i = 0; i < 4; ++i) km[i] = *(const v4u*)(lds + L_KMF + i * 1024 + lane * 16);
            SMF_V(s0, qx0, cat8(km[0], km[1]));
            SMF_V(s0, qx1, cat8(km[2], km[3]));
            asm volatile("s_nop 7" : "+v"(s0));
            const int xtx = rfl(amap(PT_L + s * PT_STEP + utx, far)), xtm = rfl(amap(PT_L + s * PT_STEP + utm, far));
#pragma unroll
            for (int rr = 0; rr < 2; ++rr) {
                const float x0 = rr ? s0.z + s0.w : s0.x + s0.y, x1 = rr ? s1.z : s1.x, x2 = rr ? s1.w : s1.y;
                const float mx = -SC * dmax16(max3f(x0, x1, x2));
                const float e0 = __builtin_amdgcn_exp2f(__builtin_fmaf(x0, SC, mx)), e1 = __builtin_amdgcn_exp2f(__builtin_fmaf(x1, SC, mx)), e2 = __builtin_amdgcn_exp2f(__builtin_fmaf(x2, SC, mx));
                const float inv = __builtin_amdgcn_rcpf(rowsum16(e0 + e1 + e2));
                const float p0 = e0 * inv;
                st32(pk2(e1 * inv, e2 * inv), cx.ar, adtx + rr * 32, xtx);
                const u32 pm = pk2(p0, dppf<0x104>(p0));
                if (pmlane) st32(pm, cx.ar, adtm + rr * 32, xtm);
            }
            if (s + 1 < NSEG) tok_prep(s + 1);
        } else {
            __builtin_amdgcn_s_sleep(6);
            if (wave == 6 && s + 2 < NSEG) help_prep(s + 2);
        }
        if (CLS == 2 && wave == 7) {
            pace_next(pc, read_ref(ld32(cx.ar, adts, xts), cx, adts, xts), s == 0, p);
            if (h == 0 && s >= 1 && lane == 0) __hip_atomic_store(cx.ctl + CW_S + 16 * (s - 1), 1u, RLX_AGENT);
        }
    }
    if (key == SENT) __hip_atomic_store(cx.ctl + CW_TMO, 1u, RLX_AGENT);
    VMCNT(0);
    BAR();
    if (CLS == 2 && wave == 7 && lane == 0) __hip_atomic_store(cx.ctl + CW_FIN + 16 * h, 1u, RLX_AGENT);
    DFLUSH();
}

DEV void vo_task(const Params& p, const Ctx& cx, unsigned char* lds, int v, int wave) {
    const int h = 8 * (v & 3) + (v >> 2) / 3, part = (v >> 2) % 3;
    const int g0 = 7 * part, ng = min(20 - g0, 7);
    const int lane = lane_id(), i = lane & 15, gq = lane >> 4;
    const u32 lds0 = (u32)(size_t)(LAS unsigned char*)lds;
    constexpr int LV = 0, LO = 57344;
    {
        const int row = wave * 4 + gq, f = (row & 13) | ((row >> 4) << 1);
        const rsrc_t rv = mkrs(p.Wv, DM * HD * 2);
#pragma unroll
        for (int g = 0; g < 7; ++g) dma16(rfl(lds0 + LV + g * 8192 + wave * 1024), row * (HD * 2) + ((i ^ f) * 16), rv, rfl((min(g0 + g, 19) * 32 * HD + h * DH) * 2));
    }
    const rsrc_t ro = mkrs(p.Wo, DM * HD * 2);
    const int tp = lane >> 3, q4 = (lane >> 1) & 3, hf = lane & 1;
    auto issue = [&](int cg) {
#pragma unroll
        for (int k = 0; k < 4; ++k)
            dma16(rfl(lds0 + LO + (cg % 3) * 32768 + (wave * 4 + k) * 1024), q4 * (DM * 2) + ((tp ^ (k >> 1)) * 16 + hf * 8) * 2, ro, rfl(((h * DH + wave * 16 + k * 4) * DM + cg * 128) * 2));
    };
    issue(0); issue(1); issue(2);
    const int rd0 = LO + (2 * gq) * 1024 + (i >> 2) * 32 + (i & 3) * 8 + (wave ^ (gq & 1)) * 128;
    const int dl = 16 * (i >> 3) + 8 * ((i >> 1) & 1) + 4 * ((i >> 2) & 1) + (i & 1);
    const int fl = (dl & 13) | ((dl >> 4) << 1);
#pragma unroll 1
    for (int cg = 0; cg < 5; ++cg) {
        if (cg == 0) VMCNT(8); else if (cg == 1) VMCNT(14); else if (cg < 4) VMCNT(16); else VMCNT(12);
        BAR();
        if (cg == 0) EVT(6);
        v4u w[4];
#pragma unroll
        for (int ss = 0; ss < 4; ++ss) {
            const v2u t0 = __builtin_bit_cast(v2u, __builtin_amdgcn_ds_read_tr16_b64_v4i16((LAS s16x4*)(lds + rd0 + (cg % 3) * 32768 + ss * 8192)));
            const v2u t1 = __builtin_bit_cast(v2u, __builtin_amdgcn_ds_read_tr16_b64_v4i16((LAS s16x4*)(lds + rd0 + (cg % 3) * 32768 + ss * 8192 + 1024)));
            w[ss] = (v4u){t0.x, t0.y, t1.x, t1.y};
        }
        if (cg < 2) { BAR(); issue(cg + 3); }
        for (int g = 0; g < ng; ++g) {
            f32x4 d0 = {0, 0, 0, 0}, d1 = d0;
#pragma unroll
            for (int ss = 0; ss < 4; ++ss) {
                d0 = mfma16(*(const v4u*)(lds + LV + g * 8192 + dl * 256 + (((4 * ss + gq) ^ fl) * 16)), w[ss], d0);
                d1 = mfma16(*(const v4u*)(lds + LV + g * 8192 + (dl + 2) * 256 + (((4 * ss + gq) ^ fl) * 16)), w[ss], d1);
            }
            st128((v4u){pk2(d0.x, d0.y), pk2(d0.z, d0.w), pk2(d1.x, d1.y), pk2(d1.z, d1.w)}, cx.ar, lane * 16, rfl(amap(VO_L + ((h * 20 + g0 + g) * 40 + cg * 8 + wave) * 1024, cx.par)));
        }
    }
    EVT(7);
    VMCNT(0);
    BAR();
    if (wave == 0 && lane == 0) __hip_atomic_store(cx.ctl + CW_V + 16 * v, 1u, RLX_AGENT);
}
DEV void wait_flags96(GAS u32* base, int lane, GAS u32* ctl) {
    u32 spins = 0;
    for (;;) {
        const u32 a = __hip_atomic_load(base + 16 * lane, RLX_AGENT), c = __hip_atomic_load(base + 16 * (64 + (lane & 31)), RLX_AGENT);
        if (__all(a && c)) break;
        __builtin_amdgcn_s_sleep(4);
        if (spin_fail(spins, ctl)) break;
    }
    asm volatile("" ::: "memory");
}

DEV void r_role(const Params& p, const Ctx& cx, unsigned char* lds, int c8, int wave) {
    const int lane = lane_id(), n = lane & 15, gq = lane >> 4;
    const int par = cx.par;
    load_mem0(p.mem0, lds, wave * 64 + lane);
    if (wave == 0) wait_flags96(cx.ctl + CW_V, lane, cx.ctl);
    BAR();
    EVT(2);
    v8u wr[20];
    {
        const int vb = VO_L + (((4 * wave + (n >> 3)) * 20) * 40 + (c8 >> 1)) * 1024 + (16 * gq + 8 * (c8 & 1) + (n & 7)) * 16;
#pragma unroll
        for (int i = 0; i < 20; ++i) {
            const int o = vb + (i & 1) * (2 * 20 * 40960) + (i >> 1) * (2 * 40960);
            wr[i] = cat8(ld128(cx.ar, amap(o, par), 0), ld128(cx.ar, amap(o + 40960, par), 0));
        }
    }
    EVT(3);
    SIDX_DECL;
    const u32 lbase = (u32)(size_t)(LAS unsigned char*)lds + lane * 16;
    const int gl = lane * 16;
    const int ztw = L_ZT + wave * 512 + (n >> 3) * 128 + (n & 7) * 16 + gq * 4;
    const int zl = gq * 128 + (n & 7) * 16;
    const int pml = gq * 128 + (n >> 1) * 16;
    const u32 redw = (u32)(size_t)(LAS unsigned char*)lds + L_RED + (n >> 3) * 2560 + ((2 * gq) * 4 + ((n & 7) >> 1)) * 80 + wave * 8 + (n & 1) * 4;
    const u32 gdst = (u32)(size_t)(LAS unsigned char*)lds + L_MEMF + wave * 2048 + gq * 256 + (n & 7) * 32 + (n >> 3) * 16;
    u32 key = 0xFFFFFFFEu;
    const int redr = L_RED + (lane & 31) * 80;
    const int adts = (lane & 31) * 64;
    int ready = 0;
    if (wave == 6) ready = wait_upto<1>(prog_load<1>(0, lane, cx.ctl), 0, 0, lane, cx.ctl);
    Pace pc = {0u, p.c2, p.per, 0, false};
    for (int s = 0; s < NSEG; ++s) {
        const int sb = s * SLOT;
        f32x4 z0 = {0, 0, 0, 0}, z1 = z0;
        asm volatile("" : "+v"(z0), "+v"(z1));
        const int x0 = rfl(amap(sb + MEM_O + wave * 2048, par)), x1 = rfl(amap(sb + MEM_O + wave * 2048 + 1024, par));
        const int xzx = ZX_P + s * ZX_STEP + c8 * 16384 + wave * 2048;
        asm volatile("" :: "s"(x0), "s"(x1), "s"(xzx));
        if (wave == 1 && s > 0) wait_global(pc.p1);
        int pv = 64;
        if (s > 0 && wave < 5) gather_mem(cx, gl, x0, x1, gdst, key);
        else {
            BAR();
            if (wave == 6 && s + 1 >= ready) { __builtin_amdgcn_s_sleep(4); pv = prog_load<1>(0, lane, cx.ctl); }
            RAWBAR();
        }
        const v4u zx0 = ld128(cx.ar, gl, xzx), zx1 = ld128(cx.ar, gl + 1024, xzx);
        const int xts = rfl(amap(sb + TS_O, par));
        u32 tv = 0;
        bool late = false;
        if (wave == 1) late = s == 0 || (int)(gnow() - pc.p1) > p.gmax;
        DT(d_b1);
        {
            v4u a_[5];
#pragma unroll
            for (int ks = 0; ks < 5; ++ks) LDS_RD128(a_[ks], lbase, L_MEMF + ks * 1024);
#pragma unroll
            for (int ks = 0; ks < 5; ++ks) { LGKM_WAIT(4, a_[ks]); SMF_W(z0, a_[ks], 2 * ks); SMF_W(z1, a_[ks], 2 * ks + 1); LDS_RD128(a_[ks], lbase, L_MEMF + (ks + 5) * 1024); }
#pragma unroll
            for (int ks = 5; ks < 10; ++ks) { LGKM_WAIT(9 - ks, a_[ks - 5]); SMF_W(z0, a_[ks - 5], 2 * ks); SMF_W(z1, a_[ks - 5], 2 * ks + 1); }
        }
        asm volatile("s_nop 7" : "+v"(z0), "+v"(z1));
        *(u32*)(lds + ztw) = pk2(z0.x + z0.y, z0.z + z0.w);
        *(u32*)(lds + ztw + 256) = pk2(z1.x + z1.y, z1.z + z1.w);
        asm volatile("s_waitcnt lgkmcnt(0)" ::: "memory");
        v4u zm = *(const v4u*)(lds + L_ZT + wave * 512 + zl);
        if (n < 8) st128(zm, cx.ar, zl, rfl(amap(ZM_L + s * ZM_STEP + c8 * 4096 + wave * 512, par)));
        if (s + 1 == NSEG) break;
        if (wave == 6 && s + 1 >= ready) ready = wait_upto<1>(pv, s + 1, 0, lane, cx.ctl);
        DT(d_zp);
        u32 dsp = 0;
        DV(6, pc.p1);
        const int xpub = rfl(amap(sb + SLOT + MEM_O + c8 * 128, par));
        const int xx0 = rfl(amap(sb + PX_O + wave * 2048, par)), xx1 = rfl(amap(sb + PX_O + wave * 2048 + 1024, par)), xm = rfl(amap(sb + PM_O + wave * 512, par));
        v8u zb0 = cat8(zx0, zx1), zb1;
#pragma unroll
        for (int i = 0; i < 8; ++i) zb1[i] = __builtin_amdgcn_update_dpp(0, zb0[i], 0x108, 0xf, 0xf, true);
        asm volatile("" : "+v"(zb0), "+v"(zb1), "+v"(zm) : "s"(xx0), "s"(xx1), "s"(xm), "s"(xts), "s"(xpub));
        if (wave == 1) {
            tv = ld32(cx.ar, adts, xts);
            wait_global(late ? read_ref(tv, cx, adts, xts) + pc.c2 - p.cp : pc.p1 + pc.c2);
        }
        {
            u32 cnt = 0;
            asm volatile(
                "v_mov_b32 v116, 0\n v_mov_b32 v117, 0\n v_mov_b32 v118, 0\n v_mov_b32 v119, 0\n"
                "s_barrier\n"
                "1:\n"
                "buffer_load_dwordx4 v[108:111], %[gl], %[rs], %[x0] offen sc1\n"
                "buffer_load_dwordx4 v[112:115], %[gl], %[rs], %[x1] offen sc1\n"
                "s_mov_b64 exec, %[ev]\n"
                "buffer_load_dwordx4 v[116:119], %[pml], %[rs], %[xm] offen sc1\n"
                "s_mov_b64 exec, -1\n"
                POLL_COUNT
                "v_mov_b32 v120, 0\n v_mov_b32 v121, 0\n v_mov_b32 v122, 0\n v_mov_b32 v123, 0\n"
                "s_nop 1\n"
                "s_waitcnt vmcnt(2)\n"
                "v_smfmac_f32_16x16x64_bf16 v[120:123], v[108:111], %[zb0], %[si]\n"
                "v_max3_u32 v124, v108, v109, v110\n"
                "s_waitcnt vmcnt(1)\n"
                "v_smfmac_f32_16x16x64_bf16 v[120:123], v[112:115], %[zb1], %[si]\n"
                "v_max3_u32 v124, v124, v111, v112\n"
                "v_max3_u32 v124, v124, v113, v114\n"
                "s_waitcnt vmcnt(0)\n"
                "v_mfma_f32_16x16x32_bf16 v[120:123], v[116:119], %[zm], v[120:123]\n"
                "v_max3_u32 v124, v124, v115, v116\n"
                "v_max3_u32 v124, v124, v117, v118\n"
                "v_max_u32 v124, v124, v119\n"
                "v_cmp_lt_u32 vcc, %[key], v124\n"
                "s_nop 2\n"
                "s_cbranch_vccnz 1b\n"
                "v_add_f32 v124, v120, v121\n"
                "v_add_f32 v125, v122, v123\n"
                "ds_write2_b32 %[rw], v124, v125 offset1:80\n"
                "s_waitcnt lgkmcnt(0)\n"
                "s_barrier\n"
                : [cnt] "+s"(cnt), [key] "+s"(key)
                : [gl] "v"(gl), [pml] "v"(pml), [rs] "s"(cx.ar), [x0] "s"(xx0), [x1] "s"(xx1), [xm] "s"(xm), [ev] "s"(0x5555555555555555ull), [cap] "s"(POLLCAP),
                  [zb0] "v"(zb0), [zb1] "v"(zb1), [zm] "v"(zm), [si] "v"(sidx), [rw] "v"(redw)
                : "memory", "scc", "vcc", "v108", "v109", "v110", "v111", "v112", "v113", "v114", "v115", "v116", "v117", "v118", "v119", "v120", "v121", "v122", "v123", "v124", "v125");
            dsp = cnt - 1;
        }
        if (wave == 7) {
            const unsigned char* rb = lds + redr;
            f32x4 r0 = *(const f32x4*)(rb), r1 = *(const f32x4*)(rb + 16), r2 = *(const f32x4*)(rb + 32), r3 = *(const f32x4*)(rb + 48);
            r0 = (r0 + r1) + (r2 + r3);
            if (lane < 32) st32(pk2s(r0.x + r0.z, r0.y + r0.w), cx.ar, lane * 4, xpub);
        }
        DT(d_pub);
        DV(0, d_b1); DV(1, d_zp); DV(2, d_pub); DV(7, d_pub); DV(3, d_pub); DV(4, late); DV(5, dsp);
        if (wave == 1) pace_next(pc, read_ref(tv, cx, adts, xts), s == 0, p);
    }
    if (key == SENT) __hip_atomic_store(cx.ctl + CW_TMO, 1u, RLX_AGENT);
    VMCNT(0);
    BAR();
    if (wave == 6 && lane == 0) __hip_atomic_store(cx.ctl + CW_FIN + 16 * (32 + c8), 1u, RLX_AGENT);
    DFLUSH();
}

constexpr int L_XT = L_STAGE, L_VI = 0;
template <int TYPE>
DEV void l_worker(const Params& p, const Ctx& cx, unsigned char* lds, int h, int second, int wave) {
    constexpr int K1 = TYPE == 0 ? 3 : 10;
    v4u w4[40];
    if (TYPE == 0) load_units<1>(p, h, lds, wave, lane_id(), [&](int slot, v2u (&t)[4]) {
        const int ks = slot >> 1, tau = slot & 1;
        w4[(2 * ks) * 2 + tau] = (v4u){t[0].x, t[0].y, t[1].x, t[1].y}; w4[(2 * ks + 1) * 2 + tau] = (v4u){t[2].x, t[2].y, t[3].x, t[3].y}; });
    else load_units<2>(p, h, lds, wave, lane_id(), [&](int slot, v2u (&t)[4]) {
        w4[2 * slot] = (v4u){t[0].x, t[0].y, t[1].x, t[1].y}; w4[2 * slot + 1] = (v4u){t[2].x, t[2].y, t[3].x, t[3].y}; });
    EVT(1);
    const int lane = lane_id();
    const u32 lds0 = (u32)(size_t)(LAS unsigned char*)lds;
    const int i = lane & 15, gq = lane >> 4;
    const rsrc_t rxin = mkrs(p.x, S * DM * 2);
    const int xrow = (wave & 3) * 8 + (lane >> 3);
    const int xvo = xrow * (DM * 2) + (((lane & 7) ^ ((xrow >> 1) & 7)) * 16) + (5 * (wave >> 2)) * 128;
    const u32 xld = lds0 + L_XT + (5 * (wave >> 2)) * 4096 + (wave & 3) * 1024;
    auto issue1 = [&](int s, int buf, int k) { dma16(rfl(xld + buf * 40960 + k * 4096), xvo, rxin, rfl(min(s, NSEG - 1) * (32 * DM * 2) + k * 128)); };
    const int nit = second ? K1 : NSEG - K1;
    auto seg_of = [&](int it) { return second ? (it < K1 ? 2 * it + 1 : NSEG) : it + min(it, K1); };
#pragma unroll
    for (int k = 0; k < 5; ++k) issue1(seg_of(0), 0, k);
#pragma unroll
    for (int k = 0; k < 5; ++k) issue1(seg_of(1), 1, k);
    const int sel = cx.par;
    const u32 xb0 = lds0 + L_XT + i * 128 + ((gq ^ (i >> 1)) * 16), xb1 = lds0 + L_XT + i * 128 + (((4 + gq) ^ (i >> 1)) * 16);
    int ost0, ost1, ubase;
    if (TYPE == 0) {
        const int hp = wave & 3;
        if (wave < 4) { ost0 = (i >> 3) * 2048 + (hp >> 1) * 1024 + (16 * (2 * (hp & 1) + (gq >> 1)) + 2 * (i & 7) + (gq & 1)) * 16; ost1 = ost0 + 4096; ubase = QX_L + h * 8192; }
        else { const int ip = i >> 1; ost0 = (i & 1) * 4096 + hp * 1024 + (16 * gq + 4 * (ip & 1) + ((ip >> 1) & 3)) * 16; ost1 = ost0 + 128; ubase = KX_L + h * 8192; }
    } else { ost0 = (i >> 3) * 16384 + (16 * gq + (i & 7)) * 16; ost1 = 0; ubase = ZX_P + wave * 32768 + (h >> 2) * 2048 + (h & 1) * 1024 + ((h >> 1) & 1) * 128; }
    const int viw = L_VI + i * 256 + (((4 * (wave >> 1) + gq) ^ i) * 16) + (wave & 1) * 8;
    const int vtok = 8 * (i >> 2) + (i & 3);
    auto signal = [&](int it) { if (wave == 0 && lane == 0) __hip_atomic_store(cx.ctl + CW_P + 16 * (64 * TYPE + 2 * h + second), (u32)seg_of(it + 1), RLX_AGENT); };
    auto phase1 = [&](int it, int buf) {
        const int bn = buf == 0 ? 2 : buf - 1, sn = seg_of(it + 2);
        const u32 x0 = xb0 + buf * 40960, x1 = xb1 + buf * 40960;
        f32x4 d00 = {0, 0, 0, 0}, d01 = d00, d10 = d00, d11 = d00;
        asm volatile("" : "+v"(d00), "+v"(d01), "+v"(d10), "+v"(d11));
        v4u b[8];
#define XRD(slot, ss) do { LDS_RD128(b[2 * (slot)], ((ss) & 1) ? x1 : x0, ((ss) >> 1) * 4096); LDS_RD128(b[2 * (slot) + 1], ((ss) & 1) ? x1 : x0, ((ss) >> 1) * 4096 + 2048); } while (0)
#pragma unroll
        for (int ss = 0; ss < 4; ++ss) XRD(ss, ss);
#pragma unroll
        for (int ss = 0; ss < 20; ++ss) {
            const int sl = ss & 3;
            if (ss < 17) LGKM_WAIT2(6, b[2 * sl], b[2 * sl + 1]); else LGKM_WAIT2(2 * (19 - ss), b[2 * sl], b[2 * sl + 1]);
            if (TYPE == 0) { MF_WA(d00, 2 * ss, b[2 * sl]); MF_WA(d01, 2 * ss, b[2 * sl + 1]); MF_WA(d10, 2 * ss + 1, b[2 * sl]); MF_WA(d11, 2 * ss + 1, b[2 * sl + 1]); }
            else { MF_WA(d00, ss, b[2 * sl]); MF_WA(d01, ss, b[2 * sl + 1]); }
            if (ss + 4 < 20) XRD(sl, ss + 4);
            if (TYPE == 0 && ss == 8) { VMCNT(0); RAWBAR(); if (it > 0) signal(it - 1); }
            if (TYPE == 0 && ss >= 9 && ss < 19 && (ss & 1)) issue1(sn, bn, (ss - 9) >> 1);
            if (TYPE == 1 && ss % 4 == 1) issue1(sn, bn, ss >> 2);
        }
#undef XRD
        asm volatile("s_nop 7" : "+v"(d00), "+v"(d01), "+v"(d10), "+v"(d11));
        if (TYPE == 0) {
            const int so = rfl(amap(ubase + seg_of(it) * (H * 8192), sel));
            u32 a0 = pk2(d00.x, d00.y), a1 = pk2(d00.z, d00.w), a2 = pk2(d10.x, d10.y), a3 = pk2(d10.z, d10.w);
            u32 c0 = pk2(d01.x, d01.y), c1 = pk2(d01.z, d01.w), c2 = pk2(d11.x, d11.y), c3 = pk2(d11.z, d11.w);
            if (wave < 4) {
                auto sa = __builtin_amdgcn_permlane16_swap(a0, a1, false, false), sb = __builtin_amdgcn_permlane16_swap(a2, a3, false, false);
                auto sc = __builtin_amdgcn_permlane16_swap(c0, c1, false, false), sd = __builtin_amdgcn_permlane16_swap(c2, c3, false, false);
                st128((v4u){sa[0], sb[0], sa[1], sb[1]}, cx.ar, ost0, so);
                st128((v4u){sc[0], sd[0], sc[1], sd[1]}, cx.ar, ost1, so);
            } else {
                st128((v4u){a0, a1, a2, a3}, cx.ar, ost0, so);
                st128((v4u){c0, c1, c2, c3}, cx.ar, ost1, so);
            }
        } else {
            *(v2u*)(lds + viw + (it & 1) * 8192) = (v2u){pk2(d00.x, d00.y), pk2(d00.z, d00.w)};
            *(v2u*)(lds + viw + (it & 1) * 8192 + 4096) = (v2u){pk2(d01.x, d01.y), pk2(d01.z, d01.w)};
        }
    };
    auto phase2 = [&](int it, bool mid) {
        const int ub = ubase + seg_of(it) * ZX_STEP;
        v4u a[8];
#pragma unroll
        for (int k = 0; k < 8; ++k) { const int tok = vtok + 4 * (k >> 2); a[k] = *(const v4u*)(lds + L_VI + (it & 1) * 8192 + tok * 256 + (((4 * (k & 3) + gq) ^ (tok & 15)) * 16)); }
        f32x4 d0[5], d1[5];
#pragma unroll
        for (int cg = 0; cg < 5; ++cg) { d0[cg] = (f32x4){0, 0, 0, 0}; d1[cg] = d0[cg]; asm volatile("" : "+v"(d0[cg]), "+v"(d1[cg])); }
        asm volatile("s_nop 1");
#pragma unroll
        for (int cg = 0; cg < 6; ++cg) {
            if (cg < 5) {
#pragma unroll
                for (int ss = 0; ss < 4; ++ss) { MF_WB(d0[cg], a[ss], 20 + cg * 4 + ss); MF_WB(d1[cg], a[4 + ss], 20 + cg * 4 + ss); }
            } else asm volatile("s_nop 7");
            if (cg == 0 && mid) { VMCNT(5); BAR(); if (it > 0) signal(it - 1); }
            if (cg > 0) {
                asm volatile("" : "+v"(d0[cg - 1]), "+v"(d1[cg - 1]));
                st128((v4u){pk2(d0[cg - 1].x, d0[cg - 1].y), pk2(d0[cg - 1].z, d0[cg - 1].w), pk2(d1[cg - 1].x, d1[cg - 1].y), pk2(d1[cg - 1].z, d1[cg - 1].w)},
                      cx.ar, ost0, ub + (cg - 1) * (8 * 32768));
            }
        }
    };
    VMCNT(0);
    BAR();
    if (TYPE == 0) {
        int buf = 0;
        for (int it = 0; it < nit; ++it) {
            phase1(it, buf);
            buf = buf == 2 ? 0 : buf + 1;
            if (it == 0) { VMCNT(0); BAR(); signal(0); EVT(2); }
        }
    } else {
        phase1(0, 0);
        BAR();
        phase2(0, false);
        VMCNT(0); BAR(); signal(0);
        EVT(2);
        phase1(1, 1);
        VMCNT(5); BAR();
        int buf = 2;
        for (int k = 1; k < nit; ++k) {
            phase1(k + 1, buf);
            phase2(k, true);
            buf = buf == 2 ? 0 : buf + 1;
        }
    }
    VMCNT(0);
    BAR();
    signal(nit - 1);
}

constexpr int SS_K = 33 * 144, SS_U = 4288, SS_CH = 2 * SS_K + 2 * SS_U + 32;
static_assert(SS_CH % 16 == 0 && 8 * SS_CH <= L_TOTAL, "ssm lds");
DEV void ssm_task(const Params& p, const Ctx& cx, unsigned char* lds, int task, int wave, int far) {
    const int lane = lane_id(), tid = wave * 64 + lane;
    const int d = task * 8 + wave;
    unsigned char* ch = lds + wave * SS_CH;
    unsigned char* tmpb = ch + 2 * SS_K;
    const int i = lane & 15, gq = lane >> 4;
    v4u xr[2][3];
    {
        const float dt = rbf(__expf(bf2f(p.logdt[d])));
#pragma unroll
        for (int k = 0; k < 2; ++k) {
            const int nn = lane + 64 * k, o = d * NS + nn;
            const float br = dt * bf2f(p.Bre[o]), bi = dt * bf2f(p.Bim[o]), cr = bf2f(p.Cre[o]), ci = bf2f(p.Cim[o]);
            *(f32x4*)(tmpb + nn * 16) = (f32x4){dt * bf2f(p.Are[o]) * 1.4426950408889634f, dt * bf2f(p.Aim[o]) * 0.15915494309189535f, cr * br - ci * bi, cr * bi + ci * br};
        }
        asm volatile("s_waitcnt lgkmcnt(0)" ::: "memory");
        {
            const rsrc_t rxin = mkrs(p.x, S * DM * 2);
#pragma unroll
            for (int k = 0; k < 2; ++k)
#pragma unroll
                for (int j = 0; j < 3; ++j) xr[k][j] = __builtin_amdgcn_raw_buffer_load_b128(rxin, ((2 * (tid + 512 * k) + j) * DM + task * 8) * 2, 0, 0);
            __builtin_amdgcn_sched_barrier(0);
        }
        const float lu = 16.f * i;
        const float lv0 = (float)i;
        const float lv1 = i < 8 ? 256.f + 2.f * i : 512.f + 256.f * ((i - 8) >> 2) + 4.f * (i & 3);
        const float lv2 = i < 8 ? 1024.f + 256.f * (i >> 1) + 8.f * (i & 1) : 2048.f;
        f32x4 k0 = {0, 0, 0, 0}, k1 = k0, k2 = k0;
#pragma unroll 2
        for (int it = 0; it < 32; ++it) {
            const f32x4 q = *(const f32x4*)(tmpb + (4 * it + gq) * 16);
            auto cexp = [&](float l, float& re, float& im) {
                const float e = __builtin_amdgcn_exp2f(q.x * l), th = __builtin_amdgcn_fractf(q.y * l);
                re = e * __builtin_amdgcn_cosf(th); im = e * __builtin_amdgcn_sinf(th);
            };
            float er, ei, r0, i0, r1, i1, r2, i2;
            cexp(lu, er, ei); cexp(lv0, r0, i0); cexp(lv1, r1, i1); cexp(lv2, r2, i2);
            const float ur = q.z * er - q.w * ei, nui = -(q.z * ei + q.w * er);
            k0 = __builtin_amdgcn_mfma_f32_16x16x4f32(ur, r0, k0, 0, 0, 0); k1 = __builtin_amdgcn_mfma_f32_16x16x4f32(ur, r1, k1, 0, 0, 0); k2 = __builtin_amdgcn_mfma_f32_16x16x4f32(ur, r2, k2, 0, 0, 0);
            k0 = __builtin_amdgcn_mfma_f32_16x16x4f32(nui, i0, k0, 0, 0, 0); k1 = __builtin_amdgcn_mfma_f32_16x16x4f32(nui, i1, k1, 0, 0, 0); k2 = __builtin_amdgcn_mfma_f32_16x16x4f32(nui, i2, k2, 0, 0, 0);
        }
        float* tab = (float*)(tmpb + 2048);
        *(f32x4*)(tab + i * 20 + 4 * gq) = k0; *(f32x4*)(tab + (16 + i) * 20 + 4 * gq) = k1; *(f32x4*)(tab + (32 + i) * 20 + 4 * gq) = k2;
        asm volatile("s_waitcnt lgkmcnt(0)" ::: "memory");
        if (lane < 36) { *(u32*)(ch + lane * 4) = 0; *(u32*)(ch + SS_K + lane * 4) = 0; }
#pragma unroll 2
        for (int it = 0; it < 16; ++it) {
            const int l0 = it * 128 + lane * 2;
            float kv[2];
#pragma unroll
            for (int e = 0; e < 2; ++e) {
                const int lr = (int)rbf((float)(l0 + e));
                const int a = (lr >> 4) & 15, j = lr & 15, hi = lr >> 8;
                int col;
                if (hi == 0) col = j; else if (hi == 1) col = 16 + (j >> 1); else if (hi < 4) col = 24 + 4 * (hi - 2) + (j >> 2); else if (hi < 8) col = 32 + 2 * (hi - 4) + (j >> 3); else col = 40;
                kv[e] = tab[col * 20 + a];
            }
            const float h0 = rbf(kv[0]), h1 = rbf(kv[1]);
            const int o = ((l0 >> 6) + 1) * 144 + ((l0 & 56) + 6 - (l0 & 6)) * 2;
            *(u32*)(ch + o) = pk2(h1, h0);
            *(u32*)(ch + SS_K + o) = pk2(kv[1] - h1, kv[0] - h0);
        }
    }
    EVT(8);
    BAR();
    {
        if (lane < 24) { *(u32*)(ch + 2 * SS_K + lane * 4) = 0; if (lane < 23) *(u32*)(ch + 2 * SS_K + SS_U + lane * 4) = 0; }
#pragma unroll
        for (int k = 0; k < 2; ++k) {
            const int l = 2 * (tid + 512 * k);
#pragma unroll
            for (int c = 0; c < 8; ++c) {
                const u32 sel = (c & 1) ? 0x07060302u : 0x05040100u;
                unsigned char* ub = lds + c * SS_CH + 2 * SS_K;
                *(u32*)(ub + (l + 48) * 2) = __builtin_amdgcn_perm(xr[k][1][c >> 1], xr[k][0][c >> 1], sel);
                *(u32*)(ub + SS_U + (l + 48) * 2) = __builtin_amdgcn_perm(xr[k][2][c >> 1], xr[k][1][c >> 1], sel);
                if (k == 0 && tid == 0) *(u32*)(ub + SS_U + 46 * 2) = __builtin_amdgcn_perm(xr[0][0][c >> 1], 0u, sel);
            }
        }
    }
    BAR();
    EVT(9);
    f32x4 acc[2][4];
#pragma unroll
    for (int I = 0; I < 2; ++I)
#pragma unroll
        for (int rt = 0; rt < 4; ++rt) acc[I][rt] = (f32x4){0, 0, 0, 0};
    {
        const int st0 = i - 8 * gq - 7 + 48;
        const unsigned char* ua = ch + 2 * SS_K + ((st0 & 1) ? SS_U + (st0 - 1) * 2 : st0 * 2);
        auto afrag = [&](int o) { const u32* q = (const u32*)(ua + o * 32); return (v4u){q[0], q[1], q[2], q[3]}; };
        v4u am2 = {0, 0, 0, 0};
        v4u am1 = afrag(-1);
        const int kb = gq * 16;
        auto step = [&](int t, int I, const v4u& a0, const v4u& a1, const v4u& a2, const v4u& a3) {
            const int blk = max(16 * I + i - t + 1, 0);
            const unsigned char* kp = ch + blk * 144 + kb;
#pragma unroll
            for (int hl = 0; hl < 2; ++hl) {
                const v4u b0 = *(const v4u*)(kp + hl * SS_K), b1 = *(const v4u*)(kp + hl * SS_K + 64);
                acc[I][0] = mfma16(a0, b0, acc[I][0]); acc[I][1] = mfma16(a1, b0, acc[I][1]); acc[I][2] = mfma16(a2, b0, acc[I][2]); acc[I][3] = mfma16(a3, b0, acc[I][3]);
                acc[I][0] = mfma16(am2, b1, acc[I][0]); acc[I][1] = mfma16(am1, b1, acc[I][1]); acc[I][2] = mfma16(a0, b1, acc[I][2]); acc[I][3] = mfma16(a1, b1, acc[I][3]);
            }
        };
#pragma unroll 1
        for (int t = 0; t < 16; ++t) {
            const v4u a0 = afrag(4 * t), a1 = afrag(4 * t + 1), a2 = afrag(4 * t + 2), a3 = afrag(4 * t + 3);
            step(t, 0, a0, a1, a2, a3); step(t, 1, a0, a1, a2, a3);
            am2 = a2; am1 = a3;
        }
#pragma unroll 1
        for (int t = 16; t < 32; ++t) {
            const v4u a0 = afrag(4 * t), a1 = afrag(4 * t + 1), a2 = afrag(4 * t + 2), a3 = afrag(4 * t + 3);
            step(t, 1, a0, a1, a2, a3);
            am2 = a2; am1 = a3;
        }
    }
    EVT(10);
    {
        const float dsk = bf2f(p.dskip[d]);
        asm volatile("s_waitcnt lgkmcnt(0)" ::: "memory");
#pragma unroll
        for (int I = 0; I < 2; ++I)
#pragma unroll
            for (int rt = 0; rt < 4; ++rt) {
                const int l = 64 * (16 * I + i) + 16 * rt + 4 * gq;
                const v2u uu = *(const v2u*)(ch + 2 * SS_K + (l + 48) * 2);
                const f32x4 y = acc[I][rt];
                const float b0 = rbf(y.x) + rbf(bf2f(uu.x & 0xffffu) * dsk), b1 = rbf(y.y) + rbf(bfhi(uu.x) * dsk), b2 = rbf(y.z) + rbf(bf2f(uu.y & 0xffffu) * dsk), b3 = rbf(y.w) + rbf(bfhi(uu.y) * dsk);
                *(v2u*)(ch + (16 * I + i) * 136 + (16 * rt + 4 * gq) * 2) = (v2u){pk2(b0, b1), pk2(b2, b3)};
            }
    }
    BAR();
#pragma unroll
    for (int k = 0; k < 2; ++k) {
        const int l = 2 * (tid + 512 * k);
        u32 e[8];
#pragma unroll
        for (int c = 0; c < 8; ++c) e[c] = *(const u32*)(lds + c * SS_CH + (l >> 6) * 136 + (l & 63) * 2);
        v4u lo, hi;
#pragma unroll
        for (int c = 0; c < 4; ++c) { lo[c] = __builtin_amdgcn_perm(e[2 * c + 1], e[2 * c], 0x05040100u); hi[c] = __builtin_amdgcn_perm(e[2 * c + 1], e[2 * c], 0x07060302u); }
        const int so = rfl(amap(BS_L + task * 32768 + k * 16384 + wave * 2048, far));
        st128(lo, cx.ar, lane * 32, so); st128(hi, cx.ar, lane * 32 + 16, so);
    }
    VMCNT(0);
    BAR();
    if (tid == 0) __hip_atomic_fetch_add(cx.ctl + CW_SSM + 64 * (task >> 4), 1u, RLX_AGENT);
}

constexpr int L_OP = 0, L_OA = 81920, L_OG = L_OA + 32 * 272;
DEV void out_task(const Params& p, const Ctx& cx, unsigned char* lds, int s, int cgp, int wave, int& ready) {
    const int lane = lane_id(), tid = wave * 64 + lane, far = cx.par ^ 1;
    const int n = lane & 15, gq = lane >> 4, gl = lane * 16;
    SIDX_DECL;
    const int ct = 8 * cgp + wave;
    const int zxl = (n >> 3) * 16384 + (16 * gq + (n & 7)) * 16, zml = (n >> 3) * 4096 + gq * 128 + (n & 7) * 16;
    if (wave == 0 && s >= ready) ready = wait_upto<1>(prog_load<1>(0, lane, cx.ctl), s, 0, lane, cx.ctl);
    BAR();
    v8u wr[20];
#pragma unroll
    for (int k = 0; k < 16; ++k) {
        const int so = ZX_P + s * ZX_STEP + ct * 32768 + (k >> 1) * 2048;
        wr[k] = cat8(ld128(cx.ar, zxl + (k & 1) * 128, so), ld128(cx.ar, zxl + (k & 1) * 128 + 1024, so));
    }
    const int otok = tid >> 4, oc = tid & 15;
    const v4u bv = ld128(cx.ar, amap(BS_L + (16 * cgp + oc) * 32768 + (s * 32 + otok) * 16, far), 0);
    {
        const rsrc_t rxin = mkrs(p.x, S * DM * 2), rg = mkrs(p.gw, DM * 2);
        const int tok = wave * 4 + gq;
        float sum = 0.f;
#pragma unroll
        for (int k = 0; k < 5; ++k) {
            const v4u xv = __builtin_amdgcn_raw_buffer_load_b128(rxin, ((s * 32 + tok) * DM + k * 128 + n * 8) * 2, 0, 0);
            const v4u gv = __builtin_amdgcn_raw_buffer_load_b128(rg, (k * 128 + n * 8) * 2, 0, 0);
#pragma unroll
            for (int e = 0; e < 4; ++e) sum += rbf(bf2f(xv[e] & 0xffffu) * bf2f(gv[e] & 0xffffu)) + rbf(bfhi(xv[e]) * bfhi(gv[e]));
        }
        sum = rbf(rowsum16(sum));
        const float g = rbf(1.f / (1.f + __expf(-sum)));
        if (n == 0) *(float*)(lds + L_OG + tok * 4) = g;
    }
    if (wave == 0) {
        if (s < NSEG - 1) wait_ge(cx.ctl + CW_S + 16 * s, 1u, cx.ctl);
        else {
            u32 spins = 0;
            for (;;) {
                const u32 a = __hip_atomic_load(cx.ctl + CW_FIN + 16 * lane, RLX_AGENT), c = __hip_atomic_load(cx.ctl + CW_FIN + 16 * (lane < 48 ? lane + 64 : lane), RLX_AGENT);
                if (__all(a && c)) break;
                __builtin_amdgcn_s_sleep(2);
                if (spin_fail(spins, cx.ctl)) break;
            }
        }
    }
    BAR();
    {
        v4u t[10];
#pragma unroll
        for (int k = 0; k < 10; ++k) t[k] = ld128(cx.ar, gl, rfl(amap(PT_L + s * PT_STEP + (wave * 10 + k) * 1024, far)));
        const int so = rfl(amap(ZM_L + s * ZM_STEP + ct * 8192, cx.par));
#pragma unroll
        for (int k = 0; k < 4; ++k) wr[16 + k] = cat8(ld128(cx.ar, zml + k * 1024, so), ld128(cx.ar, zml + k * 1024 + 512, so));
#pragma unroll
        for (int k = 0; k < 10; ++k) *(v4u*)(lds + L_OP + (wave * 10 + k) * 1024 + gl) = t[k];
    }
    BAR();
    f32x4 acc[4];
#pragma unroll
    for (int g = 0; g < 4; ++g) acc[g] = (f32x4){0, 0, 0, 0};
    asm volatile("s_nop 1" : "+v"(acc[0]), "+v"(acc[1]), "+v"(acc[2]), "+v"(acc[3]));
#pragma unroll
    for (int ks = 0; ks < 20; ++ks)
#pragma unroll
        for (int g = 0; g < 4; ++g) {
            const v4u a = *(const v4u*)(lds + L_OP + (ks < 16 ? g * 16 + ks : 64 + g * 4 + ks - 16) * 1024 + gl);
            SMF_W(acc[g], a, ks);
        }
    asm volatile("s_nop 7" : "+v"(acc[0]), "+v"(acc[1]), "+v"(acc[2]), "+v"(acc[3]));
#pragma unroll
    for (int g = 0; g < 4; ++g) {
        const u32 v = pk2(acc[g].x + acc[g].y, acc[g].z + acc[g].w);
        unsigned char* o = lds + L_OA + (8 * g + 2 * gq) * 272 + (wave * 16 + n) * 2;
        *(u16*)o = (u16)v; *(u16*)(o + 272) = (u16)(v >> 16);
    }
    BAR();
    {
        const v4u av = *(const v4u*)(lds + L_OA + otok * 272 + oc * 16);
        const float g = *(const float*)(lds + L_OG + otok * 4), og = rbf(1.f - g);
        v4u o;
#pragma unroll
        for (int e = 0; e < 4; ++e)
            o[e] = pk2(rbf(g * bf2f(av[e] & 0xffffu)) + rbf(og * bf2f(bv[e] & 0xffffu)), rbf(g * bfhi(av[e])) + rbf(og * bfhi(bv[e])));
        *(v4u*)(p.out + (s * 32 + otok) * DM + cgp * 128 + oc * 8) = o;
    }
}

DEV unsigned char* arena_base(unsigned char* ws) { return (unsigned char*)(((size_t)ws + CTRL_BYTES + (2u << 20) - 1) & ~(size_t)((2u << 20) - 1)); }

__global__ void __launch_bounds__(512) gm_init(unsigned char* ws) {
    const int tid = threadIdx.x, b = blockIdx.x;
    const rsrc_t ar = mkrs(arena_base(ws), ARENA_BYTES);
    if (b == 0) {
        if (tid >= 64) return;
        u32 mn[2] = {100000u, 100000u};
        for (int it = 0; it < 3; ++it)
#pragma unroll
            for (int q = 0; q < 2; ++q) {
                const u32 c0 = now32();
                st32(0u, ar, tid * 4, (4 << 20) + it * 256 + q * 8192);
                VMCNT(0);
                mn[q] = min(mn[q], now32() - c0);
            }
        if (tid < 32) ((u32*)ws)[CW_PAR + tid] = mn[1] < mn[0];
    } else if (b == 1) {
        for (int i = tid; i < CTRL_BYTES / 16; i += 512) if (i < CW_PAR / 4 || i >= CW_PAR / 4 + 8) ((v4u*)ws)[i] = (v4u){0, 0, 0, 0};
    } else {
        const v4u sv = {SENT, SENT, SENT, SENT};
#pragma unroll
        for (int k = 0; k < 4; ++k) st128(sv, ar, (b - 2) * 32768 + k * 8192 + tid * 16, 0);
    }
}

__global__ void __launch_bounds__(512, 2) gm_kernel(Params p) {
    extern __shared__ __attribute__((aligned(16))) unsigned char lds[];
    const int b = blockIdx.x;
    const int wave = rfl(threadIdx.x >> 6);
    Ctx cx;
    cx.ctl = (GAS u32*)p.ws;
    cx.ar = mkrs(arena_base(p.ws), ARENA_BYTES);
    cx.par = rfl(cx.ctl[CW_PAR]) & 1;
    EVT(0);
    if ((b & 7) >= 4) {
        const int u = (b >> 3) * 4 + (b & 7) - 4, j = u >> 2;
        const int h = 8 * (u & 3) + (j >> 2), type = (j >> 1) & 1, second = j & 1;
        if (type) l_worker<1>(p, cx, lds, h, second, wave); else l_worker<0>(p, cx, lds, h, second, wave);
        EVT(3);
        if (!second) return;
        ssm_task(p, cx, lds, type * 32 + h, wave, cx.par ^ 1);
        EVT(4);
        const int nb = type ? 12 : 10, gi = h / nb;
        if (gi >= (type ? 2 : 3)) return;
        const int cgp = type ? 2 + gi : (gi == 2 ? 4 : gi);
        if (wave == 0) wait_ge(cx.ctl + CW_SSM + 64 * cgp, 16u, cx.ctl);
        int ready = 0;
#pragma unroll 1
        for (int s = h % nb; s < NSEG; s += nb) out_task(p, cx, lds, s, cgp, wave, ready);
        EVT(5);
        return;
    }
    const int w = (b >> 3) * 4 + (b & 7);
    if (w < 32) {
        if (wave < 2) a_role<0>(p, cx, lds, w, wave);
        else if (wave < 6) a_role<1>(p, cx, lds, w, wave);
        else a_role<2>(p, cx, lds, w, wave);
    } else {
        vo_task(p, cx, lds, w - 32, wave);
        EVT(1);
        if (w < 112) r_role(p, cx, lds, w - 32, wave);
        else ssm_task(p, cx, lds, w - 48, wave, cx.par ^ 1);
    }
    EVT(5);
}

static const int g_dA = 4300, g_dB = 2650, g_dR = 1200, g_dZ = 4;
static const int g_dbg = 0;

extern "C" void kernel_launch(void* const* d_in, const int* in_sizes, int n_in,
                              void* d_out, int out_size, void* d_ws, size_t ws_size,
                              hipStream_t stream) {
    Params p;
    p.x = (const u16*)d_in[0]; p.mem0 = (const u16*)d_in[1]; p.Wq = (const u16*)d_in[2]; p.Wk = (const u16*)d_in[3]; p.Wv = (const u16*)d_in[4]; p.Wo = (const u16*)d_in[5];
    p.Are = (const u16*)d_in[8]; p.Aim = (const u16*)d_in[9]; p.Bre = (const u16*)d_in[10]; p.Bim = (const u16*)d_in[11]; p.Cre = (const u16*)d_in[12]; p.Cim = (const u16*)d_in[13];
    p.logdt = (const u16*)d_in[14]; p.dskip = (const u16*)d_in[15]; p.gw = (const u16*)d_in[16];
    p.out = (u16*)d_out; p.ws = (unsigned char*)d_ws;
    p.per = g_dA; p.c2 = g_dB; p.gmax = g_dR; p.maxrel = g_dZ; p.cp = 950; p.cmL = g_dbg ? g_dbg : 500;
    gm_init<<<130, 512, 0, stream>>>((unsigned char*)d_ws);
    (void)hipFuncSetAttribute((const void*)gm_kernel, hipFuncAttributeMaxDynamicSharedMemorySize, L_TOTAL);
    gm_kernel<<<256, 512, L_TOTAL, stream>>>(p);
}
```

```cpp
#include <hip/hip_runtime.h>
#include <hip/hip_bf16.h>
#include <stdint.h>

typedef unsigned short u16;
typedef unsigned int u32;
typedef u32 v4u __attribute__((ext_vector_type(4)));
typedef u32 v2u __attribute__((ext_vector_type(2)));
typedef u32 v8u __attribute__((ext_vector_type(8)));
typedef float f32x4 __attribute__((ext_vector_type(4)));
typedef __bf16 bf16x8 __attribute__((ext_vector_type(8)));
typedef __bf16 bf16x2 __attribute__((ext_vector_type(2)));
typedef short s16x4 __attribute__((ext_vector_type(4)));
typedef __amdgpu_buffer_rsrc_t rsrc_t;
#define GAS __attribute__((address_space(1)))
#define LAS __attribute__((address_space(3)))
#define RLX_AGENT __ATOMIC_RELAXED, __HIP_MEMORY_SCOPE_AGENT
#define DEV __device__ __forceinline__

constexpr int S = 2048, DM = 640, H = 32, DH = 128, NSEG = 64, HD = H * DH, NS = 128;
constexpr int SC1 = 16;
constexpr u32 SENT = 0xFFFFFFFFu;

DEV float bf2f(u32 b) { return __uint_as_float(b << 16); }
DEV float bfhi(u32 b) { return __uint_as_float(b & 0xFFFF0000u); }
DEV float rbf(float f) { return (float)(__bf16)f; }
DEV u32 pk2(float lo, float hi) { bf16x2 v = {(__bf16)lo, (__bf16)hi}; return __builtin_bit_cast(u32, v); }
DEV u32 pk2s(float lo, float hi) { return min(pk2(lo, hi), 0xFFFFFFFEu); }
DEV int rfl(int v) { return __builtin_amdgcn_readfirstlane(v); }
DEV int lane_id() { int l; asm volatile("v_mbcnt_lo_u32_b32 %0, -1, 0\n\tv_mbcnt_hi_u32_b32 %0, -1, %0" : "=v"(l)); return l; }

constexpr int CTRL_BYTES = 21504;
constexpr int ARENA_BYTES = 220 << 20;
constexpr int SLOT = 32768, MEM_O = 0, PX_O = 10240, PM_O = PX_O + 16384, TS_O = PM_O + 4096;
constexpr int QX_L = 3 << 20, KX_L = 19 << 20, ZM_L = 35 << 20, ZM_STEP = 80 * 4096;
constexpr int PT_L = 3 << 20, PT_STEP = 81920, BS_L = 8 << 20;
constexpr int VO_P = 112 << 20, ZX_P = 140 << 20, ZX_STEP = 80 * 16384;
constexpr int CW_TMO = 0, CW_PAR = 64, CW_SSM = 128  , CW_S = 512, CW_FIN = 1536, CW_P = 3328;

DEV int amap(int o, int sel) { return ((o >> 13) << 14) | (o & 8191) | ((((o >> 20) ^ sel) & 1) << 13); }
DEV u32 max4(v4u v) { return max(max(v.x, v.y), max(v.z, v.w)); }
DEV v8u cat8(v4u a, v4u b) { return __builtin_shufflevector(a, b, 0, 1, 2, 3, 4, 5, 6, 7); }
DEV rsrc_t mkrs(const void* p, int bytes) { return __builtin_amdgcn_make_buffer_rsrc((void*)p, 0, bytes, 0x00020000); }
DEV v4u ld128(rsrc_t r, int off, int so) { return __builtin_amdgcn_raw_buffer_load_b128(r, off, so, SC1); }
DEV u32 ld32(rsrc_t r, int off, int so) { return __builtin_amdgcn_raw_buffer_load_b32(r, off, so, SC1); }
DEV void st128(v4u v, rsrc_t r, int off, int so) { __builtin_amdgcn_raw_buffer_store_b128(v, r, off, so, SC1); }
DEV void st32(u32 v, rsrc_t r, int off, int so) { __builtin_amdgcn_raw_buffer_store_b32(v, r, off, so, SC1); }
#define VMCNT(n) asm volatile("s_waitcnt vmcnt(%0)" :: "i"(n) : "memory")
#define BAR() do { asm volatile("s_waitcnt lgkmcnt(0)" ::: "memory"); __builtin_amdgcn_s_barrier(); asm volatile("" ::: "memory"); } while (0)
template <int CTRL> DEV float dppf(float v) { return __builtin_bit_cast(float, __builtin_amdgcn_update_dpp(0, __builtin_bit_cast(int, v), CTRL, 0xf, 0xf, true)); }
DEV float rowsum16(float v) { v += dppf<0x128>(v); v += dppf<0x124>(v); v += dppf<0x122>(v); v += dppf<0x121>(v); return v; }
DEV void dma16(u32 ldsaddr, int voff, rsrc_t rs, int soff) {
    u32 keep;
    asm volatile("s_nop 4\n\ts_mov_b32 %0, m0\n\ts_mov_b32 m0, %1\n\ts_nop 0\n\tbuffer_load_dwordx4 %2, %3, %4 offen lds\n\ts_mov_b32 m0, %0"
                 : "=&s"(keep) : "s"(ldsaddr), "v"(voff), "s"(rs), "s"(soff) : "memory");
}

constexpr u32 SPINCAP = 1u << 20;
#define EVT(i)

#define DV(slot, v)
#define DFLUSH()
#define DT(name)
struct Params {
    const u16 *x, *mem0, *Wq, *Wk, *Wv, *Wo, *Are, *Aim, *Bre, *Bim, *Cre, *Cim, *logdt, *dskip, *gw;
    u16* out;
    unsigned char* ws;
    int per, c2, gmax, cp, cmL, maxrel, glate, pad;
};
struct Ctx { GAS u32* ctl; rsrc_t ar; int par; };

constexpr int L_MEMF = 0, L_QF = 10240, L_KMF = 12288, L_KXF = 16384  , L_RED = 10240, L_ZT = 16384, L_STAGE = 24576, L_DUMP = 32768, L_TOTAL = L_STAGE + 131072;

DEV bool spin_fail(u32& spins, GAS u32* ctl) {
    if (++spins > SPINCAP) { __hip_atomic_store(ctl + CW_TMO, 1u, RLX_AGENT); return true; }
    return (spins & 1023u) == 0 && rfl(__hip_atomic_load(ctl + CW_TMO, RLX_AGENT)) != 0;
}
DEV u32 wait_ge(GAS u32* w, u32 want, GAS u32* ctl) {
    u32 spins = 0, v;
    for (;;) { v = rfl(__hip_atomic_load(w, RLX_AGENT)); if (v >= want) break; __builtin_amdgcn_s_sleep(8); if (spin_fail(spins, ctl)) break; }
    asm volatile("" ::: "memory");
    return v;
}
template <int MODE> DEV int prog_load(int h, int lane, GAS u32* ctl) { return (int)__hip_atomic_load(ctl + CW_P + 16 * (MODE == 0 ? 2 * h + (lane & 1) : 64 + lane), RLX_AGENT); }
template <int MODE>
DEV int wait_upto(int v, int need, int h, int lane, GAS u32* ctl) {
    u32 spins = 0; int r;
    for (;;) {
        v = max(v, lane & 1);
        v = min(v, __builtin_amdgcn_update_dpp(64, v, 0x128, 0xf, 0xf, false)); v = min(v, __builtin_amdgcn_update_dpp(64, v, 0x124, 0xf, 0xf, false));
        v = min(v, __builtin_amdgcn_update_dpp(64, v, 0x122, 0xf, 0xf, false)); v = min(v, __builtin_amdgcn_update_dpp(64, v, 0x121, 0xf, 0xf, false));
        r = min(min(__builtin_amdgcn_readlane(v, 0), __builtin_amdgcn_readlane(v, 16)), min(__builtin_amdgcn_readlane(v, 32), __builtin_amdgcn_readlane(v, 48)));
        if (r > need) break;
        __builtin_amdgcn_s_sleep(8);
        if (spin_fail(spins, ctl)) { r = 64; break; }
        v = prog_load<MODE>(h, lane, ctl);
    }
    asm volatile("" ::: "memory");
    return r;
}
DEV u32 now32() { unsigned long long t; asm volatile("s_memtime %0\n s_waitcnt lgkmcnt(0)" : "=s"(t) :: "memory"); return (u32)t; }
DEV u32 gnow() { unsigned long long t; asm volatile("s_memrealtime %0\n s_waitcnt lgkmcnt(0)" : "=s"(t) :: "memory"); return (u32)t * 24u + 48u; }
DEV u32 wait_global(u32 T) {
    u32 off, e, d;
    asm volatile(
        "s_memrealtime vcc\n s_memtime s[92:93]\n s_waitcnt lgkmcnt(0)\n"
        "s_mul_i32 %0, vcc_lo, 24\n s_sub_u32 %2, %3, %0\n s_sub_u32 %0, %0, s92\n s_cmp_gt_i32 %2, 150\n s_cbranch_scc0 4f\n"
        "1:\n"
        "s_memrealtime vcc\n s_memtime s[92:93]\n s_waitcnt lgkmcnt(0)\n"
        "s_mul_i32 %1, vcc_lo, 24\n s_sub_u32 %1, %1, s92\n"
        "s_sub_u32 %2, %1, %0\n s_cmp_gt_i32 %2, 0\n s_cselect_b32 %0, %1, %0\n"
        "s_sub_u32 %2, %3, s92\n s_sub_u32 %2, %2, %0\n"
        "s_cmp_gt_i32 %2, 700\n s_cbranch_scc0 2f\n s_cmp_gt_i32 %2, 0x40000\n s_cbranch_scc1 4f\n s_sleep 2\n s_branch 1b\n"
        "2:\n s_cmp_gt_i32 %2, 230\n s_cbranch_scc1 1b\n"
        "3:\n s_sub_u32 %2, %2, 32\n s_cmp_gt_i32 %2, 120\n s_cbranch_scc1 3b\n"
        "4:\n"
        : "=&s"(off), "=&s"(e), "=&s"(d) : "s"(T) : "memory", "scc", "vcc", "s92", "s93");
    return off;
}
#define RAWBAR() do { __builtin_amdgcn_s_barrier(); asm volatile("" ::: "memory"); } while (0)
DEV u32 read_ref(u32 v, const Ctx& cx, int ad, int so) {
    u32 spins = 0;
    while (__builtin_expect(__any(v == SENT), 0)) {
        if (spin_fail(spins, cx.ctl)) break;
        __builtin_amdgcn_s_sleep(2);
        asm volatile("" ::: "memory");
        v = ld32(cx.ar, ad, so);
    }
    const u32 b = rfl(v);
    int d = (int)(v - b);
    d = max(d, __builtin_amdgcn_update_dpp(0, d, 0x128, 0xf, 0xf, true)); d = max(d, __builtin_amdgcn_update_dpp(0, d, 0x124, 0xf, 0xf, true));
    d = max(d, __builtin_amdgcn_update_dpp(0, d, 0x122, 0xf, 0xf, true)); d = max(d, __builtin_amdgcn_update_dpp(0, d, 0x121, 0xf, 0xf, true));
    return b + (u32)max(__builtin_amdgcn_readlane(d, 0), __builtin_amdgcn_readlane(d, 16));
}

struct Pace { u32 p1; int c2, per, relax, fails; bool sched; };
DEV void pace_next(Pace& q, u32 tref, int s, const Params& p) {
    const int g = (int)(tref - q.p1);
    const bool late = s == 0 || g > p.gmax;
    if (q.sched && s > 6) {
        q.fails = late ? q.fails + 2 : max(q.fails - 1, 0);
        if (q.fails >= 3 && q.relax < p.maxrel) { if (g > p.gmax + 650) q.c2 += 48; q.per += 56; ++q.relax; q.fails = 0; }
    }
    q.p1 = late ? tref + q.per - p.cmL : q.p1 + q.per;
    q.sched = !late;
}

#define SMF_A(acc, a, w) asm volatile("v_smfmac_f32_16x16x64_bf16 %0, %1, %2, %3" : "+v"(acc) : "v"(a), "a"(w), "v"(sidx))
#define SMF_V(acc, a, w) asm volatile("v_smfmac_f32_16x16x64_bf16 %0, %1, %2, %3" : "+v"(acc) : "v"(a), "v"(w), "v"(sidx))
#define SMF_W(acc, a, i) do { if ((i) < 16) SMF_A(acc, a, wr[i]); else SMF_V(acc, a, wr[i]); } while (0)
#define MF_WA(acc, i, b) do { if ((i) < 32) asm volatile("v_mfma_f32_16x16x32_bf16 %0, %1, %2, %0" : "+v"(acc) : "a"(w4[i]), "v"(b)); \
                              else asm volatile("v_mfma_f32_16x16x32_bf16 %0, %1, %2, %0" : "+v"(acc) : "v"(w4[i]), "v"(b)); } while (0)
#define MF_WB(acc, a, i) do { if ((i) < 32) asm volatile("v_mfma_f32_16x16x32_bf16 %0, %1, %2, %0" : "+v"(acc) : "v"(a), "a"(w4[i])); \
                              else asm volatile("v_mfma_f32_16x16x32_bf16 %0, %1, %2, %0" : "+v"(acc) : "v"(a), "v"(w4[i])); } while (0)
#define LDS_RD128(dst, addr, off) asm volatile("ds_read_b128 %0, %1 offset:%2" : "=v"(dst) : "v"(addr), "i"(off) : "memory")
#define LGKM_WAIT(n, x) asm volatile("s_waitcnt lgkmcnt(%1)" : "+v"(x) : "i"(n) : "memory")
#define LGKM_WAIT2(n, x, y) asm volatile("s_waitcnt lgkmcnt(%2)" : "+v"(x), "+v"(y) : "i"(n) : "memory")
#define SIDX_DECL int sidx = (lane & 1) ? 0xEEEE : 0x4444; asm volatile("s_nop 1" : "+v"(sidx))
DEV float dmax16(float v) {
    asm volatile("s_nop 1\n v_max_f32_dpp %0, %0, %0 row_ror:8 row_mask:0xf bank_mask:0xf\n s_nop 1\n v_max_f32_dpp %0, %0, %0 row_ror:4 row_mask:0xf bank_mask:0xf\n"
                 "s_nop 1\n v_max_f32_dpp %0, %0, %0 row_ror:2 row_mask:0xf bank_mask:0xf\n s_nop 1\n v_max_f32_dpp %0, %0, %0 row_ror:1 row_mask:0xf bank_mask:0xf" : "+v"(v));
    return v;
}
DEV float max3f(float a, float b, float c) { float r; asm("v_max3_f32 %0, %1, %2, %3" : "=v"(r) : "v"(a), "v"(b), "v"(c)); return r; }
DEV f32x4 mfma16(v4u a, v4u b, f32x4 c) { return __builtin_amdgcn_mfma_f32_16x16x32_bf16(__builtin_bit_cast(bf16x8, a), __builtin_bit_cast(bf16x8, b), c, 0, 0, 0); }

template <int MODE, class Sink>
DEV void load_units(const Params& p, int h, unsigned char* lds, int wave, int lane, Sink sink) {
    constexpr int NU = MODE == 3 ? 10 : 20, NR = (NU + 3) / 4;
    const u32 lds0 = (u32)(size_t)(LAS unsigned char*)lds;
    const int tp = lane >> 3, q4 = (lane >> 1) & 3, hf = lane & 1;
    auto issue = [&](int r) {
        const int u = r * 4 + (wave >> 1), hh = wave & 1;
        if (u >= NU) return;
        bool second; int base, stride, map;
        if (MODE <= 1) { second = u & 1; base = (64 * (u >> 1) * HD + h * DH) * 2; stride = HD * 2; map = MODE; }
        else if (MODE == 2 && u < 10) { second = false; base = (64 * u * HD + h * DH) * 2; stride = HD * 2; map = 1; }
        else { const int uo = MODE == 2 ? u - 10 : u; second = true; base = ((h * DH + 64 * (uo & 1)) * DM + (uo >> 1) * 128) * 2; stride = DM * 2; map = MODE == 2 ? 2 : 1; }
        const int gq = map == 0 ? 8 * (q4 >> 1) + (q4 & 1) : q4;
        const rsrc_t rs = mkrs(second ? (MODE <= 1 ? p.Wk : p.Wo) : (MODE <= 1 ? p.Wq : p.Wv), DM * HD * 2);
#pragma unroll
        for (int i = 0; i < 8; ++i) {
            const int gB = i >> 1, i4 = i & 1;
            const int f = 32 * hh + (map == 0 ? 16 * (gB >> 1) + 4 * (gB & 1) + 2 * i4 : map == 1 ? 8 * gB + 4 * i4 : 16 * i4 + 4 * gB);
            const int voff = gq * stride + ((tp ^ (gB & 1)) * 16 + hf * 8) * 2;
            const u32 la = rfl(lds0 + L_STAGE + (r & 1) * 65536 + (wave >> 1) * 16384 + (hh * 8 + i) * 1024);
            dma16(la, voff, rs, rfl(base + f * stride));
        }
    };
    const int gB = lane >> 4;
    const int rd0 = (2 * gB) * 1024 + ((lane & 15) >> 2) * 32 + (lane & 3) * 8;
    issue(0);
#pragma unroll
    for (int r = 0; r < NR; ++r) {
        if (r + 1 < NR) { issue(r + 1); if ((r + 1) * 4 + 3 < NU) VMCNT(8); else VMCNT(0); } else if (MODE != 3) VMCNT(0);
        BAR();
#pragma unroll
        for (int k = 0; k < 4; ++k) {
            int uu, tl;
            if (MODE == 1) { uu = (k >> 1) * 2 + (wave >> 2); tl = 2 * (wave & 3) + (k & 1); } else { uu = k; tl = wave; }
            if (MODE != 1 && r * 4 + k >= NU) continue;
            v2u t[4];
#pragma unroll
            for (int j = 0; j < 4; ++j) {
                s16x4 v = __builtin_amdgcn_ds_read_tr16_b64_v4i16((LAS s16x4*)(lds + L_STAGE + (r & 1) * 65536 + uu * 16384 + ((j >> 1) * 8 + (j & 1)) * 1024 + rd0 + (tl ^ (gB & 1)) * 128));
                t[j] = __builtin_bit_cast(v2u, v);
            }
            sink(r * 4 + k, t);
        }
        BAR();
    }
}

constexpr u32 POLLCAP = 1u << 17;
#define POLL_COUNT "s_add_u32 %[cnt], %[cnt], 1\n s_cmp_lt_u32 %[cnt], %[cap]\n s_cselect_b32 %[key], %[key], -1\n"
DEV void gather_mem(const Ctx& cx, int gl, int s0, int s1, u32 ldst, u32& key) {
    u32 cnt = 0;
    asm volatile(
        "s_barrier\n"
        "1:\n"
        "buffer_load_dwordx4 v[116:119], %[gl], %[rs], %[s0] offen sc1\n"
        "buffer_load_dwordx4 v[120:123], %[gl], %[rs], %[s1] offen sc1\n"
        POLL_COUNT
        "s_waitcnt vmcnt(1)\n"
        "ds_write_b128 %[ld], v[116:119]\n"
        "v_max3_u32 v124, v116, v117, v118\n"
        "s_waitcnt vmcnt(0)\n"
        "ds_write_b128 %[ld], v[120:123] offset:1024\n"
        "v_max3_u32 v124, v124, v119, v120\n"
        "v_max3_u32 v124, v124, v121, v122\n"
        "v_max_u32 v124, v124, v123\n"
        "v_cmp_lt_u32 vcc, %[key], v124\n"
        "s_waitcnt lgkmcnt(0)\n"
        "s_cbranch_vccnz 1b\n"
        "s_barrier\n"
        : [cnt] "+s"(cnt), [key] "+s"(key)
        : [gl] "v"(gl), [rs] "s"(cx.ar), [s0] "s"(s0), [s1] "s"(s1), [ld] "v"(ldst), [cap] "s"(POLLCAP)
        : "memory", "scc", "vcc", "v116", "v117", "v118", "v119", "v120", "v121", "v122", "v123", "v124");
}
DEV void gather_mem4(const Ctx& cx, int gl, int s0, int s1, int s2, unsigned long long half, u32 ldst, u32 ldst2, u32& key) {
    u32 cnt = 0;
    asm volatile(
        "v_mov_b32 v112, 0\n v_mov_b32 v113, 0\n v_mov_b32 v114, 0\n v_mov_b32 v115, 0\n"
        "s_barrier\n"
        "1:\n"
        "buffer_load_dwordx4 v[116:119], %[gl], %[rs], %[s0] offen sc1\n"
        "buffer_load_dwordx4 v[120:123], %[gl], %[rs], %[s1] offen sc1\n"
        "s_mov_b64 exec, %[hf]\n"
        "buffer_load_dwordx4 v[112:115], %[gl], %[rs], %[s2] offen sc1\n"
        "s_mov_b64 exec, -1\n"
        POLL_COUNT
        "s_waitcnt vmcnt(2)\n"
        "ds_write_b128 %[ld], v[116:119]\n"
        "v_max3_u32 v124, v116, v117, v118\n"
        "s_waitcnt vmcnt(1)\n"
        "ds_write_b128 %[ld], v[120:123] offset:1024\n"
        "v_max3_u32 v124, v124, v119, v120\n"
        "v_max3_u32 v124, v124, v121, v122\n"
        "s_waitcnt vmcnt(0)\n"
        "ds_write_b128 %[ld2], v[112:115]\n"
        "v_max3_u32 v124, v124, v123, v112\n"
        "v_max3_u32 v124, v124, v113, v114\n"
        "v_max_u32 v124, v124, v115\n"
        "v_cmp_lt_u32 vcc, %[key], v124\n"
        "s_waitcnt lgkmcnt(0)\n"
        "s_cbranch_vccnz 1b\n"
        "s_barrier\n"
        : [cnt] "+s"(cnt), [key] "+s"(key)
        : [gl] "v"(gl), [rs] "s"(cx.ar), [s0] "s"(s0), [s1] "s"(s1), [s2] "s"(s2), [hf] "s"(half), [ld] "v"(ldst), [ld2] "v"(ldst2), [cap] "s"(POLLCAP)
        : "memory", "scc", "vcc", "v112", "v113", "v114", "v115", "v116", "v117", "v118", "v119", "v120", "v121", "v122", "v123", "v124");
}
DEV void load_mem0(const u16* mem0, unsigned char* lds, int tid) {
    for (int i = tid; i < 640; i += 512) { const int m = i / 80, c8 = i % 80; *(v4u*)(lds + L_MEMF + (c8 >> 1) * 256 + (2 * m + (c8 & 1)) * 16) = *(const v4u*)(mem0 + m * DM + c8 * 8); }
}

template <int CLS>
DEV void a_role(const Params& p, const Ctx& cx, unsigned char* lds, int h, int wave) {
    v8u wr[20];
    load_units<0>(p, h, lds, wave, lane_id(), [&](int slot, v2u (&t)[4]) { wr[slot] = (v8u){t[0].x, t[0].y, t[1].x, t[1].y, t[2].x, t[2].y, t[3].x, t[3].y}; });
    load_mem0(p.mem0, lds, wave * 64 + lane_id());
    for (int i = wave * 64 + lane_id(); i < 1024; i += 512) *(u32*)(lds + L_KMF + i * 4) = 0;
    const int par = cx.par, far = par ^ 1;
    EVT(2);
    const int lane = lane_id(), n = lane & 15, gq = lane >> 4;
    SIDX_DECL;
    const u32 lbase = (u32)(size_t)(LAS unsigned char*)lds + lane * 16;
    const int gl = lane * 16;
    const int qw = L_QF + (wave >> 2) * 1024 + (2 * ((wave >> 1) & 1) + (n >> 3)) * 256 + (2 * gq) * 32 + ((n >> 1) & 1) * 16 + (4 * ((n >> 2) & 1) + 2 * (wave & 1) + (n & 1)) * 2;
    const int kw = L_KMF + (wave >> 1) * 1024 + (16 * (n >> 2) + gq) * 16 + (4 * (wave & 1) + (n & 3)) * 2;
    const int pair = 8 * (n >> 3) + 2 * (n & 3) + ((n >> 2) & 1);
    const int tk0 = 2 * pair, tk1 = 2 * pair + 1;
    const u32 lq = (u32)(size_t)(LAS unsigned char*)lds + gq * 256 + (4 * (wave & 1) + (n >> 2)) * 32 + (n & 1) * 16;
    const int adpx = (n >> 3) * 256 + gq * 32 + (n & 7) * 4, upx = PX_O + h * 512 + (wave & 1) * 128;
    const int adpm = gq * 16 + (n & 3) * 4, upm = PM_O + h * 128 + (wave & 1) * 64;
    const int g8 = (wave - 2) & 3;
    const int t0 = 8 * g8 + gq;
    const int ql = gq * 256 + (((n >> 1) & 1) * 4 + (n >> 2)) * 32 + (n & 1) * 16;
    const int adtx = (n >> 3) * 256 + gq * 32 + (n & 7) * 4, utx = (g8 * 32 + h) * 512;
    const int adtm = gq * 16 + (n & 1) * 8 + ((n >> 1) & 1) * 4, utm = 65536 + (g8 * 32 + h) * 128;
    const float SC = 0.08838834764831845f * 1.4426950408889634f;
    const float NEGR = -1.0e9f / 0.08838834764831845f;
    const float pad0 = (n < 8) ? 0.f : -__builtin_inff();
    const bool pmlane = n < 4;
    const int adts = (lane & 31) * 64;
    int ready = 0;
    v4u qx0, qx1, qn0, qn1;
    f32x4 s1, s2;
    auto tok_load = [&](int sn) {
        const int qb = rfl(amap(QX_L + (sn * H + h) * 8192 + g8 * 2048, par));
        qn0 = ld128(cx.ar, ql, qb); qn1 = ld128(cx.ar, ql + 1024, qb);
    };
    auto tok_prep = [&](int sn) {
        qx0 = qn0; qx1 = qn1;
        v4u kx[8];
#pragma unroll
        for (int i = 0; i < 8; ++i) kx[i] = *(const v4u*)(lds + L_KXF + (sn & 1) * 8192 + i * 1024 + lane * 16);
        s1 = (f32x4){0, 0, 0, 0}; s2 = s1;
        asm volatile("s_nop 1" : "+v"(s1), "+v"(s2), "+v"(qx0), "+v"(qx1));
        SMF_V(s1, qx0, cat8(kx[0], kx[1]));
        SMF_V(s2, qx0, cat8(kx[4], kx[5]));
        SMF_V(s1, qx1, cat8(kx[2], kx[3]));
        SMF_V(s2, qx1, cat8(kx[6], kx[7]));
        asm volatile("s_nop 7" : "+v"(s1), "+v"(s2));
        s1.x = (tk0 <= t0) ? s1.x + s1.y : NEGR; s1.z = (tk0 <= t0 + 4) ? s1.z + s1.w : NEGR;
        s1.y = (tk1 <= t0) ? s2.x + s2.y : NEGR; s1.w = (tk1 <= t0 + 4) ? s2.z + s2.w : NEGR;
    };
    v4u kt[8], k1 = {0, 0, 0, 0};
    auto help_load = [&](int sn) {
        const int kb = rfl(amap(KX_L + (sn * H + h) * 8192, par));
#pragma unroll
        for (int i = 0; i < 8; ++i) kt[i] = ld128(cx.ar, gl + (i & 3) * 1024, kb + (i >> 2) * 4096);
    };
    auto help_store = [&](int sn) {
#pragma unroll
        for (int i = 0; i < 8; ++i) *(v4u*)(lds + L_KXF + (sn & 1) * 8192 + i * 1024 + lane * 16) = kt[i];
    };
    if (CLS == 2) {
        ready = wait_upto<0>(prog_load<0>(h, lane, cx.ctl), wave - 6, h, lane, cx.ctl);
        help_load(wave - 6); help_store(wave - 6);
    }
    BAR();
    if (CLS == 1) { tok_load(0); tok_prep(0); }
    Pace pc = {0u, p.c2, p.per, 0, 0, false};
    u32 off = gnow() - now32(), key = 0xFFFFFFFEu;
    const int gw = wave < 2 ? wave : wave - 4;
    const u32 gdst = (u32)(size_t)(LAS unsigned char*)lds + L_MEMF + gw * 2048 + gq * 256 + (n & 7) * 32 + (n >> 3) * 16;
    const u32 gdst2 = ((lane >> 5) == (gw & 1)) ? gdst - gw * 2048 + 8192 + (gw >> 1) * 1024 : (u32)(size_t)(LAS unsigned char*)lds + L_DUMP + lane * 16;
    for (int s = 0; s < NSEG; ++s) {
        const int sb = s * SLOT;
        f32x4 aq = {0, 0, 0, 0}, ak = {0, 0, 0, 0};
        asm volatile("" : "+v"(aq), "+v"(ak));
        const int xpx = rfl(amap(sb + upx, par)), xpm = rfl(amap(sb + upm, par)), xts = rfl(amap(sb + TS_O, par));
        if (s > 0) {
            const int x0 = rfl(amap(sb + MEM_O + gw * 2048, par)), x2 = rfl(amap(sb + MEM_O + 8192 + (gw >> 1) * 1024, par));
            asm volatile("s_waitcnt lgkmcnt(0)" :: "s"(x0), "s"(x2), "s"(xpx), "s"(xpm), "s"(xts) : "memory");
            DT(d_arr); DV(7, d_arr);
            asm volatile("s_waitcnt lgkmcnt(0)" ::: "memory");
            if (CLS == 2 && wave == 7) off = wait_global(pc.p1);
            if (CLS != 1) gather_mem4(cx, gl, x0, x0 + 1024, x2, (gw & 1) ? 0xFFFFFFFF00000000ull : 0xFFFFFFFFull, gdst, gdst2, key);
            else { RAWBAR(); RAWBAR(); }
        } else BAR();
        if (CLS == 2 && wave == 7) { const u32 ts = min(now32() + off, 0xFFFFFFFEu); if (lane == 0) st32(ts, cx.ar, h * 64, xts); }
        {
            v4u a_[5];
#pragma unroll
            for (int ks = 0; ks < 5; ++ks) LDS_RD128(a_[ks], lbase, L_MEMF + ks * 1024);
            if (CLS == 1) tok_load(min(s + 1, NSEG - 1));
            if (s > 0) k1 = ld128(cx.ar, gl, rfl(amap(KX_L + (min(s + 2, NSEG - 1) * H + h) * 8192 + wave * 1024, par)));
#pragma unroll
            for (int ks = 0; ks < 5; ++ks) { LGKM_WAIT(4, a_[ks]); SMF_W(aq, a_[ks], 2 * ks); SMF_W(ak, a_[ks], 2 * ks + 1); LDS_RD128(a_[ks], lbase, L_MEMF + (ks + 5) * 1024); }
#pragma unroll
            for (int ks = 5; ks < 10; ++ks) { LGKM_WAIT(9 - ks, a_[ks - 5]); SMF_W(aq, a_[ks - 5], 2 * ks); SMF_W(ak, a_[ks - 5], 2 * ks + 1); }
        }
        v4u kx[8];
        if (CLS == 0) {
            const u32 lk = lbase + (s & 1) * 8192;
#pragma unroll
            for (int i = 0; i < 8; ++i) LDS_RD128(kx[i], lk, L_KXF + i * 1024);
        } else asm volatile("s_nop 7");
        asm volatile("" : "+v"(aq), "+v"(ak));
        {
            const u32 q2 = pk2(aq.x + aq.y, aq.z + aq.w), k2 = pk2(ak.x + ak.y, ak.z + ak.w);
            *(u16*)(lds + qw) = (u16)q2; *(u16*)(lds + qw + 32) = (u16)(q2 >> 16);
            *(u16*)(lds + kw) = (u16)k2; *(u16*)(lds + kw + 64) = (u16)(k2 >> 16);
        }
        f32x4 s0 = {pad0, 0, pad0, 0};
        if (CLS == 0) { s1 = (f32x4){0, 0, 0, 0}; s2 = s1; asm volatile("" : "+v"(s0), "+v"(s1), "+v"(s2) : "s"(xpx), "s"(xpm)); }
        if (CLS == 1) asm volatile("" : "+v"(s0));
        BAR();
        if (CLS == 0) {
            v4u a0, a1, km[4];
            LDS_RD128(a0, lq, L_QF); LDS_RD128(a1, lq, L_QF + 1024);
#pragma unroll
            for (int i = 0; i < 4; ++i) LDS_RD128(km[i], lbase, L_KMF + i * 1024);
            LGKM_WAIT(5, a0);
            SMF_V(s1, a0, cat8(kx[0], kx[1]));
            SMF_V(s2, a0, cat8(kx[4], kx[5]));
            LGKM_WAIT(4, a1);
            SMF_V(s1, a1, cat8(kx[2], kx[3]));
            SMF_V(s2, a1, cat8(kx[6], kx[7]));
            LGKM_WAIT2(2, km[0], km[1]);
            SMF_V(s0, a0, cat8(km[0], km[1]));
            LGKM_WAIT2(0, km[2], km[3]);
            SMF_V(s0, a1, cat8(km[2], km[3]));
            asm volatile("s_nop 4" : "+v"(s1), "+v"(s2));
            float x1 = s1.x + s1.y, x2 = s2.x + s2.y;
            asm volatile("s_nop 0" : "+v"(s0), "+v"(x1), "+v"(x2));
            const float x0 = s0.x + s0.y;
            const float mx = -SC * dmax16(max3f(x0, x1, x2));
            const float e0 = __builtin_amdgcn_exp2f(__builtin_fmaf(x0, SC, mx)), e1 = __builtin_amdgcn_exp2f(__builtin_fmaf(x1, SC, mx)), e2 = __builtin_amdgcn_exp2f(__builtin_fmaf(x2, SC, mx));
            const float inv = __builtin_amdgcn_rcpf(rowsum16(e0 + e1 + e2));
            const float p0 = e0 * inv;
            st32(pk2s(e1 * inv, e2 * inv), cx.ar, adpx, xpx);
            const u32 pm = pk2s(p0, dppf<0x104>(p0));
            if (pmlane) st32(pm, cx.ar, adpm, xpm);
            DT(d_pub); DV(3, d_pub);
        } else if (CLS == 1) {
            __builtin_amdgcn_s_sleep(4);
            v4u km[4];
#pragma unroll
            for (int i = 0; i < 4; ++i) km[i] = *(const v4u*)(lds + L_KMF + i * 1024 + lane * 16);
            SMF_V(s0, qx0, cat8(km[0], km[1]));
            SMF_V(s0, qx1, cat8(km[2], km[3]));
            asm volatile("s_nop 7" : "+v"(s0));
            const int xtx = rfl(amap(PT_L + s * PT_STEP + utx, far)), xtm = rfl(amap(PT_L + s * PT_STEP + utm, far));
#pragma unroll
            for (int rr = 0; rr < 2; ++rr) {
                const float x0 = rr ? s0.z + s0.w : s0.x + s0.y, x1 = rr ? s1.z : s1.x, x2 = rr ? s1.w : s1.y;
                const float mx = -SC * dmax16(max3f(x0, x1, x2));
                const float e0 = __builtin_amdgcn_exp2f(__builtin_fmaf(x0, SC, mx)), e1 = __builtin_amdgcn_exp2f(__builtin_fmaf(x1, SC, mx)), e2 = __builtin_amdgcn_exp2f(__builtin_fmaf(x2, SC, mx));
                const float inv = __builtin_amdgcn_rcpf(rowsum16(e0 + e1 + e2));
                const float p0 = e0 * inv;
                st32(pk2(e1 * inv, e2 * inv), cx.ar, adtx + rr * 128, xtx);
                const u32 pm = pk2(p0, dppf<0x104>(p0));
                if (pmlane) st32(pm, cx.ar, adtm + rr * 64, xtm);
            }
            if (s + 1 < NSEG) tok_prep(s + 1);
        } else {
            if (wave == 6) {
                if (s == 0) { ready = wait_upto<0>(prog_load<0>(h, lane, cx.ctl), 2, h, lane, cx.ctl); help_load(2); help_store(2); }
                if (s + 3 >= ready && ready < NSEG) ready = wait_upto<0>(prog_load<0>(h, lane, cx.ctl), min(s + 3, NSEG - 1), h, lane, cx.ctl);
            } else __builtin_amdgcn_s_sleep(6);
        }
        if (s > 0) *(v4u*)(lds + L_KXF + (s & 1) * 8192 + wave * 1024 + lane * 16) = k1;
        if (CLS == 2 && wave == 7) {
            pace_next(pc, read_ref(ld32(cx.ar, adts, xts), cx, adts, xts), s, p);
        }
    }
    if (key == SENT) __hip_atomic_store(cx.ctl + CW_TMO, 1u, RLX_AGENT);
    VMCNT(0);
    BAR();
    if (CLS == 2 && wave == 7 && lane == 0) __hip_atomic_store(cx.ctl + CW_FIN + 16 * h, 1u, RLX_AGENT);
    DFLUSH();
}

DEV void r_role(const Params& p, const Ctx& cx, unsigned char* lds, int c8, int wave) {
    const int lane = lane_id(), n = lane & 15, gq = lane >> 4;
    const int par = cx.par;
    for (int k = 0; k < p.pad; ++k) __builtin_amdgcn_s_sleep(16);
    v8u wr[20];
    {
        const int vb = (((n >> 3) * 20) * 40) * 1024 + (16 * gq + (n & 7)) * 16;
        const int vs = VO_P + ((4 * wave * 20) * 40 + (c8 >> 1)) * 1024 + (c8 & 1) * 128;
#pragma unroll
        for (int i = 0; i < 20; ++i) {
            const int o = vs + (i & 1) * (2 * 20 * 40960) + (i >> 1) * (2 * 40960);
            wr[i] = cat8(__builtin_amdgcn_raw_buffer_load_b128(cx.ar, vb, o, 0), __builtin_amdgcn_raw_buffer_load_b128(cx.ar, vb, o + 40960, 0));
        }
    }
    load_mem0(p.mem0, lds, wave * 64 + lane);
    EVT(3);
    SIDX_DECL;
    const u32 lbase = (u32)(size_t)(LAS unsigned char*)lds + lane * 16;
    const int gl = lane * 16;
    const int zl = gq * 128 + (n & 7) * 16;
    const u32 ztw = (u32)(size_t)(LAS unsigned char*)lds + L_ZT + wave * 512 + (n >> 3) * 128 + (n & 7) * 16 + gq * 4, ztr = (u32)(size_t)(LAS unsigned char*)lds + L_ZT + wave * 512 + zl;
    const int pml = gq * 128 + (n >> 1) * 16;
    const u32 redw = (u32)(size_t)(LAS unsigned char*)lds + L_RED + (n >> 3) * 2560 + ((2 * gq) * 4 + ((n & 7) >> 1)) * 80 + wave * 8 + (n & 1) * 4;
    const u32 gdst = (u32)(size_t)(LAS unsigned char*)lds + L_MEMF + wave * 2048 + gq * 256 + (n & 7) * 32 + (n >> 3) * 16;
    u32 key = 0xFFFFFFFEu;
    const int redr = L_RED + (lane & 31) * 80;
    const int adts = (lane & 31) * 64;
    int ready = 0;
    if (wave == 6) ready = wait_upto<1>(prog_load<1>(0, lane, cx.ctl), 1, 0, lane, cx.ctl);
    BAR();
    v4u zn0 = ld128(cx.ar, gl, ZX_P + c8 * 16384 + wave * 2048), zn1 = ld128(cx.ar, gl + 1024, ZX_P + c8 * 16384 + wave * 2048);
    Pace pc = {0u, p.c2, p.per, 0, 0, false};
    for (int s = 0; s < NSEG; ++s) {
        const int sb = s * SLOT;
        f32x4 z0 = {0, 0, 0, 0}, z1 = z0;
        asm volatile("" : "+v"(z0), "+v"(z1));
        const int x0 = rfl(amap(sb + MEM_O + wave * 2048, par)), x1 = rfl(amap(sb + MEM_O + wave * 2048 + 1024, par));
        const int xzx = ZX_P + min(s + 1, NSEG - 1) * ZX_STEP + c8 * 16384 + wave * 2048;
        v8u zb0 = cat8(zn0, zn1), zb1;
#pragma unroll
        for (int i = 0; i < 8; ++i) zb1[i] = __builtin_amdgcn_update_dpp(0, zb0[i], 0x108, 0xf, 0xf, true);
        asm volatile("" : "+v"(zb0), "+v"(zb1) : "s"(x0), "s"(x1), "s"(xzx));
        if (wave == 1 && s > 0) wait_global(pc.p1);
        int pv = 64;
        if (s > 0 && wave < 5) gather_mem(cx, gl, x0, x1, gdst, key);
        else {
            BAR();
            if (wave == 6 && s + 2 >= ready) { __builtin_amdgcn_s_sleep(4); pv = prog_load<1>(0, lane, cx.ctl); }
            RAWBAR();
        }
        const int xts = rfl(amap(sb + TS_O, par));
        u32 tv = 0;
        bool late = false;
        DT(d_b1);
        {
            v4u a_[5];
#pragma unroll
            for (int ks = 0; ks < 5; ++ks) LDS_RD128(a_[ks], lbase, L_MEMF + ks * 1024);
            zn0 = ld128(cx.ar, gl, xzx); zn1 = ld128(cx.ar, gl + 1024, xzx);
#pragma unroll
            for (int ks = 0; ks < 5; ++ks) { LGKM_WAIT(4, a_[ks]); SMF_W(z0, a_[ks], 2 * ks); SMF_W(z1, a_[ks], 2 * ks + 1); LDS_RD128(a_[ks], lbase, L_MEMF + (ks + 5) * 1024); }
#pragma unroll
            for (int ks = 5; ks < 10; ++ks) { LGKM_WAIT(9 - ks, a_[ks - 5]); SMF_W(z0, a_[ks - 5], 2 * ks); SMF_W(z1, a_[ks - 5], 2 * ks + 1); }
        }
        const int xzm = rfl(amap(ZM_L + s * ZM_STEP + c8 * 4096 + wave * 512, par));
        const int xpub = rfl(amap(sb + SLOT + MEM_O + c8 * 128, par));
        const int xx0 = rfl(amap(sb + PX_O + wave * 2048, par)), xx1 = rfl(amap(sb + PX_O + wave * 2048 + 1024, par)), xm = rfl(amap(sb + PM_O + wave * 512, par));
        asm volatile("s_nop 7" : "+v"(z0), "+v"(z1) : "s"(xx0), "s"(xx1), "s"(xm), "s"(xts), "s"(xpub), "s"(xzm));
        const u32 zt0 = pk2(z0.x + z0.y, z0.z + z0.w), zt1 = pk2(z1.x + z1.y, z1.z + z1.w);
        if (s + 1 == NSEG) {
            *(LAS u32*)(size_t)ztw = zt0; *(LAS u32*)(size_t)(ztw + 256) = zt1;
            asm volatile("s_waitcnt lgkmcnt(0)" ::: "memory");
            if (n < 8) st128(*(const LAS v4u*)(size_t)ztr, cx.ar, zl, xzm);
            break;
        }
        DT(d_zp);
        u32 dsp = 0;
        DV(6, pc.p1);
        DT(d_arr); DV(7, d_arr);
        if (wave == 1) {
            late = s == 0 || (int)(gnow() - pc.p1) > p.glate;
            tv = ld32(cx.ar, adts, xts);
            wait_global(late ? read_ref(tv, cx, adts, xts) + pc.c2 - p.cp : pc.p1 + pc.c2);
        }
        {
            u32 cnt = 0;
            asm volatile(
                "v_mov_b32 v116, 0\n v_mov_b32 v117, 0\n v_mov_b32 v118, 0\n v_mov_b32 v119, 0\n"
                "s_barrier\n"
                "1:\n"
                "buffer_load_dwordx4 v[108:111], %[gl], %[rs], %[x0] offen sc1\n"
                "buffer_load_dwordx4 v[112:115], %[gl], %[rs], %[x1] offen sc1\n"
                "s_mov_b64 exec, %[ev]\n"
                "buffer_load_dwordx4 v[116:119], %[pml], %[rs], %[xm] offen sc1\n"
                "s_mov_b64 exec, -1\n"
                "ds_write2st64_b32 %[zw], %[zt0], %[zt1] offset1:1\n"
                POLL_COUNT
                "s_waitcnt lgkmcnt(0)\n"
                "ds_read_b128 v[104:107], %[zr]\n"
                "v_mov_b32 v120, 0\n v_mov_b32 v121, 0\n v_mov_b32 v122, 0\n v_mov_b32 v123, 0\n"
                "s_mov_b64 exec, %[lo8]\n"
                "s_waitcnt lgkmcnt(0)\n"
                "buffer_store_dwordx4 v[104:107], %[zl], %[rs], %[xz] offen sc1\n"
                "s_mov_b64 exec, -1\n"
                "s_waitcnt vmcnt(3)\n"
                "v_smfmac_f32_16x16x64_bf16 v[120:123], v[108:111], %[zb0], %[si]\n"
                "v_max3_u32 v124, v108, v109, v110\n"
                "s_waitcnt vmcnt(2)\n"
                "v_smfmac_f32_16x16x64_bf16 v[120:123], v[112:115], %[zb1], %[si]\n"
                "v_max3_u32 v124, v124, v111, v112\n"
                "v_max3_u32 v124, v124, v113, v114\n"
                "s_waitcnt vmcnt(1)\n"
                "v_mfma_f32_16x16x32_bf16 v[120:123], v[116:119], v[104:107], v[120:123]\n"
                "v_max3_u32 v124, v124, v115, v116\n"
                "v_max3_u32 v124, v124, v117, v118\n"
                "v_max_u32 v124, v124, v119\n"
                "v_cmp_lt_u32 vcc, %[key], v124\n"
                "s_nop 2\n"
                "s_cbranch_vccnz 1b\n"
                "v_add_f32 v124, v120, v121\n"
                "v_add_f32 v125, v122, v123\n"
                "ds_write2_b32 %[rw], v124, v125 offset1:80\n"
                "s_waitcnt lgkmcnt(0)\n"
                "s_barrier\n"
                : [cnt] "+s"(cnt), [key] "+s"(key)
                : [gl] "v"(gl), [pml] "v"(pml), [rs] "s"(cx.ar), [x0] "s"(xx0), [x1] "s"(xx1), [xm] "s"(xm), [xz] "s"(xzm), [ev] "s"(0x5555555555555555ull), [lo8] "s"(0x00FF00FF00FF00FFull),
                  [cap] "s"(POLLCAP), [zb0] "v"(zb0), [zb1] "v"(zb1), [si] "v"(sidx), [rw] "v"(redw), [zw] "v"(ztw), [zr] "v"(ztr), [zl] "v"(zl), [zt0] "v"(zt0), [zt1] "v"(zt1)
                : "memory", "scc", "vcc", "v104", "v105", "v106", "v107", "v108", "v109", "v110", "v111", "v112", "v113", "v114", "v115", "v116", "v117", "v118", "v119", "v120", "v121", "v122", "v123", "v124", "v125");
            dsp = cnt - 1;
        }
        if (wave == 7) {
            const unsigned char* rb = lds + redr;
            f32x4 r0 = *(const f32x4*)(rb), r1 = *(const f32x4*)(rb + 16), r2 = *(const f32x4*)(rb + 32), r3 = *(const f32x4*)(rb + 48);
            r0 = (r0 + r1) + (r2 + r3);
            if (lane < 32) st32(pk2s(r0.x + r0.z, r0.y + r0.w), cx.ar, lane * 4, xpub);
        }
        DT(d_pub);
        DV(0, d_b1); DV(1, d_zp); DV(2, d_pub); DV(3, d_pub); DV(4, late); DV(5, dsp);
        if (wave == 1) pace_next(pc, read_ref(tv, cx, adts, xts), s, p);
        if (wave == 6 && s + 2 >= ready && ready < NSEG) ready = wait_upto<1>(pv, min(s + 2, NSEG - 1), 0, lane, cx.ctl);
    }
    if (key == SENT) __hip_atomic_store(cx.ctl + CW_TMO, 1u, RLX_AGENT);
    VMCNT(0);
    BAR();
    if (wave == 6 && lane == 0) __hip_atomic_store(cx.ctl + CW_FIN + 16 * (32 + c8), 1u, RLX_AGENT);
    DFLUSH();
}

constexpr int L_XT = L_STAGE, L_VI = 0;
template <int TYPE>
DEV void l_worker(const Params& p, const Ctx& cx, unsigned char* lds, int h, int second, int wave) {
    constexpr int K1 = TYPE == 0 ? 3 : 10;
    v4u w4[40];
    if (TYPE == 0) load_units<1>(p, h, lds, wave, lane_id(), [&](int slot, v2u (&t)[4]) {
        const int ks = slot >> 1, tau = slot & 1;
        w4[(2 * ks) * 2 + tau] = (v4u){t[0].x, t[0].y, t[1].x, t[1].y}; w4[(2 * ks + 1) * 2 + tau] = (v4u){t[2].x, t[2].y, t[3].x, t[3].y}; });
    else load_units<2>(p, h, lds, wave, lane_id(), [&](int slot, v2u (&t)[4]) {
        w4[2 * slot] = (v4u){t[0].x, t[0].y, t[1].x, t[1].y}; w4[2 * slot + 1] = (v4u){t[2].x, t[2].y, t[3].x, t[3].y}; });
    EVT(1);
    const int lane = lane_id();
    const u32 lds0 = (u32)(size_t)(LAS unsigned char*)lds;
    const int i = lane & 15, gq = lane >> 4;
    const rsrc_t rxin = mkrs(p.x, S * DM * 2);
    const int xrow = (wave & 3) * 8 + (lane >> 3);
    const int xvo = xrow * (DM * 2) + (((lane & 7) ^ ((xrow >> 1) & 7)) * 16) + (5 * (wave >> 2)) * 128;
    const u32 xld = lds0 + L_XT + (5 * (wave >> 2)) * 4096 + (wave & 3) * 1024;
    auto issue1 = [&](int s, int buf, int k) { dma16(rfl(xld + buf * 40960 + k * 4096), xvo, rxin, rfl(min(s, NSEG - 1) * (32 * DM * 2) + k * 128)); };
    const int nit = second ? K1 : NSEG - K1;
    auto seg_of = [&](int it) { return second ? (it < K1 ? 2 * it + 1 : NSEG) : it + min(it, K1); };
#pragma unroll
    for (int k = 0; k < 5; ++k) issue1(seg_of(0), 0, k);
#pragma unroll
    for (int k = 0; k < 5; ++k) issue1(seg_of(1), 1, k);
    const int sel = cx.par;
    const u32 xb0 = lds0 + L_XT + i * 128 + ((gq ^ (i >> 1)) * 16), xb1 = lds0 + L_XT + i * 128 + (((4 + gq) ^ (i >> 1)) * 16);
    int ost0, ost1, ubase;
    if (TYPE == 0) {
        const int hp = wave & 3;
        if (wave < 4) { ost0 = (i >> 3) * 2048 + (hp >> 1) * 1024 + (16 * (2 * (hp & 1) + (gq >> 1)) + 2 * (i & 7) + (gq & 1)) * 16; ost1 = ost0 + 4096; ubase = QX_L + h * 8192; }
        else { const int ip = i >> 1; ost0 = (i & 1) * 4096 + hp * 1024 + (16 * gq + 4 * (ip & 1) + ((ip >> 1) & 3)) * 16; ost1 = ost0 + 128; ubase = KX_L + h * 8192; }
    } else { ost0 = (i >> 3) * 16384 + (16 * gq + (i & 7)) * 16; ost1 = 0; ubase = ZX_P + wave * 32768 + (h >> 2) * 2048 + (h & 1) * 1024 + ((h >> 1) & 1) * 128; }
    const int viw = L_VI + i * 256 + (((4 * (wave >> 1) + gq) ^ i) * 16) + (wave & 1) * 8;
    const int vtok = 8 * (i >> 2) + (i & 3);
    auto signal = [&](int it) { if (wave == 0 && lane == 0) __hip_atomic_store(cx.ctl + CW_P + 16 * (64 * TYPE + 2 * h + second), (u32)seg_of(it + 1), RLX_AGENT); };
    auto phase1 = [&](int it, int buf) {
        const int bn = buf == 0 ? 2 : buf - 1, sn = seg_of(it + 2);
        const u32 x0 = xb0 + buf * 40960, x1 = xb1 + buf * 40960;
        f32x4 d00 = {0, 0, 0, 0}, d01 = d00, d10 = d00, d11 = d00;
        asm volatile("" : "+v"(d00), "+v"(d01), "+v"(d10), "+v"(d11));
        v4u b[8];
#define XRD(slot, ss) do { LDS_RD128(b[2 * (slot)], ((ss) & 1) ? x1 : x0, ((ss) >> 1) * 4096); LDS_RD128(b[2 * (slot) + 1], ((ss) & 1) ? x1 : x0, ((ss) >> 1) * 4096 + 2048); } while (0)
#pragma unroll
        for (int ss = 0; ss < 4; ++ss) XRD(ss, ss);
#pragma unroll
        for (int ss = 0; ss < 20; ++ss) {
            const int sl = ss & 3;
            if (ss < 17) LGKM_WAIT2(6, b[2 * sl], b[2 * sl + 1]); else LGKM_WAIT2(2 * (19 - ss), b[2 * sl], b[2 * sl + 1]);
            if (TYPE == 0) { MF_WA(d00, 2 * ss, b[2 * sl]); MF_WA(d01, 2 * ss, b[2 * sl + 1]); MF_WA(d10, 2 * ss + 1, b[2 * sl]); MF_WA(d11, 2 * ss + 1, b[2 * sl + 1]); }
            else { MF_WA(d00, ss, b[2 * sl]); MF_WA(d01, ss, b[2 * sl + 1]); }
            if (ss + 4 < 20) XRD(sl, ss + 4);
            if (TYPE == 0 && ss == 8) { VMCNT(0); RAWBAR(); if (it > 0) signal(it - 1); }
            if (TYPE == 0 && ss >= 9 && ss < 19 && (ss & 1)) issue1(sn, bn, (ss - 9) >> 1);
            if (TYPE == 1 && ss % 4 == 1) issue1(sn, bn, ss >> 2);
        }
#undef XRD
        asm volatile("s_nop 7" : "+v"(d00), "+v"(d01), "+v"(d10), "+v"(d11));
        if (TYPE == 0) {
            const int so = rfl(amap(ubase + seg_of(it) * (H * 8192), sel));
            u32 a0 = pk2(d00.x, d00.y), a1 = pk2(d00.z, d00.w), a2 = pk2(d10.x, d10.y), a3 = pk2(d10.z, d10.w);
            u32 c0 = pk2(d01.x, d01.y), c1 = pk2(d01.z, d01.w), c2 = pk2(d11.x, d11.y), c3 = pk2(d11.z, d11.w);
            if (wave < 4) {
                auto sa = __builtin_amdgcn_permlane16_swap(a0, a1, false, false), sb = __builtin_amdgcn_permlane16_swap(a2, a3, false, false);
                auto sc = __builtin_amdgcn_permlane16_swap(c0, c1, false, false), sd = __builtin_amdgcn_permlane16_swap(c2, c3, false, false);
                st128((v4u){sa[0], sb[0], sa[1], sb[1]}, cx.ar, ost0, so);
                st128((v4u){sc[0], sd[0], sc[1], sd[1]}, cx.ar, ost1, so);
            } else {
                st128((v4u){a0, a1, a2, a3}, cx.ar, ost0, so);
                st128((v4u){c0, c1, c2, c3}, cx.ar, ost1, so);
            }
        } else {
            *(v2u*)(lds + viw + (it & 1) * 8192) = (v2u){pk2(d00.x, d00.y), pk2(d00.z, d00.w)};
            *(v2u*)(lds + viw + (it & 1) * 8192 + 4096) = (v2u){pk2(d01.x, d01.y), pk2(d01.z, d01.w)};
        }
    };
    auto phase2 = [&](int it, bool mid) {
        const int ub = ubase + seg_of(it) * ZX_STEP;
        v4u a[8];
#pragma unroll
        for (int k = 0; k < 8; ++k) { const int tok = vtok + 4 * (k >> 2); a[k] = *(const v4u*)(lds + L_VI + (it & 1) * 8192 + tok * 256 + (((4 * (k & 3) + gq) ^ (tok & 15)) * 16)); }
        f32x4 d0[5], d1[5];
#pragma unroll
        for (int cg = 0; cg < 5; ++cg) { d0[cg] = (f32x4){0, 0, 0, 0}; d1[cg] = d0[cg]; asm volatile("" : "+v"(d0[cg]), "+v"(d1[cg])); }
        asm volatile("s_nop 1");
#pragma unroll
        for (int cg = 0; cg < 6; ++cg) {
            if (cg < 5) {
#pragma unroll
                for (int ss = 0; ss < 4; ++ss) { MF_WB(d0[cg], a[ss], 20 + cg * 4 + ss); MF_WB(d1[cg], a[4 + ss], 20 + cg * 4 + ss); }
            } else asm volatile("s_nop 7");
            if (cg == 0 && mid) { VMCNT(5); BAR(); if (it > 0) signal(it - 1); }
            if (cg > 0) {
                asm volatile("" : "+v"(d0[cg - 1]), "+v"(d1[cg - 1]));
                st128((v4u){pk2(d0[cg - 1].x, d0[cg - 1].y), pk2(d0[cg - 1].z, d0[cg - 1].w), pk2(d1[cg - 1].x, d1[cg - 1].y), pk2(d1[cg - 1].z, d1[cg - 1].w)},
                      cx.ar, ost0, ub + (cg - 1) * (8 * 32768));
            }
        }
    };
    VMCNT(0);
    BAR();
    if (TYPE == 0) {
        int buf = 0;
        for (int it = 0; it < nit; ++it) {
            phase1(it, buf);
            buf = buf == 2 ? 0 : buf + 1;
            if (it == 0) { VMCNT(0); BAR(); signal(0); EVT(2); }
        }
    } else {
        phase1(0, 0);
        BAR();
        phase2(0, false);
        VMCNT(0); BAR(); signal(0);
        EVT(2);
        phase1(1, 1);
        VMCNT(5); BAR();
        int buf = 2;
        for (int k = 1; k < nit; ++k) {
            phase1(k + 1, buf);
            phase2(k, true);
            buf = buf == 2 ? 0 : buf + 1;
        }
    }
    VMCNT(0);
    BAR();
    signal(nit - 1);
}

constexpr int SS_K = 33 * 144, SS_U = 4288, SS_CH = 2 * SS_K + 2 * SS_U + 32;
static_assert(SS_CH % 16 == 0 && 8 * SS_CH <= L_TOTAL, "ssm lds");
DEV void ssm_task(const Params& p, const Ctx& cx, unsigned char* lds, int task, int wave, int far) {
    const int lane = lane_id(), tid = wave * 64 + lane;
    const int d = task * 8 + wave;
    unsigned char* ch = lds + wave * SS_CH;
    unsigned char* tmpb = ch + 2 * SS_K;
    const int i = lane & 15, gq = lane >> 4;
    v4u xr[2][3];
    {
        const float dt = rbf(__expf(bf2f(p.logdt[d])));
#pragma unroll
        for (int k = 0; k < 2; ++k) {
            const int nn = lane + 64 * k, o = d * NS + nn;
            const float br = dt * bf2f(p.Bre[o]), bi = dt * bf2f(p.Bim[o]), cr = bf2f(p.Cre[o]), ci = bf2f(p.Cim[o]);
            *(f32x4*)(tmpb + nn * 16) = (f32x4){dt * bf2f(p.Are[o]) * 1.4426950408889634f, dt * bf2f(p.Aim[o]) * 0.15915494309189535f, cr * br - ci * bi, cr * bi + ci * br};
        }
        asm volatile("s_waitcnt lgkmcnt(0)" ::: "memory");
        {
            const rsrc_t rxin = mkrs(p.x, S * DM * 2);
#pragma unroll
            for (int k = 0; k < 2; ++k)
#pragma unroll
                for (int j = 0; j < 3; ++j) xr[k][j] = __builtin_amdgcn_raw_buffer_load_b128(rxin, ((2 * (tid + 512 * k) + j) * DM + task * 8) * 2, 0, 0);
            __builtin_amdgcn_sched_barrier(0);
        }
        const float lu = 16.f * i;
        const float lv0 = (float)i;
        const float lv1 = i < 8 ? 256.f + 2.f * i : 512.f + 256.f * ((i - 8) >> 2) + 4.f * (i & 3);
        const float lv2 = i < 8 ? 1024.f + 256.f * (i >> 1) + 8.f * (i & 1) : 2048.f;
        f32x4 k0 = {0, 0, 0, 0}, k1 = k0, k2 = k0;
#pragma unroll 2
        for (int it = 0; it < 32; ++it) {
            const f32x4 q = *(const f32x4*)(tmpb + (4 * it + gq) * 16);
            auto cexp = [&](float l, float& re, float& im) {
                const float e = __builtin_amdgcn_exp2f(q.x * l), th = __builtin_amdgcn_fractf(q.y * l);
                re = e * __builtin_amdgcn_cosf(th); im = e * __builtin_amdgcn_sinf(th);
            };
            float er, ei, r0, i0, r1, i1, r2, i2;
            cexp(lu, er, ei); cexp(lv0, r0, i0); cexp(lv1, r1, i1); cexp(lv2, r2, i2);
            const float ur = q.z * er - q.w * ei, nui = -(q.z * ei + q.w * er);
            k0 = __builtin_amdgcn_mfma_f32_16x16x4f32(ur, r0, k0, 0, 0, 0); k1 = __builtin_amdgcn_mfma_f32_16x16x4f32(ur, r1, k1, 0, 0, 0); k2 = __builtin_amdgcn_mfma_f32_16x16x4f32(ur, r2, k2, 0, 0, 0);
            k0 = __builtin_amdgcn_mfma_f32_16x16x4f32(nui, i0, k0, 0, 0, 0); k1 = __builtin_amdgcn_mfma_f32_16x16x4f32(nui, i1, k1, 0, 0, 0); k2 = __builtin_amdgcn_mfma_f32_16x16x4f32(nui, i2, k2, 0, 0, 0);
        }
        float* tab = (float*)(tmpb + 2048);
        *(f32x4*)(tab + i * 20 + 4 * gq) = k0; *(f32x4*)(tab + (16 + i) * 20 + 4 * gq) = k1; *(f32x4*)(tab + (32 + i) * 20 + 4 * gq) = k2;
        asm volatile("s_waitcnt lgkmcnt(0)" ::: "memory");
        if (lane < 36) { *(u32*)(ch + lane * 4) = 0; *(u32*)(ch + SS_K + lane * 4) = 0; }
#pragma unroll 2
        for (int it = 0; it < 16; ++it) {
            const int l0 = it * 128 + lane * 2;
            float kv[2];
#pragma unroll
            for (int e = 0; e < 2; ++e) {
                const int lr = (int)rbf((float)(l0 + e));
                const int a = (lr >> 4) & 15, j = lr & 15, hi = lr >> 8;
                int col;
                if (hi == 0) col = j; else if (hi == 1) col = 16 + (j >> 1); else if (hi < 4) col = 24 + 4 * (hi - 2) + (j >> 2); else if (hi < 8) col = 32 + 2 * (hi - 4) + (j >> 3); else col = 40;
                kv[e] = tab[col * 20 + a];
            }
            const float h0 = rbf(kv[0]), h1 = rbf(kv[1]);
            const int o = ((l0 >> 6) + 1) * 144 + ((l0 & 56) + 6 - (l0 & 6)) * 2;
            *(u32*)(ch + o) = pk2(h1, h0);
            *(u32*)(ch + SS_K + o) = pk2(kv[1] - h1, kv[0] - h0);
        }
    }
    EVT(8);
    BAR();
    {
        if (lane < 24) { *(u32*)(ch + 2 * SS_K + lane * 4) = 0; if (lane < 23) *(u32*)(ch + 2 * SS_K + SS_U + lane * 4) = 0; }
#pragma unroll
        for (int k = 0; k < 2; ++k) {
            const int l = 2 * (tid + 512 * k);
#pragma unroll
            for (int c = 0; c < 8; ++c) {
                const u32 sel = (c & 1) ? 0x07060302u : 0x05040100u;
                unsigned char* ub = lds + c * SS_CH + 2 * SS_K;
                *(u32*)(ub + (l + 48) * 2) = __builtin_amdgcn_perm(xr[k][1][c >> 1], xr[k][0][c >> 1], sel);
                *(u32*)(ub + SS_U + (l + 48) * 2) = __builtin_amdgcn_perm(xr[k][2][c >> 1], xr[k][1][c >> 1], sel);
                if (k == 0 && tid == 0) *(u32*)(ub + SS_U + 46 * 2) = __builtin_amdgcn_perm(xr[0][0][c >> 1], 0u, sel);
            }
        }
    }
    BAR();
    EVT(9);
    f32x4 acc[2][4];
#pragma unroll
    for (int I = 0; I < 2; ++I)
#pragma unroll
        for (int rt = 0; rt < 4; ++rt) acc[I][rt] = (f32x4){0, 0, 0, 0};
    {
        const int st0 = i - 8 * gq - 7 + 48;
        const unsigned char* ua = ch + 2 * SS_K + ((st0 & 1) ? SS_U + (st0 - 1) * 2 : st0 * 2);
        auto afrag = [&](int o) { const u32* q = (const u32*)(ua + o * 32); return (v4u){q[0], q[1], q[2], q[3]}; };
        v4u am2 = {0, 0, 0, 0};
        v4u am1 = afrag(-1);
        const int kb = gq * 16;
        auto step = [&](int t, int I, const v4u& a0, const v4u& a1, const v4u& a2, const v4u& a3) {
            const int blk = max(16 * I + i - t + 1, 0);
            const unsigned char* kp = ch + blk * 144 + kb;
#pragma unroll
            for (int hl = 0; hl < 2; ++hl) {
                const v4u b0 = *(const v4u*)(kp + hl * SS_K), b1 = *(const v4u*)(kp + hl * SS_K + 64);
                acc[I][0] = mfma16(a0, b0, acc[I][0]); acc[I][1] = mfma16(a1, b0, acc[I][1]); acc[I][2] = mfma16(a2, b0, acc[I][2]); acc[I][3] = mfma16(a3, b0, acc[I][3]);
                acc[I][0] = mfma16(am2, b1, acc[I][0]); acc[I][1] = mfma16(am1, b1, acc[I][1]); acc[I][2] = mfma16(a0, b1, acc[I][2]); acc[I][3] = mfma16(a1, b1, acc[I][3]);
            }
        };
#pragma unroll 1
        for (int t = 0; t < 16; ++t) {
            const v4u a0 = afrag(4 * t), a1 = afrag(4 * t + 1), a2 = afrag(4 * t + 2), a3 = afrag(4 * t + 3);
            step(t, 0, a0, a1, a2, a3); step(t, 1, a0, a1, a2, a3);
            am2 = a2; am1 = a3;
        }
#pragma unroll 1
        for (int t = 16; t < 32; ++t) {
            const v4u a0 = afrag(4 * t), a1 = afrag(4 * t + 1), a2 = afrag(4 * t + 2), a3 = afrag(4 * t + 3);
            step(t, 1, a0, a1, a2, a3);
            am2 = a2; am1 = a3;
        }
    }
    EVT(10);
    {
        const float dsk = bf2f(p.dskip[d]);
        asm volatile("s_waitcnt lgkmcnt(0)" ::: "memory");
#pragma unroll
        for (int I = 0; I < 2; ++I)
#pragma unroll
            for (int rt = 0; rt < 4; ++rt) {
                const int l = 64 * (16 * I + i) + 16 * rt + 4 * gq;
                const v2u uu = *(const v2u*)(ch + 2 * SS_K + (l + 48) * 2);
                const f32x4 y = acc[I][rt];
                const float b0 = rbf(y.x) + rbf(bf2f(uu.x & 0xffffu) * dsk), b1 = rbf(y.y) + rbf(bfhi(uu.x) * dsk), b2 = rbf(y.z) + rbf(bf2f(uu.y & 0xffffu) * dsk), b3 = rbf(y.w) + rbf(bfhi(uu.y) * dsk);
                *(v2u*)(ch + (16 * I + i) * 136 + (16 * rt + 4 * gq) * 2) = (v2u){pk2(b0, b1), pk2(b2, b3)};
            }
    }
    BAR();
#pragma unroll
    for (int k = 0; k < 2; ++k) {
        const int l = 2 * (tid + 512 * k);
        u32 e[8];
#pragma unroll
        for (int c = 0; c < 8; ++c) e[c] = *(const u32*)(lds + c * SS_CH + (l >> 6) * 136 + (l & 63) * 2);
        v4u lo, hi;
#pragma unroll
        for (int c = 0; c < 4; ++c) { lo[c] = __builtin_amdgcn_perm(e[2 * c + 1], e[2 * c], 0x05040100u); hi[c] = __builtin_amdgcn_perm(e[2 * c + 1], e[2 * c], 0x07060302u); }
        const int so = rfl(amap(BS_L + task * 32768 + k * 16384 + wave * 2048, far));
        st128(lo, cx.ar, lane * 32, so); st128(hi, cx.ar, lane * 32 + 16, so);
    }
    VMCNT(0);
    BAR();
    if (tid == 0) __hip_atomic_fetch_add(cx.ctl + CW_SSM + 64 * (task >> 4), 1u, RLX_AGENT);
}

constexpr int L_OP = 0, L_OA = 81920, L_OG = L_OA + 32 * 272;
DEV void out_task(const Params& p, const Ctx& cx, unsigned char* lds, int s, int cgp, int wave, int& ready) {
    const int lane = lane_id(), tid = wave * 64 + lane, far = cx.par ^ 1;
    const int n = lane & 15, gq = lane >> 4, gl = lane * 16;
    SIDX_DECL;
    const int ct = 8 * cgp + wave;
    const int zxl = (n >> 3) * 16384 + (16 * gq + (n & 7)) * 16, zml = (n >> 3) * 4096 + gq * 128 + (n & 7) * 16;
    if (wave == 0 && s >= ready) ready = wait_upto<1>(prog_load<1>(0, lane, cx.ctl), s, 0, lane, cx.ctl);
    BAR();
    v8u wr[20];
#pragma unroll
    for (int k = 0; k < 16; ++k) {
        const int so = ZX_P + s * ZX_STEP + ct * 32768 + (k >> 1) * 2048;
        wr[k] = cat8(ld128(cx.ar, zxl + (k & 1) * 128, so), ld128(cx.ar, zxl + (k & 1) * 128 + 1024, so));
    }
    const int otok = tid >> 4, oc = tid & 15;
    const v4u bv = ld128(cx.ar, amap(BS_L + (16 * cgp + oc) * 32768 + (s * 32 + otok) * 16, far), 0);
    {
        const rsrc_t rxin = mkrs(p.x, S * DM * 2), rg = mkrs(p.gw, DM * 2);
        const int tok = wave * 4 + gq;
        float sum = 0.f;
#pragma unroll
        for (int k = 0; k < 5; ++k) {
            const v4u xv = __builtin_amdgcn_raw_buffer_load_b128(rxin, ((s * 32 + tok) * DM + k * 128 + n * 8) * 2, 0, 0);
            const v4u gv = __builtin_amdgcn_raw_buffer_load_b128(rg, (k * 128 + n * 8) * 2, 0, 0);
#pragma unroll
            for (int e = 0; e < 4; ++e) sum += rbf(bf2f(xv[e] & 0xffffu) * bf2f(gv[e] & 0xffffu)) + rbf(bfhi(xv[e]) * bfhi(gv[e]));
        }
        sum = rbf(rowsum16(sum));
        const float g = rbf(1.f / (1.f + __expf(-sum)));
        if (n == 0) *(float*)(lds + L_OG + tok * 4) = g;
    }
    if (wave == 0) {
        if (s < NSEG - 2) wait_ge(cx.ctl + CW_S + 16 * s, 1u, cx.ctl);
        else {
            u32 spins = 0;
            for (;;) {
                const u32 a = __hip_atomic_load(cx.ctl + CW_FIN + 16 * lane, RLX_AGENT), c = __hip_atomic_load(cx.ctl + CW_FIN + 16 * (lane < 48 ? lane + 64 : lane), RLX_AGENT);
                if (__all(a && c)) break;
                __builtin_amdgcn_s_sleep(2);
                if (spin_fail(spins, cx.ctl)) break;
            }
        }
    }
    BAR();
    {
        v4u t[10];
#pragma unroll
        for (int k = 0; k < 10; ++k) t[k] = ld128(cx.ar, gl, rfl(amap(PT_L + s * PT_STEP + (k < 8 ? wave * 8 + k : 48 + wave * 2 + 8 + k) * 1024, far)));
        const int so = rfl(amap(ZM_L + s * ZM_STEP + ct * 8192, cx.par));
#pragma unroll
        for (int k = 0; k < 4; ++k) wr[16 + k] = cat8(ld128(cx.ar, zml + k * 1024, so), ld128(cx.ar, zml + k * 1024 + 512, so));
#pragma unroll
        for (int k = 0; k < 8; ++k) *(v4u*)(lds + L_OP + (wave * 8 + k) * 1024 + gl) = t[k];
#pragma unroll
        for (int k = 8; k < 10; ++k) {
            unsigned char* d = lds + L_OP + (56 + wave * 2 + k) * 1024 + gq * 256 + (lane & 7) * 32 + ((lane >> 3) & 1) * 8;
            *(v2u*)d = (v2u){t[k].x, t[k].y}; *(v2u*)(d + 16) = (v2u){t[k].z, t[k].w};
        }
    }
    BAR();
    f32x4 acc[4];
#pragma unroll
    for (int g = 0; g < 4; ++g) acc[g] = (f32x4){0, 0, 0, 0};
    asm volatile("s_nop 1" : "+v"(acc[0]), "+v"(acc[1]), "+v"(acc[2]), "+v"(acc[3]));
#pragma unroll
    for (int ks = 0; ks < 20; ++ks)
#pragma unroll
        for (int g = 0; g < 4; ++g) {
            const v4u a = *(const v4u*)(lds + L_OP + (ks < 16 ? g * 16 + ks : 64 + g * 4 + ks - 16) * 1024 + gl);
            SMF_W(acc[g], a, ks);
        }
    asm volatile("s_nop 7" : "+v"(acc[0]), "+v"(acc[1]), "+v"(acc[2]), "+v"(acc[3]));
#pragma unroll
    for (int g = 0; g < 4; ++g) {
        const u32 v = pk2(acc[g].x + acc[g].y, acc[g].z + acc[g].w);
        unsigned char* o = lds + L_OA + (8 * g + 2 * gq) * 272 + (wave * 16 + n) * 2;
        *(u16*)o = (u16)v; *(u16*)(o + 272) = (u16)(v >> 16);
    }
    BAR();
    {
        const v4u av = *(const v4u*)(lds + L_OA + otok * 272 + oc * 16);
        const float g = *(const float*)(lds + L_OG + otok * 4), og = rbf(1.f - g);
        v4u o;
#pragma unroll
        for (int e = 0; e < 4; ++e)
            o[e] = pk2(rbf(g * bf2f(av[e] & 0xffffu)) + rbf(og * bf2f(bv[e] & 0xffffu)), rbf(g * bfhi(av[e])) + rbf(og * bfhi(bv[e])));
        *(v4u*)(p.out + (s * 32 + otok) * DM + cgp * 128 + oc * 8) = o;
    }
}

DEV unsigned char* arena_base(unsigned char* ws) { return (unsigned char*)(((size_t)ws + CTRL_BYTES + (2u << 20) - 1) & ~(size_t)((2u << 20) - 1)); }

constexpr int LI_V = 0, LI_O = 40960, LI_TOTAL = LI_O + 3 * 32768;
__global__ void __launch_bounds__(576) gm_init(const u16* Wv, const u16* Wo, unsigned char* ws) {
    extern __shared__ __attribute__((aligned(16))) unsigned char lds[];
    const int b = blockIdx.x, wave = rfl(threadIdx.x >> 6), lane = threadIdx.x & 63;
    const rsrc_t ar = mkrs(arena_base(ws), ARENA_BYTES);
    if (wave == 8) {
        if (b == 0) {
            u32 mn[2] = {100000u, 100000u};
            for (int it = 0; it < 4; ++it)
#pragma unroll
                for (int q = 0; q < 2; ++q) {
                    const u32 c0 = now32();
                    st32(0u, ar, lane * 4, (4 << 20) + it * 256 + q * 8192);
                    VMCNT(0);
                    mn[q] = min(mn[q], now32() - c0);
                }
            if (lane < 32) ((u32*)ws)[CW_PAR + lane] = mn[1] < mn[0];
        } else if (b == 1) {
            for (int i = lane; i < CTRL_BYTES / 16; i += 64) if (i < CW_PAR / 4 || i >= CW_PAR / 4 + 8) ((v4u*)ws)[i] = (v4u){0, 0, 0, 0};
        }
        const v4u sv = {SENT, SENT, SENT, SENT};
#pragma unroll
        for (int k = 0; k < 16; ++k) st128(sv, ar, lane * 16, b * 16384 + k * 1024);
        return;
    }
    const int h = 4 * (b & 7) + (b >> 6), g0 = 5 * ((b >> 4) & 3), ch = (b >> 3) & 1;
    const int i = lane & 15, gq = lane >> 4;
    const u32 lds0 = (u32)(size_t)(LAS unsigned char*)lds;
    {
        const int row = wave * 4 + gq, f = (row & 13) | ((row >> 4) << 1);
        const rsrc_t rv = mkrs(Wv, DM * HD * 2);
#pragma unroll
        for (int g = 0; g < 5; ++g) dma16(rfl(lds0 + LI_V + g * 8192 + wave * 1024), row * (HD * 2) + ((i ^ f) * 16), rv, rfl(((g0 + g) * 32 * HD + h * DH) * 2));
    }
    {
        const rsrc_t ro = mkrs(Wo, DM * HD * 2);
        const int tp = lane >> 3, q4 = (lane >> 1) & 3, hf = lane & 1;
#pragma unroll
        for (int cg = 0; cg < 3; ++cg)
#pragma unroll
            for (int k = 0; k < 4; ++k)
                dma16(rfl(lds0 + LI_O + cg * 32768 + (wave * 4 + k) * 1024), q4 * (DM * 2) + (((tp ^ (k >> 1)) & (cg == 2 ? 3 : 7)) * 16 + hf * 8) * 2, ro,
                      rfl(((h * DH + wave * 16 + k * 4) * DM + ch * 320 + cg * 128) * 2));
    }
    const int rd0 = LI_O + (2 * gq) * 1024 + (i >> 2) * 32 + (i & 3) * 8 + (wave ^ (gq & 1)) * 128;
    const int dl = 16 * (i >> 3) + 8 * ((i >> 1) & 1) + 4 * ((i >> 2) & 1) + (i & 1);
    const int fl = (dl & 13) | ((dl >> 4) << 1);
#pragma unroll
    for (int cg = 0; cg < 3; ++cg) {
        if (cg == 0) VMCNT(8); else if (cg == 1) VMCNT(9); else VMCNT(10);
        BAR();
        if (cg == 2 && wave >= 4) break;
        v4u w[4];
#pragma unroll
        for (int ss = 0; ss < 4; ++ss) {
            const v2u t0 = __builtin_bit_cast(v2u, __builtin_amdgcn_ds_read_tr16_b64_v4i16((LAS s16x4*)(lds + rd0 + cg * 32768 + ss * 8192)));
            const v2u t1 = __builtin_bit_cast(v2u, __builtin_amdgcn_ds_read_tr16_b64_v4i16((LAS s16x4*)(lds + rd0 + cg * 32768 + ss * 8192 + 1024)));
            w[ss] = (v4u){t0.x, t0.y, t1.x, t1.y};
        }
#pragma unroll
        for (int g = 0; g < 5; ++g) {
            f32x4 d0 = {0, 0, 0, 0}, d1 = d0;
#pragma unroll
            for (int ss = 0; ss < 4; ++ss) {
                d0 = mfma16(*(const v4u*)(lds + LI_V + g * 8192 + dl * 256 + (((4 * ss + gq) ^ fl) * 16)), w[ss], d0);
                d1 = mfma16(*(const v4u*)(lds + LI_V + g * 8192 + (dl + 2) * 256 + (((4 * ss + gq) ^ fl) * 16)), w[ss], d1);
            }
            st128((v4u){pk2(d0.x, d0.y), pk2(d0.z, d0.w), pk2(d1.x, d1.y), pk2(d1.z, d1.w)}, ar, lane * 16, VO_P + ((h * 20 + g0 + g) * 40 + ch * 20 + cg * 8 + wave) * 1024);
        }
    }
}

__global__ void __launch_bounds__(512, 2) gm_kernel(Params p) {
    extern __shared__ __attribute__((aligned(16))) unsigned char lds[];
    const int b = blockIdx.x;
    const int wave = rfl(threadIdx.x >> 6);
    Ctx cx;
    cx.ctl = (GAS u32*)p.ws;
    cx.ar = mkrs(arena_base(p.ws), ARENA_BYTES);
    cx.par = rfl(cx.ctl[CW_PAR]) & 1;
    EVT(0);
    if ((b & 7) >= 4) {
        const int u = (b >> 3) * 4 + (b & 7) - 4, j = u >> 2;
        const int h = 8 * (u & 3) + (j >> 2), type = (j >> 1) & 1, second = j & 1;
        if (type) l_worker<1>(p, cx, lds, h, second, wave); else l_worker<0>(p, cx, lds, h, second, wave);
        EVT(3);
        if (!second) return;
        ssm_task(p, cx, lds, type * 32 + h, wave, cx.par ^ 1);
        EVT(4);
        const int nb = type ? 12 : 10, gi = h / nb;
        if (gi >= (type ? 2 : 3)) return;
        const int cgp = type ? 2 + gi : (gi == 2 ? 4 : gi);
        if (wave == 0) { wait_ge(cx.ctl + CW_SSM + 64 * cgp, 16u, cx.ctl); for (int k = 0; k < h % nb; ++k) __builtin_amdgcn_s_sleep(14); }
        int ready = 0;
#pragma unroll 1
        for (int s = h % nb; s < NSEG; s += nb) out_task(p, cx, lds, s, cgp, wave, ready);
        EVT(5);
        return;
    }
    const int w = (b >> 3) * 4 + (b & 7);
    if (w < 32) {
        if (wave < 2) a_role<0>(p, cx, lds, w, wave);
        else if (wave < 6) a_role<1>(p, cx, lds, w, wave);
        else a_role<2>(p, cx, lds, w, wave);
    } else if (w < 112) r_role(p, cx, lds, w - 32, wave);
    else {
        ssm_task(p, cx, lds, w - 48, wave, cx.par ^ 1);
        if (w == 112 && wave == 0) {
            const int lane = lane_id();
            for (int s = 2; s < NSEG; ++s) {
                const int so = rfl(amap(s * SLOT + TS_O, cx.par));
                u32 spins = 0;
                for (;;) {
                    asm volatile("" ::: "memory");
                    if (!__any(ld32(cx.ar, (lane & 31) * 64, so) == SENT)) break;
                    __builtin_amdgcn_s_sleep(12);
                    if (spin_fail(spins, cx.ctl)) break;
                }
                if (lane == 0) __hip_atomic_store(cx.ctl + CW_S + 16 * (s - 2), 1u, RLX_AGENT);
                if (spins) __builtin_amdgcn_s_sleep(44);
            }
        }
    }
    EVT(5);
}

static const int g_dA = 4330, g_dB = 2740, g_dR = 1200, g_dZ = 5;
static const int g_dbg = 0;

extern "C" void kernel_launch(void* const* d_in, const int* in_sizes, int n_in,
                              void* d_out, int out_size, void* d_ws, size_t ws_size,
                              hipStream_t stream) {
    Params p;
    p.x = (const u16*)d_in[0]; p.mem0 = (const u16*)d_in[1]; p.Wq = (const u16*)d_in[2]; p.Wk = (const u16*)d_in[3]; p.Wv = (const u16*)d_in[4]; p.Wo = (const u16*)d_in[5];
    p.Are = (const u16*)d_in[8]; p.Aim = (const u16*)d_in[9]; p.Bre = (const u16*)d_in[10]; p.Bim = (const u16*)d_in[11]; p.Cre = (const u16*)d_in[12]; p.Cim = (const u16*)d_in[13];
    p.logdt = (const u16*)d_in[14]; p.dskip = (const u16*)d_in[15]; p.gw = (const u16*)d_in[16];
    p.out = (u16*)d_out; p.ws = (unsigned char*)d_ws;
    p.per = g_dA; p.c2 = g_dB; p.gmax = g_dR; p.maxrel = g_dZ; p.cp = 950; p.cmL = 600; p.glate = 2350; p.pad = g_dbg;
    (void)hipFuncSetAttribute((const void*)gm_init, hipFuncAttributeMaxDynamicSharedMemorySize, LI_TOTAL);
    gm_init<<<256, 576, LI_TOTAL, stream>>>(p.Wv, p.Wo, (unsigned char*)d_ws);
    (void)hipFuncSetAttribute((const void*)gm_kernel, hipFuncAttributeMaxDynamicSharedMemorySize, L_TOTAL);
    gm_kernel<<<256, 512, L_TOTAL, stream>>>(p);
}
```

```cpp
#include <hip/hip_runtime.h>
#include <hip/hip_bf16.h>
#include <stdint.h>

typedef unsigned short u16;
typedef unsigned int u32;
typedef u32 v4u __attribute__((ext_vector_type(4)));
typedef u32 v2u __attribute__((ext_vector_type(2)));
typedef u32 v8u __attribute__((ext_vector_type(8)));
typedef float f32x4 __attribute__((ext_vector_type(4)));
typedef __bf16 bf16x8 __attribute__((ext_vector_type(8)));
typedef __bf16 bf16x2 __attribute__((ext_vector_type(2)));
typedef short s16x4 __attribute__((ext_vector_type(4)));
typedef __amdgpu_buffer_rsrc_t rsrc_t;
#define GAS __attribute__((address_space(1)))
#define LAS __attribute__((address_space(3)))
#define RLX_AGENT __ATOMIC_RELAXED, __HIP_MEMORY_SCOPE_AGENT
#define DEV __device__ __forceinline__

constexpr int S = 2048, DM = 640, H = 32, DH = 128, NSEG = 64, HD = H * DH, NS = 128;
constexpr int SC1 = 16;
constexpr u32 SENT = 0xFFFFFFFFu;

DEV float bf2f(u32 b) { return __uint_as_float(b << 16); }
DEV float bfhi(u32 b) { return __uint_as_float(b & 0xFFFF0000u); }
DEV float rbf(float f) { return (float)(__bf16)f; }
DEV u32 pk2(float lo, float hi) { bf16x2 v = {(__bf16)lo, (__bf16)hi}; return __builtin_bit_cast(u32, v); }
DEV u32 pk2s(float lo, float hi) { return min(pk2(lo, hi), 0xFFFFFFFEu); }
DEV int rfl(int v) { return __builtin_amdgcn_readfirstlane(v); }
DEV int lane_id() { int l; asm volatile("v_mbcnt_lo_u32_b32 %0, -1, 0\n\tv_mbcnt_hi_u32_b32 %0, -1, %0" : "=v"(l)); return l; }

constexpr int CTRL_BYTES = 21504;
constexpr int ARENA_BYTES = 220 << 20;
constexpr int SLOT = 32768, MEM_O = 0, PX_O = 10240, PM_O = PX_O + 16384, TS_O = PM_O + 4096;
constexpr int QX_L = 3 << 20, KX_L = 19 << 20, ZM_L = 35 << 20, ZM_STEP = 80 * 4096;
constexpr int PT_L = 3 << 20, PT_STEP = 81920, BS_L = 8 << 20;
constexpr int VO_P = 112 << 20, ZX_P = 140 << 20, ZX_STEP = 80 * 16384;
constexpr int CW_TMO = 0, CW_PAR = 64, CW_SSM = 128  , CW_S = 512, CW_FIN = 1536, CW_P = 3328;

DEV int amap(int o, int sel) { return ((o >> 13) << 14) | (o & 8191) | ((((o >> 20) ^ sel) & 1) << 13); }
DEV u32 max4(v4u v) { return max(max(v.x, v.y), max(v.z, v.w)); }
DEV v8u cat8(v4u a, v4u b) { return __builtin_shufflevector(a, b, 0, 1, 2, 3, 4, 5, 6, 7); }
DEV rsrc_t mkrs(const void* p, int bytes) { return __builtin_amdgcn_make_buffer_rsrc((void*)p, 0, bytes, 0x00020000); }
DEV v4u ld128(rsrc_t r, int off, int so) { return __builtin_amdgcn_raw_buffer_load_b128(r, off, so, SC1); }
DEV u32 ld32(rsrc_t r, int off, int so) { return __builtin_amdgcn_raw_buffer_load_b32(r, off, so, SC1); }
DEV void st128(v4u v, rsrc_t r, int off, int so) { __builtin_amdgcn_raw_buffer_store_b128(v, r, off, so, SC1); }
DEV void st32(u32 v, rsrc_t r, int off, int so) { __builtin_amdgcn_raw_buffer_store_b32(v, r, off, so, SC1); }
#define VMCNT(n) asm volatile("s_waitcnt vmcnt(%0)" :: "i"(n) : "memory")
#define BAR() do { asm volatile("s_waitcnt lgkmcnt(0)" ::: "memory"); __builtin_amdgcn_s_barrier(); asm volatile("" ::: "memory"); } while (0)
template <int CTRL> DEV float dppf(float v) { return __builtin_bit_cast(float, __builtin_amdgcn_update_dpp(0, __builtin_bit_cast(int, v), CTRL, 0xf, 0xf, true)); }
DEV float rowsum16(float v) { v += dppf<0x128>(v); v += dppf<0x124>(v); v += dppf<0x122>(v); v += dppf<0x121>(v); return v; }
DEV void dma16(u32 ldsaddr, int voff, rsrc_t rs, int soff) {
    u32 keep;
    asm volatile("s_nop 4\n\ts_mov_b32 %0, m0\n\ts_mov_b32 m0, %1\n\ts_nop 0\n\tbuffer_load_dwordx4 %2, %3, %4 offen lds\n\ts_mov_b32 m0, %0"
                 : "=&s"(keep) : "s"(ldsaddr), "v"(voff), "s"(rs), "s"(soff) : "memory");
}

constexpr u32 SPINCAP = 1u << 20;
constexpr float A_CYCLES = 1570.f;
#define EVT(i)

#define DV(slot, v)
#define DFLUSH()
#define DT(name)
struct Params {
    const u16 *x, *mem0, *Wq, *Wk, *Wv, *Wo, *Are, *Aim, *Bre, *Bim, *Cre, *Cim, *logdt, *dskip, *gw;
    u16* out;
    unsigned char* ws;
    int g1, c3, gmax, cp, cmL, maxrel, glate, pad;
};
struct Ctx { GAS u32* ctl; rsrc_t ar; int par; };

constexpr int L_MEMF = 0, L_QF = 10240, L_KMF = 12288, L_KXF = 16384  , L_RED = 10240, L_ZT = 16384, L_STAGE = 24576, L_DUMP = 32768, L_TOTAL = L_STAGE + 131072;

DEV bool spin_fail(u32& spins, GAS u32* ctl) {
    if (++spins > SPINCAP) { __hip_atomic_store(ctl + CW_TMO, 1u, RLX_AGENT); return true; }
    return (spins & 1023u) == 0 && rfl(__hip_atomic_load(ctl + CW_TMO, RLX_AGENT)) != 0;
}
DEV u32 wait_ge(GAS u32* w, u32 want, GAS u32* ctl) {
    u32 spins = 0, v;
    for (;;) { v = rfl(__hip_atomic_load(w, RLX_AGENT)); if (v >= want) break; __builtin_amdgcn_s_sleep(8); if (spin_fail(spins, ctl)) break; }
    asm volatile("" ::: "memory");
    return v;
}
template <int MODE> DEV int prog_load(int h, int lane, GAS u32* ctl) { return (int)__hip_atomic_load(ctl + CW_P + 16 * (MODE == 0 ? 2 * h + (lane & 1) : 64 + lane), RLX_AGENT); }
template <int MODE>
DEV int wait_upto(int v, int need, int h, int lane, GAS u32* ctl) {
    u32 spins = 0; int r;
    for (;;) {
        v = max(v, lane & 1);
        v = min(v, __builtin_amdgcn_update_dpp(64, v, 0x128, 0xf, 0xf, false)); v = min(v, __builtin_amdgcn_update_dpp(64, v, 0x124, 0xf, 0xf, false));
        v = min(v, __builtin_amdgcn_update_dpp(64, v, 0x122, 0xf, 0xf, false)); v = min(v, __builtin_amdgcn_update_dpp(64, v, 0x121, 0xf, 0xf, false));
        r = min(min(__builtin_amdgcn_readlane(v, 0), __builtin_amdgcn_readlane(v, 16)), min(__builtin_amdgcn_readlane(v, 32), __builtin_amdgcn_readlane(v, 48)));
        if (r > need) break;
        __builtin_amdgcn_s_sleep(8);
        if (spin_fail(spins, ctl)) { r = 64; break; }
        v = prog_load<MODE>(h, lane, ctl);
    }
    asm volatile("" ::: "memory");
    return r;
}
DEV u32 now32() { unsigned long long t; asm volatile("s_memtime %0\n s_waitcnt lgkmcnt(0)" : "=s"(t) :: "memory"); return (u32)t; }
DEV u32 gnow() { unsigned long long t; asm volatile("s_memrealtime %0\n s_waitcnt lgkmcnt(0)" : "=s"(t) :: "memory"); return (u32)t * 24u + 48u; }
DEV u32 wait_global(u32 T, int* slack = nullptr) {
    u32 off, e, d, d0;
    asm volatile(
        "s_memrealtime vcc\n s_memtime s[92:93]\n s_waitcnt lgkmcnt(0)\n"
        "s_mul_i32 %0, vcc_lo, 24\n s_sub_u32 %3, %4, %0\n s_sub_u32 %0, %0, s92\n s_cmp_gt_i32 %3, 150\n s_cbranch_scc0 4f\n"
        "1:\n"
        "s_memrealtime vcc\n s_memtime s[92:93]\n s_waitcnt lgkmcnt(0)\n"
        "s_mul_i32 %1, vcc_lo, 24\n s_sub_u32 %1, %1, s92\n"
        "s_sub_u32 %2, %1, %0\n s_cmp_gt_i32 %2, 0\n s_cselect_b32 %0, %1, %0\n"
        "s_sub_u32 %2, %4, s92\n s_sub_u32 %2, %2, %0\n"
        "s_cmp_gt_i32 %2, 700\n s_cbranch_scc0 2f\n s_cmp_gt_i32 %2, 0x40000\n s_cbranch_scc1 4f\n s_sleep 2\n s_branch 1b\n"
        "2:\n s_cmp_gt_i32 %2, 230\n s_cbranch_scc1 1b\n"
        "3:\n s_sub_u32 %2, %2, 32\n s_cmp_gt_i32 %2, 120\n s_cbranch_scc1 3b\n"
        "4:\n"
        : "=&s"(off), "=&s"(e), "=&s"(d), "=&s"(d0) : "s"(T) : "memory", "scc", "vcc", "s92", "s93");
    if (slack) *slack = (int)d0;
    return off;
}
#define RAWBAR() do { __builtin_amdgcn_s_barrier(); asm volatile("" ::: "memory"); } while (0)
DEV v2u ts_load(const Ctx& cx, int ad, int so) { return __builtin_amdgcn_raw_buffer_load_b64(cx.ar, ad, so, SC1); }
DEV int rowmax32(int d) {
    d = max(d, __builtin_amdgcn_update_dpp(0, d, 0x128, 0xf, 0xf, true)); d = max(d, __builtin_amdgcn_update_dpp(0, d, 0x124, 0xf, 0xf, true));
    d = max(d, __builtin_amdgcn_update_dpp(0, d, 0x122, 0xf, 0xf, true)); d = max(d, __builtin_amdgcn_update_dpp(0, d, 0x121, 0xf, 0xf, true));
    return max(__builtin_amdgcn_readlane(d, 0), __builtin_amdgcn_readlane(d, 16));
}
DEV u32 read_ref(v2u& v, const Ctx& cx, int ad, int so) {
    u32 spins = 0;
    while (__builtin_expect(__any(v.x == SENT || v.y == SENT), 0)) {
        if (spin_fail(spins, cx.ctl)) { v.y = 1600; break; }
        __builtin_amdgcn_s_sleep(2);
        asm volatile("" ::: "memory");
        v = ts_load(cx, ad, so);
    }
    const u32 b = rfl(v.x);
    return b + (u32)rowmax32((int)(v.x - b));
}

struct Pace { u32 p1; int c2, dur, x2, x3, relax, fails; bool sched; };
DEV void pace_next(Pace& q, v2u& tv, const Ctx& cx, int ad, int so, int s, const Params& p) {
    const int per = q.c2 + p.c3 + q.x3 + (q.dur >> 2);
    if (__builtin_expect(s > 0 && !__any(tv.x == SENT || tv.y == SENT || (int)(tv.x - q.p1) > p.gmax), 1)) {
        q.fails = max(q.fails - 1, 0);
        q.p1 += per;
        q.sched = true;
    } else {
        const u32 tref = read_ref(tv, cx, ad, so);
        const int g = (int)(tref - q.p1);
        const bool late = s == 0 || g > p.gmax;
        if (late && q.sched && s > 6) {
            q.fails += 2;
            if (q.fails >= 3 && q.relax < p.maxrel) { if (g > p.gmax + 650) q.x2 += 48; else q.x3 += 56; ++q.relax; q.fails = 0; }
        } else if (!late) q.fails = max(q.fails - 1, 0);
        q.p1 = late ? tref + per - (q.sched ? p.cmL : p.cmL - 400) : q.p1 + per;
        q.sched = !late;
    }
    q.c2 = p.g1 + q.x2 + q.dur;
}
DEV void pace_dur(Pace& q, v2u tv) { q.dur = rowmax32((int)tv.y); }

#define SMF_A(acc, a, w) asm volatile("v_smfmac_f32_16x16x64_bf16 %0, %1, %2, %3" : "+v"(acc) : "v"(a), "a"(w), "v"(sidx))
#define SMF_V(acc, a, w) asm volatile("v_smfmac_f32_16x16x64_bf16 %0, %1, %2, %3" : "+v"(acc) : "v"(a), "v"(w), "v"(sidx))
#define SMF_W(acc, a, i) do { if ((i) < 16) SMF_A(acc, a, wr[i]); else SMF_V(acc, a, wr[i]); } while (0)
#define MF_WA(acc, i, b) do { if ((i) < 32) asm volatile("v_mfma_f32_16x16x32_bf16 %0, %1, %2, %0" : "+v"(acc) : "a"(w4[i]), "v"(b)); \
                              else asm volatile("v_mfma_f32_16x16x32_bf16 %0, %1, %2, %0" : "+v"(acc) : "v"(w4[i]), "v"(b)); } while (0)
#define MF_WB(acc, a, i) do { if ((i) < 32) asm volatile("v_mfma_f32_16x16x32_bf16 %0, %1, %2, %0" : "+v"(acc) : "v"(a), "a"(w4[i])); \
                              else asm volatile("v_mfma_f32_16x16x32_bf16 %0, %1, %2, %0" : "+v"(acc) : "v"(a), "v"(w4[i])); } while (0)
#define LDS_RD128(dst, addr, off) asm volatile("ds_read_b128 %0, %1 offset:%2" : "=v"(dst) : "v"(addr), "i"(off) : "memory")
#define LGKM_WAIT(n, x) asm volatile("s_waitcnt lgkmcnt(%1)" : "+v"(x) : "i"(n) : "memory")
#define LGKM_WAIT2(n, x, y) asm volatile("s_waitcnt lgkmcnt(%2)" : "+v"(x), "+v"(y) : "i"(n) : "memory")
#define SIDX_DECL int sidx = (lane & 1) ? 0xEEEE : 0x4444; asm volatile("s_nop 1" : "+v"(sidx))
DEV float dmax16(float v) {
    asm volatile("s_nop 1\n v_max_f32_dpp %0, %0, %0 row_ror:8 row_mask:0xf bank_mask:0xf\n s_nop 1\n v_max_f32_dpp %0, %0, %0 row_ror:4 row_mask:0xf bank_mask:0xf\n"
                 "s_nop 1\n v_max_f32_dpp %0, %0, %0 row_ror:2 row_mask:0xf bank_mask:0xf\n s_nop 1\n v_max_f32_dpp %0, %0, %0 row_ror:1 row_mask:0xf bank_mask:0xf" : "+v"(v));
    return v;
}
DEV float max3f(float a, float b, float c) { float r; asm("v_max3_f32 %0, %1, %2, %3" : "=v"(r) : "v"(a), "v"(b), "v"(c)); return r; }
DEV f32x4 mfma16(v4u a, v4u b, f32x4 c) { return __builtin_amdgcn_mfma_f32_16x16x32_bf16(__builtin_bit_cast(bf16x8, a), __builtin_bit_cast(bf16x8, b), c, 0, 0, 0); }

template <int MODE, class Sink>
DEV void load_units(const Params& p, int h, unsigned char* lds, int wave, int lane, Sink sink) {
    constexpr int NU = MODE == 3 ? 10 : 20, NR = (NU + 3) / 4;
    const u32 lds0 = (u32)(size_t)(LAS unsigned char*)lds;
    const int tp = lane >> 3, q4 = (lane >> 1) & 3, hf = lane & 1;
    auto issue = [&](int r) {
        const int u = r * 4 + (wave >> 1), hh = wave & 1;
        if (u >= NU) return;
        bool second; int base, stride, map;
        if (MODE <= 1) { second = u & 1; base = (64 * (u >> 1) * HD + h * DH) * 2; stride = HD * 2; map = MODE; }
        else if (MODE == 2 && u < 10) { second = false; base = (64 * u * HD + h * DH) * 2; stride = HD * 2; map = 1; }
        else { const int uo = MODE == 2 ? u - 10 : u; second = true; base = ((h * DH + 64 * (uo & 1)) * DM + (uo >> 1) * 128) * 2; stride = DM * 2; map = MODE == 2 ? 2 : 1; }
        const int gq = map == 0 ? 8 * (q4 >> 1) + (q4 & 1) : q4;
        const rsrc_t rs = mkrs(second ? (MODE <= 1 ? p.Wk : p.Wo) : (MODE <= 1 ? p.Wq : p.Wv), DM * HD * 2);
#pragma unroll
        for (int i = 0; i < 8; ++i) {
            const int gB = i >> 1, i4 = i & 1;
            const int f = 32 * hh + (map == 0 ? 16 * (gB >> 1) + 4 * (gB & 1) + 2 * i4 : map == 1 ? 8 * gB + 4 * i4 : 16 * i4 + 4 * gB);
            const int voff = gq * stride + ((tp ^ (gB & 1)) * 16 + hf * 8) * 2;
            const u32 la = rfl(lds0 + L_STAGE + (r & 1) * 65536 + (wave >> 1) * 16384 + (hh * 8 + i) * 1024);
            dma16(la, voff, rs, rfl(base + f * stride));
        }
    };
    const int gB = lane >> 4;
    const int rd0 = (2 * gB) * 1024 + ((lane & 15) >> 2) * 32 + (lane & 3) * 8;
    issue(0);
#pragma unroll
    for (int r = 0; r < NR; ++r) {
        if (r + 1 < NR) { issue(r + 1); if ((r + 1) * 4 + 3 < NU) VMCNT(8); else VMCNT(0); } else if (MODE != 3) VMCNT(0);
        BAR();
#pragma unroll
        for (int k = 0; k < 4; ++k) {
            int uu, tl;
            if (MODE == 1) { uu = (k >> 1) * 2 + (wave >> 2); tl = 2 * (wave & 3) + (k & 1); } else { uu = k; tl = wave; }
            if (MODE != 1 && r * 4 + k >= NU) continue;
            v2u t[4];
#pragma unroll
            for (int j = 0; j < 4; ++j) {
                s16x4 v = __builtin_amdgcn_ds_read_tr16_b64_v4i16((LAS s16x4*)(lds + L_STAGE + (r & 1) * 65536 + uu * 16384 + ((j >> 1) * 8 + (j & 1)) * 1024 + rd0 + (tl ^ (gB & 1)) * 128));
                t[j] = __builtin_bit_cast(v2u, v);
            }
            sink(r * 4 + k, t);
        }
        BAR();
    }
}

constexpr u32 POLLCAP = 1u << 17;
#define POLL_COUNT "s_add_u32 %[cnt], %[cnt], 1\n s_cmp_lt_u32 %[cnt], %[cap]\n s_cselect_b32 %[key], %[key], -1\n"
DEV void gather_mem(const Ctx& cx, int gl, int s0, int s1, u32 ldst, u32& key) {
    u32 cnt = 0;
    asm volatile(
        "s_barrier\n"
        "1:\n"
        "buffer_load_dwordx4 v[116:119], %[gl], %[rs], %[s0] offen sc1\n"
        "buffer_load_dwordx4 v[120:123], %[gl], %[rs], %[s1] offen sc1\n"
        POLL_COUNT
        "s_waitcnt vmcnt(1)\n"
        "ds_write_b128 %[ld], v[116:119]\n"
        "v_max3_u32 v124, v116, v117, v118\n"
        "s_waitcnt vmcnt(0)\n"
        "ds_write_b128 %[ld], v[120:123] offset:1024\n"
        "v_max3_u32 v124, v124, v119, v120\n"
        "v_max3_u32 v124, v124, v121, v122\n"
        "v_max_u32 v124, v124, v123\n"
        "v_cmp_lt_u32 vcc, %[key], v124\n"
        "s_waitcnt lgkmcnt(0)\n"
        "s_cbranch_vccnz 1b\n"
        "s_barrier\n"
        : [cnt] "+s"(cnt), [key] "+s"(key)
        : [gl] "v"(gl), [rs] "s"(cx.ar), [s0] "s"(s0), [s1] "s"(s1), [ld] "v"(ldst), [cap] "s"(POLLCAP)
        : "memory", "scc", "vcc", "v116", "v117", "v118", "v119", "v120", "v121", "v122", "v123", "v124");
}
DEV void gather_mem4(const Ctx& cx, int gl, int s0, int s1, int s2, unsigned long long half, u32 ldst, u32 ldst2, u32& key) {
    u32 cnt = 0;
    asm volatile(
        "v_mov_b32 v112, 0\n v_mov_b32 v113, 0\n v_mov_b32 v114, 0\n v_mov_b32 v115, 0\n"
        "s_barrier\n"
        "1:\n"
        "buffer_load_dwordx4 v[116:119], %[gl], %[rs], %[s0] offen sc1\n"
        "buffer_load_dwordx4 v[120:123], %[gl], %[rs], %[s1] offen sc1\n"
        "s_mov_b64 exec, %[hf]\n"
        "buffer_load_dwordx4 v[112:115], %[gl], %[rs], %[s2] offen sc1\n"
        "s_mov_b64 exec, -1\n"
        POLL_COUNT
        "s_waitcnt vmcnt(2)\n"
        "ds_write_b128 %[ld], v[116:119]\n"
        "v_max3_u32 v124, v116, v117, v118\n"
        "s_waitcnt vmcnt(1)\n"
        "ds_write_b128 %[ld], v[120:123] offset:1024\n"
        "v_max3_u32 v124, v124, v119, v120\n"
        "v_max3_u32 v124, v124, v121, v122\n"
        "s_waitcnt vmcnt(0)\n"
        "ds_write_b128 %[ld2], v[112:115]\n"
        "v_max3_u32 v124, v124, v123, v112\n"
        "v_max3_u32 v124, v124, v113, v114\n"
        "v_max_u32 v124, v124, v115\n"
        "v_cmp_lt_u32 vcc, %[key], v124\n"
        "s_waitcnt lgkmcnt(0)\n"
        "s_cbranch_vccnz 1b\n"
        "s_barrier\n"
        : [cnt] "+s"(cnt), [key] "+s"(key)
        : [gl] "v"(gl), [rs] "s"(cx.ar), [s0] "s"(s0), [s1] "s"(s1), [s2] "s"(s2), [hf] "s"(half), [ld] "v"(ldst), [ld2] "v"(ldst2), [cap] "s"(POLLCAP)
        : "memory", "scc", "vcc", "v112", "v113", "v114", "v115", "v116", "v117", "v118", "v119", "v120", "v121", "v122", "v123", "v124");
}
DEV void load_mem0(const u16* mem0, unsigned char* lds, int tid) {
    for (int i = tid; i < 640; i += 512) { const int m = i / 80, c8 = i % 80; *(v4u*)(lds + L_MEMF + (c8 >> 1) * 256 + (2 * m + (c8 & 1)) * 16) = *(const v4u*)(mem0 + m * DM + c8 * 8); }
}

template <int CLS>
DEV void a_role(const Params& p, const Ctx& cx, unsigned char* lds, int h, int wave) {
    v8u wr[20];
    load_units<0>(p, h, lds, wave, lane_id(), [&](int slot, v2u (&t)[4]) { wr[slot] = (v8u){t[0].x, t[0].y, t[1].x, t[1].y, t[2].x, t[2].y, t[3].x, t[3].y}; });
    load_mem0(p.mem0, lds, wave * 64 + lane_id());
    for (int i = wave * 64 + lane_id(); i < 1024; i += 512) *(u32*)(lds + L_KMF + i * 4) = 0;
    const int par = cx.par, far = par ^ 1;
    EVT(2);
    const int lane = lane_id(), n = lane & 15, gq = lane >> 4;
    SIDX_DECL;
    const u32 lbase = (u32)(size_t)(LAS unsigned char*)lds + lane * 16;
    const int gl = lane * 16;
    const int qw = L_QF + (wave >> 2) * 1024 + (2 * ((wave >> 1) & 1) + (n >> 3)) * 256 + (2 * gq) * 32 + ((n >> 1) & 1) * 16 + (4 * ((n >> 2) & 1) + 2 * (wave & 1) + (n & 1)) * 2;
    const int kw = L_KMF + (wave >> 1) * 1024 + (16 * (n >> 2) + gq) * 16 + (4 * (wave & 1) + (n & 3)) * 2;
    const int pair = 8 * (n >> 3) + 2 * (n & 3) + ((n >> 2) & 1);
    const int tk0 = 2 * pair, tk1 = 2 * pair + 1;
    const u32 lq = (u32)(size_t)(LAS unsigned char*)lds + gq * 256 + (4 * (wave & 1) + (n >> 2)) * 32 + (n & 1) * 16;
    const int adpx = (n >> 3) * 256 + gq * 32 + (n & 7) * 4, upx = PX_O + h * 512 + (wave & 1) * 128;
    const int adpm = gq * 16 + (n & 3) * 4, upm = PM_O + h * 128 + (wave & 1) * 64;
    const int g8 = (wave - 2) & 3;
    const int t0 = 8 * g8 + gq;
    const int ql = gq * 256 + (((n >> 1) & 1) * 4 + (n >> 2)) * 32 + (n & 1) * 16;
    const int adtx = (n >> 3) * 256 + gq * 32 + (n & 7) * 4, utx = (g8 * 32 + h) * 512;
    const int adtm = gq * 16 + (n & 1) * 8 + ((n >> 1) & 1) * 4, utm = 65536 + (g8 * 32 + h) * 128;
    const float SC = 0.08838834764831845f * 1.4426950408889634f;
    const float NEGR = -1.0e9f / 0.08838834764831845f;
    const float pad0 = (n < 8) ? 0.f : -__builtin_inff();
    const bool pmlane = n < 4;
    const int adts = (lane & 31) * 64;
    int ready = 0;
    v4u qx0, qx1, qn0, qn1;
    f32x4 s1, s2;
    auto tok_load = [&](int sn) {
        const int qb = rfl(amap(QX_L + (sn * H + h) * 8192 + g8 * 2048, par));
        qn0 = ld128(cx.ar, ql, qb); qn1 = ld128(cx.ar, ql + 1024, qb);
    };
    auto tok_prep = [&](int sn) {
        qx0 = qn0; qx1 = qn1;
        v4u kx[8];
#pragma unroll
        for (int i = 0; i < 8; ++i) kx[i] = *(const v4u*)(lds + L_KXF + (sn & 1) * 8192 + i * 1024 + lane * 16);
        s1 = (f32x4){0, 0, 0, 0}; s2 = s1;
        asm volatile("s_nop 1" : "+v"(s1), "+v"(s2), "+v"(qx0), "+v"(qx1));
        SMF_V(s1, qx0, cat8(kx[0], kx[1]));
        SMF_V(s2, qx0, cat8(kx[4], kx[5]));
        SMF_V(s1, qx1, cat8(kx[2], kx[3]));
        SMF_V(s2, qx1, cat8(kx[6], kx[7]));
        asm volatile("s_nop 7" : "+v"(s1), "+v"(s2));
        s1.x = (tk0 <= t0) ? s1.x + s1.y : NEGR; s1.z = (tk0 <= t0 + 4) ? s1.z + s1.w : NEGR;
        s1.y = (tk1 <= t0) ? s2.x + s2.y : NEGR; s1.w = (tk1 <= t0 + 4) ? s2.z + s2.w : NEGR;
    };
    v4u kt[8], k1 = {0, 0, 0, 0};
    auto help_load = [&](int sn) {
        const int kb = rfl(amap(KX_L + (sn * H + h) * 8192, par));
#pragma unroll
        for (int i = 0; i < 8; ++i) kt[i] = ld128(cx.ar, gl + (i & 3) * 1024, kb + (i >> 2) * 4096);
    };
    auto help_store = [&](int sn) {
#pragma unroll
        for (int i = 0; i < 8; ++i) *(v4u*)(lds + L_KXF + (sn & 1) * 8192 + i * 1024 + lane * 16) = kt[i];
    };
    if (CLS == 2) {
        ready = wait_upto<0>(prog_load<0>(h, lane, cx.ctl), wave - 6, h, lane, cx.ctl);
        help_load(wave - 6); help_store(wave - 6);
    }
    BAR();
    if (CLS == 1) { tok_load(0); tok_prep(0); }
    Pace pc = {0u, p.g1 + 1640, 1640, 0, 0, 0, 0, false};
    u32 off = gnow() - now32(), key = 0xFFFFFFFEu;
    u32 dur = 1640, offp = 0, tp = 0;
    float slow = 0.04f;
    bool okoff = false, okp = false;
    const int gw = wave < 2 ? wave : wave - 4;
    const u32 gdst = (u32)(size_t)(LAS unsigned char*)lds + L_MEMF + gw * 2048 + gq * 256 + (n & 7) * 32 + (n >> 3) * 16;
    const u32 gdst2 = ((lane >> 5) == (gw & 1)) ? gdst - gw * 2048 + 8192 + (gw >> 1) * 1024 : (u32)(size_t)(LAS unsigned char*)lds + L_DUMP + lane * 16;
    for (int s = 0; s < NSEG; ++s) {
        const int sb = s * SLOT;
        f32x4 aq = {0, 0, 0, 0}, ak = {0, 0, 0, 0};
        asm volatile("" : "+v"(aq), "+v"(ak));
        const int xpx = rfl(amap(sb + upx, par)), xpm = rfl(amap(sb + upm, par)), xts = rfl(amap(sb + TS_O, par));
        const int xk1 = rfl(amap(KX_L + ((s == 0 ? 0 : min(s + 2, NSEG - 1)) * H + h) * 8192 + wave * 1024, par));
        if (s > 0) {
            const int x0 = rfl(amap(sb + MEM_O + gw * 2048, par)), x2 = rfl(amap(sb + MEM_O + 8192 + (gw >> 1) * 1024, par));
            asm volatile("s_waitcnt lgkmcnt(0)" :: "s"(x0), "s"(x2), "s"(xpx), "s"(xpm), "s"(xts), "s"(xk1) : "memory");
            DT(d_arr); DV(7, d_arr);
            asm volatile("s_waitcnt lgkmcnt(0)" ::: "memory");
            if (CLS == 2 && wave == 7) {
                int slack;
                off = wait_global(pc.p1, &slack);
                okoff = slack > 500;
            }
            if (CLS != 1) gather_mem4(cx, gl, x0, x0 + 1024, x2, (gw & 1) ? 0xFFFFFFFF00000000ull : 0xFFFFFFFFull, gdst, gdst2, key);
            else { RAWBAR(); RAWBAR(); }
        } else BAR();
        if (CLS == 2 && wave == 7) { const u32 ts = min(now32() + off, 0xFFFFFFFEu); if (lane == 0) __builtin_amdgcn_raw_buffer_store_b64((v2u){ts, dur}, cx.ar, h * 64, xts, SC1); }
        {
            v4u a_[5];
#pragma unroll
            for (int ks = 0; ks < 5; ++ks) LDS_RD128(a_[ks], lbase, L_MEMF + ks * 1024);
#pragma unroll
            for (int ks = 0; ks < 5; ++ks) {
                if (ks == (wave < 4 ? 3 : 0)) {
                    if (CLS == 1) tok_load(min(s + 1, NSEG - 1));
                    k1 = ld128(cx.ar, gl, xk1);
                }
                LGKM_WAIT(4, a_[ks]); SMF_W(aq, a_[ks], 2 * ks); SMF_W(ak, a_[ks], 2 * ks + 1); LDS_RD128(a_[ks], lbase, L_MEMF + (ks + 5) * 1024);
            }
#pragma unroll
            for (int ks = 5; ks < 10; ++ks) { LGKM_WAIT(9 - ks, a_[ks - 5]); SMF_W(aq, a_[ks - 5], 2 * ks); SMF_W(ak, a_[ks - 5], 2 * ks + 1); }
        }
        v4u kx[8];
        if (CLS == 0) {
            const u32 lk = lbase + (s & 1) * 8192;
#pragma unroll
            for (int i = 0; i < 8; ++i) LDS_RD128(kx[i], lk, L_KXF + i * 1024);
        } else asm volatile("s_nop 7");
        asm volatile("" : "+v"(aq), "+v"(ak));
        {
            const u32 q2 = pk2(aq.x + aq.y, aq.z + aq.w), k2 = pk2(ak.x + ak.y, ak.z + ak.w);
            *(u16*)(lds + qw) = (u16)q2; *(u16*)(lds + qw + 32) = (u16)(q2 >> 16);
            *(u16*)(lds + kw) = (u16)k2; *(u16*)(lds + kw + 64) = (u16)(k2 >> 16);
        }
        f32x4 s0 = {pad0, 0, pad0, 0};
        if (CLS == 0) { s1 = (f32x4){0, 0, 0, 0}; s2 = s1; asm volatile("" : "+v"(s0), "+v"(s1), "+v"(s2) : "s"(xpx), "s"(xpm)); }
        if (CLS == 1) asm volatile("" : "+v"(s0));
        BAR();
        if (CLS == 0) {
            v4u a0, a1, km[4];
            LDS_RD128(a0, lq, L_QF); LDS_RD128(a1, lq, L_QF + 1024);
#pragma unroll
            for (int i = 0; i < 4; ++i) LDS_RD128(km[i], lbase, L_KMF + i * 1024);
            LGKM_WAIT(5, a0);
            SMF_V(s1, a0, cat8(kx[0], kx[1]));
            SMF_V(s2, a0, cat8(kx[4], kx[5]));
            LGKM_WAIT(4, a1);
            SMF_V(s1, a1, cat8(kx[2], kx[3]));
            SMF_V(s2, a1, cat8(kx[6], kx[7]));
            LGKM_WAIT2(2, km[0], km[1]);
            SMF_V(s0, a0, cat8(km[0], km[1]));
            LGKM_WAIT2(0, km[2], km[3]);
            SMF_V(s0, a1, cat8(km[2], km[3]));
            asm volatile("s_nop 4" : "+v"(s1), "+v"(s2));
            float x1 = s1.x + s1.y, x2 = s2.x + s2.y;
            asm volatile("s_nop 0" : "+v"(s0), "+v"(x1), "+v"(x2));
            const float x0 = s0.x + s0.y;
            const float mx = -SC * dmax16(max3f(x0, x1, x2));
            const float e0 = __builtin_amdgcn_exp2f(__builtin_fmaf(x0, SC, mx)), e1 = __builtin_amdgcn_exp2f(__builtin_fmaf(x1, SC, mx)), e2 = __builtin_amdgcn_exp2f(__builtin_fmaf(x2, SC, mx));
            const float inv = __builtin_amdgcn_rcpf(rowsum16(e0 + e1 + e2));
            const float p0 = e0 * inv;
            st32(pk2s(e1 * inv, e2 * inv), cx.ar, adpx, xpx);
            const u32 pm = pk2s(p0, dppf<0x104>(p0));
            if (pmlane) st32(pm, cx.ar, adpm, xpm);
            DT(d_pub); DV(3, d_pub);
        } else if (CLS == 1) {
            __builtin_amdgcn_s_sleep(4);
            if (s > 0) *(v4u*)(lds + L_KXF + (s & 1) * 8192 + wave * 1024 + lane * 16) = k1;
            asm volatile("" : "+v"(qn0), "+v"(qn1) :: "memory");
            v4u km[4];
#pragma unroll
            for (int i = 0; i < 4; ++i) km[i] = *(const v4u*)(lds + L_KMF + i * 1024 + lane * 16);
            SMF_V(s0, qx0, cat8(km[0], km[1]));
            SMF_V(s0, qx1, cat8(km[2], km[3]));
            asm volatile("s_nop 7" : "+v"(s0));
            const int xtx = rfl(amap(PT_L + s * PT_STEP + utx, far)), xtm = rfl(amap(PT_L + s * PT_STEP + utm, far));
#pragma unroll
            for (int rr = 0; rr < 2; ++rr) {
                const float x0 = rr ? s0.z + s0.w : s0.x + s0.y, x1 = rr ? s1.z : s1.x, x2 = rr ? s1.w : s1.y;
                const float mx = -SC * dmax16(max3f(x0, x1, x2));
                const float e0 = __builtin_amdgcn_exp2f(__builtin_fmaf(x0, SC, mx)), e1 = __builtin_amdgcn_exp2f(__builtin_fmaf(x1, SC, mx)), e2 = __builtin_amdgcn_exp2f(__builtin_fmaf(x2, SC, mx));
                const float inv = __builtin_amdgcn_rcpf(rowsum16(e0 + e1 + e2));
                const float p0 = e0 * inv;
                st32(pk2(e1 * inv, e2 * inv), cx.ar, adtx + rr * 128, xtx);
                const u32 pm = pk2(p0, dppf<0x104>(p0));
                if (pmlane) st32(pm, cx.ar, adtm + rr * 64, xtm);
            }
            if (s + 1 < NSEG) tok_prep(s + 1);
        } else {
            if (wave == 6) {
                if (s == 0) { ready = wait_upto<0>(prog_load<0>(h, lane, cx.ctl), 2, h, lane, cx.ctl); help_load(2); help_store(2); }
                if (s + 3 >= ready && ready < NSEG) ready = wait_upto<0>(prog_load<0>(h, lane, cx.ctl), min(s + 3, NSEG - 1), h, lane, cx.ctl);
            } else __builtin_amdgcn_s_sleep(6);
        }
        if (CLS != 1 && s > 0) *(v4u*)(lds + L_KXF + (s & 1) * 8192 + wave * 1024 + lane * 16) = k1;
        if (CLS == 2 && wave == 7) {
            if (okoff) {
                if (okp) slow = 0.75f * slow + 0.25f * fminf(fmaxf((float)(int)(off - offp) / (float)(int)(pc.p1 - tp), 0.f), 0.4f);
                offp = off; tp = pc.p1; okp = true;
                dur = rfl((int)(A_CYCLES / (1.f - slow)));
            }
            v2u tv = ts_load(cx, adts, xts);
            pace_next(pc, tv, cx, adts, xts, s, p);
            pace_dur(pc, tv);
        }
    }
    if (key == SENT) __hip_atomic_store(cx.ctl + CW_TMO, 1u, RLX_AGENT);
    VMCNT(0);
    BAR();
    if (CLS == 2 && wave == 7 && lane == 0) __hip_atomic_store(cx.ctl + CW_FIN + 16 * h, 1u, RLX_AGENT);
    DFLUSH();
}

template <bool TW>
DEV void r_role(const Params& p, const Ctx& cx, unsigned char* lds, int c8, int wave) {
    const int lane = lane_id(), n = lane & 15, gq = lane >> 4;
    const int par = cx.par;
    v8u wr[20];
    {
        const int vb = (((n >> 3) * 20) * 40) * 1024 + (16 * gq + (n & 7)) * 16;
        const int vs = VO_P + ((4 * wave * 20) * 40 + (c8 >> 1)) * 1024 + (c8 & 1) * 128;
#pragma unroll
        for (int i = 0; i < 20; ++i) {
            const int o = vs + (i & 1) * (2 * 20 * 40960) + (i >> 1) * (2 * 40960);
            wr[i] = cat8(__builtin_amdgcn_raw_buffer_load_b128(cx.ar, vb, o, 0), __builtin_amdgcn_raw_buffer_load_b128(cx.ar, vb, o + 40960, 0));
        }
    }
    load_mem0(p.mem0, lds, wave * 64 + lane);
    EVT(3);
    SIDX_DECL;
    const u32 lbase = (u32)(size_t)(LAS unsigned char*)lds + lane * 16;
    const int gl = lane * 16;
    const int zl = gq * 128 + (n & 7) * 16;
    const u32 ztw = (u32)(size_t)(LAS unsigned char*)lds + L_ZT + wave * 512 + (n >> 3) * 128 + (n & 7) * 16 + gq * 4, ztr = (u32)(size_t)(LAS unsigned char*)lds + L_ZT + wave * 512 + zl;
    const int pml = gq * 128 + (n >> 1) * 16;
    const u32 redw = (u32)(size_t)(LAS unsigned char*)lds + L_RED + (n >> 3) * 2560 + ((2 * gq) * 4 + ((n & 7) >> 1)) * 80 + wave * 8 + (n & 1) * 4;
    const u32 gdst = (u32)(size_t)(LAS unsigned char*)lds + L_MEMF + wave * 2048 + gq * 256 + (n & 7) * 32 + (n >> 3) * 16;
    u32 key = 0xFFFFFFFEu;
    const int redr = L_RED + (lane & 31) * 80;
    const int adts = (lane & 31) * 64;
    int ready = 0;
    if (!TW && wave == 6) ready = wait_upto<1>(prog_load<1>(0, lane, cx.ctl), 1, 0, lane, cx.ctl);
    BAR();
    v4u zn0 = ld128(cx.ar, gl, ZX_P + c8 * 16384 + wave * 2048), zn1 = ld128(cx.ar, gl + 1024, ZX_P + c8 * 16384 + wave * 2048);
    Pace pc = {0u, p.g1 + 1640, 1640, 0, 0, 0, 0, false};
    v2u tv = {0, 1640};
    for (int s = 0; s < NSEG; ++s) {
        const int sb = s * SLOT;
        f32x4 z0 = {0, 0, 0, 0}, z1 = z0;
        asm volatile("" : "+v"(z0), "+v"(z1));
        const int x0 = rfl(amap(sb + MEM_O + wave * 2048, par)), x1 = rfl(amap(sb + MEM_O + wave * 2048 + 1024, par));
        const int xzx = ZX_P + min(s + 1, NSEG - 1) * ZX_STEP + c8 * 16384 + wave * 2048;
        v8u zb0 = cat8(zn0, zn1), zb1;
#pragma unroll
        for (int i = 0; i < 8; ++i) zb1[i] = __builtin_amdgcn_update_dpp(0, zb0[i], 0x108, 0xf, 0xf, true);
        asm volatile("" : "+v"(zb0), "+v"(zb1) : "s"(x0), "s"(x1), "s"(xzx));
        if (TW && s > 0) wait_global(pc.p1 + p.pad);
        int pv = 64;
        if (s > 0 && wave < 5) gather_mem(cx, gl, x0, x1, gdst, key);
        else {
            BAR();
            if (!TW && wave == 6 && s + 2 >= ready) { __builtin_amdgcn_s_sleep(4); pv = prog_load<1>(0, lane, cx.ctl); }
            RAWBAR();
        }
        const int xts = rfl(amap(sb + TS_O, par));
        bool late = false;
        DT(d_b1);
        {
            v4u a_[5];
#pragma unroll
            for (int ks = 0; ks < 5; ++ks) LDS_RD128(a_[ks], lbase, L_MEMF + ks * 1024);
            zn0 = ld128(cx.ar, gl, xzx); zn1 = ld128(cx.ar, gl + 1024, xzx);
#pragma unroll
            for (int ks = 0; ks < 5; ++ks) { LGKM_WAIT(4, a_[ks]); SMF_W(z0, a_[ks], 2 * ks); SMF_W(z1, a_[ks], 2 * ks + 1); LDS_RD128(a_[ks], lbase, L_MEMF + (ks + 5) * 1024); }
#pragma unroll
            for (int ks = 5; ks < 10; ++ks) { LGKM_WAIT(9 - ks, a_[ks - 5]); SMF_W(z0, a_[ks - 5], 2 * ks); SMF_W(z1, a_[ks - 5], 2 * ks + 1); }
        }
        const int xzm = rfl(amap(ZM_L + s * ZM_STEP + c8 * 4096 + wave * 512, par));
        const int xpub = rfl(amap(sb + SLOT + MEM_O + c8 * 128, par));
        const int xx0 = rfl(amap(sb + PX_O + wave * 2048, par)), xx1 = rfl(amap(sb + PX_O + wave * 2048 + 1024, par)), xm = rfl(amap(sb + PM_O + wave * 512, par));
        asm volatile("s_nop 7" : "+v"(z0), "+v"(z1) : "s"(xx0), "s"(xx1), "s"(xm), "s"(xts), "s"(xpub), "s"(xzm));
        const u32 zt0 = pk2(z0.x + z0.y, z0.z + z0.w), zt1 = pk2(z1.x + z1.y, z1.z + z1.w);
        if (s + 1 == NSEG) {
            *(LAS u32*)(size_t)ztw = zt0; *(LAS u32*)(size_t)(ztw + 256) = zt1;
            asm volatile("s_waitcnt lgkmcnt(0)" ::: "memory");
            if (n < 8) st128(*(const LAS v4u*)(size_t)ztr, cx.ar, zl, xzm);
            break;
        }
        DT(d_zp);
        u32 dsp = 0;
        DV(6, pc.p1);
        DT(d_arr); DV(7, d_arr);
        if (TW) {
            late = s == 0 || (int)(gnow() - pc.p1) > p.glate;
            pace_dur(pc, tv);
            tv = ts_load(cx, adts, xts);
            if (!late) {
                wait_global(pc.p1 + pc.c2 - 260);
                if ((__builtin_amdgcn_s_getreg(7 | (31 << 11)) & 0x00C0000F) == 0) late = __any(tv.x == SENT || (int)(tv.x - pc.p1) > p.g1 - 30);
            }
            wait_global(late ? read_ref(tv, cx, adts, xts) + pc.c2 - p.cp : pc.p1 + pc.c2);
        }
        {
            u32 cnt = 0;
            asm volatile(
                "v_mov_b32 v116, 0\n v_mov_b32 v117, 0\n v_mov_b32 v118, 0\n v_mov_b32 v119, 0\n"
                "s_barrier\n"
                "1:\n"
                "buffer_load_dwordx4 v[108:111], %[gl], %[rs], %[x0] offen sc1\n"
                "buffer_load_dwordx4 v[112:115], %[gl], %[rs], %[x1] offen sc1\n"
                "s_mov_b64 exec, %[ev]\n"
                "buffer_load_dwordx4 v[116:119], %[pml], %[rs], %[xm] offen sc1\n"
                "s_mov_b64 exec, -1\n"
                "ds_write2st64_b32 %[zw], %[zt0], %[zt1] offset1:1\n"
                POLL_COUNT
                "s_waitcnt lgkmcnt(0)\n"
                "ds_read_b128 v[104:107], %[zr]\n"
                "v_mov_b32 v120, 0\n v_mov_b32 v121, 0\n v_mov_b32 v122, 0\n v_mov_b32 v123, 0\n"
                "s_mov_b64 exec, %[lo8]\n"
                "s_waitcnt lgkmcnt(0)\n"
                "buffer_store_dwordx4 v[104:107], %[zl], %[rs], %[xz] offen sc1\n"
                "s_mov_b64 exec, -1\n"
                "s_waitcnt vmcnt(3)\n"
                "v_smfmac_f32_16x16x64_bf16 v[120:123], v[108:111], %[zb0], %[si]\n"
                "v_max3_u32 v124, v108, v109, v110\n"
                "s_waitcnt vmcnt(2)\n"
                "v_smfmac_f32_16x16x64_bf16 v[120:123], v[112:115], %[zb1], %[si]\n"
                "v_max3_u32 v124, v124, v111, v112\n"
                "v_max3_u32 v124, v124, v113, v114\n"
                "s_waitcnt vmcnt(1)\n"
                "v_mfma_f32_16x16x32_bf16 v[120:123], v[116:119], v[104:107], v[120:123]\n"
                "v_max3_u32 v124, v124, v115, v116\n"
                "v_max3_u32 v124, v124, v117, v118\n"
                "v_max_u32 v124, v124, v119\n"
                "v_cmp_lt_u32 vcc, %[key], v124\n"
                "s_nop 2\n"
                "s_cbranch_vccnz 1b\n"
                "v_add_f32 v124, v120, v121\n"
                "v_add_f32 v125, v122, v123\n"
                "ds_write2_b32 %[rw], v124, v125 offset1:80\n"
                "s_waitcnt lgkmcnt(0)\n"
                "s_barrier\n"
                : [cnt] "+s"(cnt), [key] "+s"(key)
                : [gl] "v"(gl), [pml] "v"(pml), [rs] "s"(cx.ar), [x0] "s"(xx0), [x1] "s"(xx1), [xm] "s"(xm), [xz] "s"(xzm), [ev] "s"(0x5555555555555555ull), [lo8] "s"(0x00FF00FF00FF00FFull),
                  [cap] "s"(POLLCAP), [zb0] "v"(zb0), [zb1] "v"(zb1), [si] "v"(sidx), [rw] "v"(redw), [zw] "v"(ztw), [zr] "v"(ztr), [zl] "v"(zl), [zt0] "v"(zt0), [zt1] "v"(zt1)
                : "memory", "scc", "vcc", "v104", "v105", "v106", "v107", "v108", "v109", "v110", "v111", "v112", "v113", "v114", "v115", "v116", "v117", "v118", "v119", "v120", "v121", "v122", "v123", "v124", "v125");
            dsp = cnt - 1;
        }
        if (!TW && wave == 7) {
            const unsigned char* rb = lds + redr;
            f32x4 r0 = *(const f32x4*)(rb), r1 = *(const f32x4*)(rb + 16), r2 = *(const f32x4*)(rb + 32), r3 = *(const f32x4*)(rb + 48);
            r0 = (r0 + r1) + (r2 + r3);
            if (lane < 32) st32(pk2s(r0.x + r0.z, r0.y + r0.w), cx.ar, lane * 4, xpub);
        }
        DT(d_pub);
        DV(0, d_b1); DV(1, d_zp); DV(2, d_pub); DV(3, d_pub); DV(4, late); DV(5, dsp);
        if (TW) pace_next(pc, tv, cx, adts, xts, s, p);
        if (!TW && wave == 6 && s + 2 >= ready && ready < NSEG) ready = wait_upto<1>(pv, min(s + 2, NSEG - 1), 0, lane, cx.ctl);
    }
    if (key == SENT) __hip_atomic_store(cx.ctl + CW_TMO, 1u, RLX_AGENT);
    VMCNT(0);
    BAR();
    if (!TW && wave == 6 && lane == 0) __hip_atomic_store(cx.ctl + CW_FIN + 16 * (32 + c8), 1u, RLX_AGENT);
    DFLUSH();
}

constexpr int L_XT = L_STAGE, L_VI = 0;
template <int TYPE>
DEV void l_worker(const Params& p, const Ctx& cx, unsigned char* lds, int h, int second, int wave) {
    constexpr int K1 = TYPE == 0 ? 3 : 10;
    v4u w4[40];
    if (TYPE == 0) load_units<1>(p, h, lds, wave, lane_id(), [&](int slot, v2u (&t)[4]) {
        const int ks = slot >> 1, tau = slot & 1;
        w4[(2 * ks) * 2 + tau] = (v4u){t[0].x, t[0].y, t[1].x, t[1].y}; w4[(2 * ks + 1) * 2 + tau] = (v4u){t[2].x, t[2].y, t[3].x, t[3].y}; });
    else load_units<2>(p, h, lds, wave, lane_id(), [&](int slot, v2u (&t)[4]) {
        w4[2 * slot] = (v4u){t[0].x, t[0].y, t[1].x, t[1].y}; w4[2 * slot + 1] = (v4u){t[2].x, t[2].y, t[3].x, t[3].y}; });
    EVT(1);
    const int lane = lane_id();
    const u32 lds0 = (u32)(size_t)(LAS unsigned char*)lds;
    const int i = lane & 15, gq = lane >> 4;
    const rsrc_t rxin = mkrs(p.x, S * DM * 2);
    const int xrow = (wave & 3) * 8 + (lane >> 3);
    const int xvo = xrow * (DM * 2) + (((lane & 7) ^ ((xrow >> 1) & 7)) * 16) + (5 * (wave >> 2)) * 128;
    const u32 xld = lds0 + L_XT + (5 * (wave >> 2)) * 4096 + (wave & 3) * 1024;
    auto issue1 = [&](int s, int buf, int k) { dma16(rfl(xld + buf * 40960 + k * 4096), xvo, rxin, rfl(min(s, NSEG - 1) * (32 * DM * 2) + k * 128)); };
    const int nit = second ? K1 : NSEG - K1;
    auto seg_of = [&](int it) { return second ? (it < K1 ? 2 * it + 1 : NSEG) : it + min(it, K1); };
#pragma unroll
    for (int k = 0; k < 5; ++k) issue1(seg_of(0), 0, k);
#pragma unroll
    for (int k = 0; k < 5; ++k) issue1(seg_of(1), 1, k);
    const int sel = cx.par;
    const u32 xb0 = lds0 + L_XT + i * 128 + ((gq ^ (i >> 1)) * 16), xb1 = lds0 + L_XT + i * 128 + (((4 + gq) ^ (i >> 1)) * 16);
    int ost0, ost1, ubase;
    if (TYPE == 0) {
        const int hp = wave & 3;
        if (wave < 4) { ost0 = (i >> 3) * 2048 + (hp >> 1) * 1024 + (16 * (2 * (hp & 1) + (gq >> 1)) + 2 * (i & 7) + (gq & 1)) * 16; ost1 = ost0 + 4096; ubase = QX_L + h * 8192; }
        else { const int ip = i >> 1; ost0 = (i & 1) * 4096 + hp * 1024 + (16 * gq + 4 * (ip & 1) + ((ip >> 1) & 3)) * 16; ost1 = ost0 + 128; ubase = KX_L + h * 8192; }
    } else { ost0 = (i >> 3) * 16384 + (16 * gq + (i & 7)) * 16; ost1 = 0; ubase = ZX_P + wave * 32768 + (h >> 2) * 2048 + (h & 1) * 1024 + ((h >> 1) & 1) * 128; }
    const int viw = L_VI + i * 256 + (((4 * (wave >> 1) + gq) ^ i) * 16) + (wave & 1) * 8;
    const int vtok = 8 * (i >> 2) + (i & 3);
    auto signal = [&](int it) { if (wave == 0 && lane == 0) __hip_atomic_store(cx.ctl + CW_P + 16 * (64 * TYPE + 2 * h + second), (u32)seg_of(it + 1), RLX_AGENT); };
    auto phase1 = [&](int it, int buf) {
        const int bn = buf == 0 ? 2 : buf - 1, sn = seg_of(it + 2);
        const u32 x0 = xb0 + buf * 40960, x1 = xb1 + buf * 40960;
        f32x4 d00 = {0, 0, 0, 0}, d01 = d00, d10 = d00, d11 = d00;
        asm volatile("" : "+v"(d00), "+v"(d01), "+v"(d10), "+v"(d11));
        v4u b[8];
#define XRD(slot, ss) do { LDS_RD128(b[2 * (slot)], ((ss) & 1) ? x1 : x0, ((ss) >> 1) * 4096); LDS_RD128(b[2 * (slot) + 1], ((ss) & 1) ? x1 : x0, ((ss) >> 1) * 4096 + 2048); } while (0)
#pragma unroll
        for (int ss = 0; ss < 4; ++ss) XRD(ss, ss);
#pragma unroll
        for (int ss = 0; ss < 20; ++ss) {
            const int sl = ss & 3;
            if (ss < 17) LGKM_WAIT2(6, b[2 * sl], b[2 * sl + 1]); else LGKM_WAIT2(2 * (19 - ss), b[2 * sl], b[2 * sl + 1]);
            if (TYPE == 0) { MF_WA(d00, 2 * ss, b[2 * sl]); MF_WA(d01, 2 * ss, b[2 * sl + 1]); MF_WA(d10, 2 * ss + 1, b[2 * sl]); MF_WA(d11, 2 * ss + 1, b[2 * sl + 1]); }
            else { MF_WA(d00, ss, b[2 * sl]); MF_WA(d01, ss, b[2 * sl + 1]); }
            if (ss + 4 < 20) XRD(sl, ss + 4);
            if (TYPE == 0 && ss == 8) { VMCNT(0); RAWBAR(); if (it > 0) signal(it - 1); }
            if (TYPE == 0 && ss >= 9 && ss < 19 && (ss & 1)) issue1(sn, bn, (ss - 9) >> 1);
            if (TYPE == 1 && ss % 4 == 1) issue1(sn, bn, ss >> 2);
        }
#undef XRD
        asm volatile("s_nop 7" : "+v"(d00), "+v"(d01), "+v"(d10), "+v"(d11));
        if (TYPE == 0) {
            const int so = rfl(amap(ubase + seg_of(it) * (H * 8192), sel));
            u32 a0 = pk2(d00.x, d00.y), a1 = pk2(d00.z, d00.w), a2 = pk2(d10.x, d10.y), a3 = pk2(d10.z, d10.w);
            u32 c0 = pk2(d01.x, d01.y), c1 = pk2(d01.z, d01.w), c2 = pk2(d11.x, d11.y), c3 = pk2(d11.z, d11.w);
            if (wave < 4) {
                auto sa = __builtin_amdgcn_permlane16_swap(a0, a1, false, false), sb = __builtin_amdgcn_permlane16_swap(a2, a3, false, false);
                auto sc = __builtin_amdgcn_permlane16_swap(c0, c1, false, false), sd = __builtin_amdgcn_permlane16_swap(c2, c3, false, false);
                st128((v4u){sa[0], sb[0], sa[1], sb[1]}, cx.ar, ost0, so);
                st128((v4u){sc[0], sd[0], sc[1], sd[1]}, cx.ar, ost1, so);
            } else {
                st128((v4u){a0, a1, a2, a3}, cx.ar, ost0, so);
                st128((v4u){c0, c1, c2, c3}, cx.ar, ost1, so);
            }
        } else {
            *(v2u*)(lds + viw + (it & 1) * 8192) = (v2u){pk2(d00.x, d00.y), pk2(d00.z, d00.w)};
            *(v2u*)(lds + viw + (it & 1) * 8192 + 4096) = (v2u){pk2(d01.x, d01.y), pk2(d01.z, d01.w)};
        }
    };
    auto phase2 = [&](int it, bool mid) {
        const int ub = ubase + seg_of(it) * ZX_STEP;
        v4u a[8];
#pragma unroll
        for (int k = 0; k < 8; ++k) { const int tok = vtok + 4 * (k >> 2); a[k] = *(const v4u*)(lds + L_VI + (it & 1) * 8192 + tok * 256 + (((4 * (k & 3) + gq) ^ (tok & 15)) * 16)); }
        f32x4 d0[5], d1[5];
#pragma unroll
        for (int cg = 0; cg < 5; ++cg) { d0[cg] = (f32x4){0, 0, 0, 0}; d1[cg] = d0[cg]; asm volatile("" : "+v"(d0[cg]), "+v"(d1[cg])); }
        asm volatile("s_nop 1");
#pragma unroll
        for (int cg = 0; cg < 6; ++cg) {
            if (cg < 5) {
#pragma unroll
                for (int ss = 0; ss < 4; ++ss) { MF_WB(d0[cg], a[ss], 20 + cg * 4 + ss); MF_WB(d1[cg], a[4 + ss], 20 + cg * 4 + ss); }
            } else asm volatile("s_nop 7");
            if (cg == 0 && mid) { VMCNT(5); BAR(); if (it > 0) signal(it - 1); }
            if (cg > 0) {
                asm volatile("" : "+v"(d0[cg - 1]), "+v"(d1[cg - 1]));
                st128((v4u){pk2(d0[cg - 1].x, d0[cg - 1].y), pk2(d0[cg - 1].z, d0[cg - 1].w), pk2(d1[cg - 1].x, d1[cg - 1].y), pk2(d1[cg - 1].z, d1[cg - 1].w)},
                      cx.ar, ost0, ub + (cg - 1) * (8 * 32768));
            }
        }
    };
    VMCNT(0);
    BAR();
    if (TYPE == 0) {
        int buf = 0;
        for (int it = 0; it < nit; ++it) {
            phase1(it, buf);
            buf = buf == 2 ? 0 : buf + 1;
            if (it == 0) { VMCNT(0); BAR(); signal(0); EVT(2); }
        }
    } else {
        phase1(0, 0);
        BAR();
        phase2(0, false);
        VMCNT(0); BAR(); signal(0);
        EVT(2);
        phase1(1, 1);
        VMCNT(5); BAR();
        int buf = 2;
        for (int k = 1; k < nit; ++k) {
            phase1(k + 1, buf);
            phase2(k, true);
            buf = buf == 2 ? 0 : buf + 1;
        }
    }
    VMCNT(0);
    BAR();
    signal(nit - 1);
}

constexpr int SS_K = 33 * 144, SS_U = 4288, SS_CH = 2 * SS_K + 2 * SS_U + 32;
static_assert(SS_CH % 16 == 0 && 8 * SS_CH <= L_TOTAL, "ssm lds");
DEV void ssm_task(const Params& p, const Ctx& cx, unsigned char* lds, int task, int wave, int far) {
    const int lane = lane_id(), tid = wave * 64 + lane;
    const int d = task * 8 + wave;
    unsigned char* ch = lds + wave * SS_CH;
    unsigned char* tmpb = ch + 2 * SS_K;
    const int i = lane & 15, gq = lane >> 4;
    v4u xr[2][3];
    {
        const float dt = rbf(__expf(bf2f(p.logdt[d])));
#pragma unroll
        for (int k = 0; k < 2; ++k) {
            const int nn = lane + 64 * k, o = d * NS + nn;
            const float br = dt * bf2f(p.Bre[o]), bi = dt * bf2f(p.Bim[o]), cr = bf2f(p.Cre[o]), ci = bf2f(p.Cim[o]);
            *(f32x4*)(tmpb + nn * 16) = (f32x4){dt * bf2f(p.Are[o]) * 1.4426950408889634f, dt * bf2f(p.Aim[o]) * 0.15915494309189535f, cr * br - ci * bi, cr * bi + ci * br};
        }
        asm volatile("s_waitcnt lgkmcnt(0)" ::: "memory");
        {
            const rsrc_t rxin = mkrs(p.x, S * DM * 2);
#pragma unroll
            for (int k = 0; k < 2; ++k)
#pragma unroll
                for (int j = 0; j < 3; ++j) xr[k][j] = __builtin_amdgcn_raw_buffer_load_b128(rxin, ((2 * (tid + 512 * k) + j) * DM + task * 8) * 2, 0, 0);
            __builtin_amdgcn_sched_barrier(0);
        }
        const float lu = 16.f * i;
        const float lv0 = (float)i;
        const float lv1 = i < 8 ? 256.f + 2.f * i : 512.f + 256.f * ((i - 8) >> 2) + 4.f * (i & 3);
        const float lv2 = i < 8 ? 1024.f + 256.f * (i >> 1) + 8.f * (i & 1) : 2048.f;
        f32x4 k0 = {0, 0, 0, 0}, k1 = k0, k2 = k0;
#pragma unroll 2
        for (int it = 0; it < 32; ++it) {
            const f32x4 q = *(const f32x4*)(tmpb + (4 * it + gq) * 16);
            auto cexp = [&](float l, float& re, float& im) {
                const float e = __builtin_amdgcn_exp2f(q.x * l), th = __builtin_amdgcn_fractf(q.y * l);
                re = e * __builtin_amdgcn_cosf(th); im = e * __builtin_amdgcn_sinf(th);
            };
            float er, ei, r0, i0, r1, i1, r2, i2;
            cexp(lu, er, ei); cexp(lv0, r0, i0); cexp(lv1, r1, i1); cexp(lv2, r2, i2);
            const float ur = q.z * er - q.w * ei, nui = -(q.z * ei + q.w * er);
            k0 = __builtin_amdgcn_mfma_f32_16x16x4f32(ur, r0, k0, 0, 0, 0); k1 = __builtin_amdgcn_mfma_f32_16x16x4f32(ur, r1, k1, 0, 0, 0); k2 = __builtin_amdgcn_mfma_f32_16x16x4f32(ur, r2, k2, 0, 0, 0);
            k0 = __builtin_amdgcn_mfma_f32_16x16x4f32(nui, i0, k0, 0, 0, 0); k1 = __builtin_amdgcn_mfma_f32_16x16x4f32(nui, i1, k1, 0, 0, 0); k2 = __builtin_amdgcn_mfma_f32_16x16x4f32(nui, i2, k2, 0, 0, 0);
        }
        float* tab = (float*)(tmpb + 2048);
        *(f32x4*)(tab + i * 20 + 4 * gq) = k0; *(f32x4*)(tab + (16 + i) * 20 + 4 * gq) = k1; *(f32x4*)(tab + (32 + i) * 20 + 4 * gq) = k2;
        asm volatile("s_waitcnt lgkmcnt(0)" ::: "memory");
        if (lane < 36) { *(u32*)(ch + lane * 4) = 0; *(u32*)(ch + SS_K + lane * 4) = 0; }
#pragma unroll 2
        for (int it = 0; it < 16; ++it) {
            const int l0 = it * 128 + lane * 2;
            float kv[2];
#pragma unroll
            for (int e = 0; e < 2; ++e) {
                const int lr = (int)rbf((float)(l0 + e));
                const int a = (lr >> 4) & 15, j = lr & 15, hi = lr >> 8;
                int col;
                if (hi == 0) col = j; else if (hi == 1) col = 16 + (j >> 1); else if (hi < 4) col = 24 + 4 * (hi - 2) + (j >> 2); else if (hi < 8) col = 32 + 2 * (hi - 4) + (j >> 3); else col = 40;
                kv[e] = tab[col * 20 + a];
            }
            const float h0 = rbf(kv[0]), h1 = rbf(kv[1]);
            const int o = ((l0 >> 6) + 1) * 144 + ((l0 & 56) + 6 - (l0 & 6)) * 2;
            *(u32*)(ch + o) = pk2(h1, h0);
            *(u32*)(ch + SS_K + o) = pk2(kv[1] - h1, kv[0] - h0);
        }
    }
    EVT(8);
    BAR();
    {
        if (lane < 24) { *(u32*)(ch + 2 * SS_K + lane * 4) = 0; if (lane < 23) *(u32*)(ch + 2 * SS_K + SS_U + lane * 4) = 0; }
#pragma unroll
        for (int k = 0; k < 2; ++k) {
            const int l = 2 * (tid + 512 * k);
#pragma unroll
            for (int c = 0; c < 8; ++c) {
                const u32 sel = (c & 1) ? 0x07060302u : 0x05040100u;
                unsigned char* ub = lds + c * SS_CH + 2 * SS_K;
                *(u32*)(ub + (l + 48) * 2) = __builtin_amdgcn_perm(xr[k][1][c >> 1], xr[k][0][c >> 1], sel);
                *(u32*)(ub + SS_U + (l + 48) * 2) = __builtin_amdgcn_perm(xr[k][2][c >> 1], xr[k][1][c >> 1], sel);
                if (k == 0 && tid == 0) *(u32*)(ub + SS_U + 46 * 2) = __builtin_amdgcn_perm(xr[0][0][c >> 1], 0u, sel);
            }
        }
    }
    BAR();
    EVT(9);
    f32x4 acc[2][4];
#pragma unroll
    for (int I = 0; I < 2; ++I)
#pragma unroll
        for (int rt = 0; rt < 4; ++rt) acc[I][rt] = (f32x4){0, 0, 0, 0};
    {
        const int st0 = i - 8 * gq - 7 + 48;
        const unsigned char* ua = ch + 2 * SS_K + ((st0 & 1) ? SS_U + (st0 - 1) * 2 : st0 * 2);
        auto afrag = [&](int o) { const u32* q = (const u32*)(ua + o * 32); return (v4u){q[0], q[1], q[2], q[3]}; };
        v4u am2 = {0, 0, 0, 0};
        v4u am1 = afrag(-1);
        const int kb = gq * 16;
        auto step = [&](int t, int I, const v4u& a0, const v4u& a1, const v4u& a2, const v4u& a3) {
            const int blk = max(16 * I + i - t + 1, 0);
            const unsigned char* kp = ch + blk * 144 + kb;
#pragma unroll
            for (int hl = 0; hl < 2; ++hl) {
                const v4u b0 = *(const v4u*)(kp + hl * SS_K), b1 = *(const v4u*)(kp + hl * SS_K + 64);
                acc[I][0] = mfma16(a0, b0, acc[I][0]); acc[I][1] = mfma16(a1, b0, acc[I][1]); acc[I][2] = mfma16(a2, b0, acc[I][2]); acc[I][3] = mfma16(a3, b0, acc[I][3]);
                acc[I][0] = mfma16(am2, b1, acc[I][0]); acc[I][1] = mfma16(am1, b1, acc[I][1]); acc[I][2] = mfma16(a0, b1, acc[I][2]); acc[I][3] = mfma16(a1, b1, acc[I][3]);
            }
        };
#pragma unroll 1
        for (int t = 0; t < 16; ++t) {
            const v4u a0 = afrag(4 * t), a1 = afrag(4 * t + 1), a2 = afrag(4 * t + 2), a3 = afrag(4 * t + 3);
            step(t, 0, a0, a1, a2, a3); step(t, 1, a0, a1, a2, a3);
            am2 = a2; am1 = a3;
        }
#pragma unroll 1
        for (int t = 16; t < 32; ++t) {
            const v4u a0 = afrag(4 * t), a1 = afrag(4 * t + 1), a2 = afrag(4 * t + 2), a3 = afrag(4 * t + 3);
            step(t, 1, a0, a1, a2, a3);
            am2 = a2; am1 = a3;
        }
    }
    EVT(10);
    {
        const float dsk = bf2f(p.dskip[d]);
        asm volatile("s_waitcnt lgkmcnt(0)" ::: "memory");
#pragma unroll
        for (int I = 0; I < 2; ++I)
#pragma unroll
            for (int rt = 0; rt < 4; ++rt) {
                const int l = 64 * (16 * I + i) + 16 * rt + 4 * gq;
                const v2u uu = *(const v2u*)(ch + 2 * SS_K + (l + 48) * 2);
                const f32x4 y = acc[I][rt];
                const float b0 = rbf(y.x) + rbf(bf2f(uu.x & 0xffffu) * dsk), b1 = rbf(y.y) + rbf(bfhi(uu.x) * dsk), b2 = rbf(y.z) + rbf(bf2f(uu.y & 0xffffu) * dsk), b3 = rbf(y.w) + rbf(bfhi(uu.y) * dsk);
                *(v2u*)(ch + (16 * I + i) * 136 + (16 * rt + 4 * gq) * 2) = (v2u){pk2(b0, b1), pk2(b2, b3)};
            }
    }
    BAR();
#pragma unroll
    for (int k = 0; k < 2; ++k) {
        const int l = 2 * (tid + 512 * k);
        u32 e[8];
#pragma unroll
        for (int c = 0; c < 8; ++c) e[c] = *(const u32*)(lds + c * SS_CH + (l >> 6) * 136 + (l & 63) * 2);
        v4u lo, hi;
#pragma unroll
        for (int c = 0; c < 4; ++c) { lo[c] = __builtin_amdgcn_perm(e[2 * c + 1], e[2 * c], 0x05040100u); hi[c] = __builtin_amdgcn_perm(e[2 * c + 1], e[2 * c], 0x07060302u); }
        const int so = rfl(amap(BS_L + task * 32768 + k * 16384 + wave * 2048, far));
        st128(lo, cx.ar, lane * 32, so); st128(hi, cx.ar, lane * 32 + 16, so);
    }
    VMCNT(0);
    BAR();
    if (tid == 0) __hip_atomic_fetch_add(cx.ctl + CW_SSM + 64 * (task >> 4), 1u, RLX_AGENT);
}

constexpr int L_OP = 0, L_OA = 81920, L_OG = L_OA + 32 * 272;
DEV void out_task(const Params& p, const Ctx& cx, unsigned char* lds, int s, int cgp, int wave, int& ready) {
    const int lane = lane_id(), tid = wave * 64 + lane, far = cx.par ^ 1;
    const int n = lane & 15, gq = lane >> 4, gl = lane * 16;
    SIDX_DECL;
    const int ct = 8 * cgp + wave;
    const int zxl = (n >> 3) * 16384 + (16 * gq + (n & 7)) * 16, zml = (n >> 3) * 4096 + gq * 128 + (n & 7) * 16;
    if (wave == 0 && s >= ready) ready = wait_upto<1>(prog_load<1>(0, lane, cx.ctl), s, 0, lane, cx.ctl);
    BAR();
    v8u wr[20];
#pragma unroll
    for (int k = 0; k < 16; ++k) {
        const int so = ZX_P + s * ZX_STEP + ct * 32768 + (k >> 1) * 2048;
        wr[k] = cat8(ld128(cx.ar, zxl + (k & 1) * 128, so), ld128(cx.ar, zxl + (k & 1) * 128 + 1024, so));
    }
    const int otok = tid >> 4, oc = tid & 15;
    const v4u bv = ld128(cx.ar, amap(BS_L + (16 * cgp + oc) * 32768 + (s * 32 + otok) * 16, far), 0);
    {
        const rsrc_t rxin = mkrs(p.x, S * DM * 2), rg = mkrs(p.gw, DM * 2);
        const int tok = wave * 4 + gq;
        float sum = 0.f;
#pragma unroll
        for (int k = 0; k < 5; ++k) {
            const v4u xv = __builtin_amdgcn_raw_buffer_load_b128(rxin, ((s * 32 + tok) * DM + k * 128 + n * 8) * 2, 0, 0);
            const v4u gv = __builtin_amdgcn_raw_buffer_load_b128(rg, (k * 128 + n * 8) * 2, 0, 0);
#pragma unroll
            for (int e = 0; e < 4; ++e) sum += rbf(bf2f(xv[e] & 0xffffu) * bf2f(gv[e] & 0xffffu)) + rbf(bfhi(xv[e]) * bfhi(gv[e]));
        }
        sum = rbf(rowsum16(sum));
        const float g = rbf(1.f / (1.f + __expf(-sum)));
        if (n == 0) *(float*)(lds + L_OG + tok * 4) = g;
    }
    if (wave == 0) {
        if (s < NSEG - 2) wait_ge(cx.ctl + CW_S + 16 * s, 1u, cx.ctl);
        else {
            u32 spins = 0;
            for (;;) {
                const u32 a = __hip_atomic_load(cx.ctl + CW_FIN + 16 * lane, RLX_AGENT), c = __hip_atomic_load(cx.ctl + CW_FIN + 16 * (lane < 48 ? lane + 64 : lane), RLX_AGENT);
                if (__all(a && c)) break;
                __builtin_amdgcn_s_sleep(2);
                if (spin_fail(spins, cx.ctl)) break;
            }
        }
    }
    BAR();
    {
        v4u t[10];
#pragma unroll
        for (int k = 0; k < 10; ++k) t[k] = ld128(cx.ar, gl, rfl(amap(PT_L + s * PT_STEP + (k < 8 ? wave * 8 + k : 48 + wave * 2 + 8 + k) * 1024, far)));
        const int so = rfl(amap(ZM_L + s * ZM_STEP + ct * 8192, cx.par));
#pragma unroll
        for (int k = 0; k < 4; ++k) wr[16 + k] = cat8(ld128(cx.ar, zml + k * 1024, so), ld128(cx.ar, zml + k * 1024 + 512, so));
#pragma unroll
        for (int k = 0; k < 8; ++k) *(v4u*)(lds + L_OP + (wave * 8 + k) * 1024 + gl) = t[k];
#pragma unroll
        for (int k = 8; k < 10; ++k) {
            unsigned char* d = lds + L_OP + (56 + wave * 2 + k) * 1024 + gq * 256 + (lane & 7) * 32 + ((lane >> 3) & 1) * 8;
            *(v2u*)d = (v2u){t[k].x, t[k].y}; *(v2u*)(d + 16) = (v2u){t[k].z, t[k].w};
        }
    }
    BAR();
    f32x4 acc[4];
#pragma unroll
    for (int g = 0; g < 4; ++g) acc[g] = (f32x4){0, 0, 0, 0};
    asm volatile("s_nop 1" : "+v"(acc[0]), "+v"(acc[1]), "+v"(acc[2]), "+v"(acc[3]));
#pragma unroll
    for (int ks = 0; ks < 20; ++ks)
#pragma unroll
        for (int g = 0; g < 4; ++g) {
            const v4u a = *(const v4u*)(lds + L_OP + (ks < 16 ? g * 16 + ks : 64 + g * 4 + ks - 16) * 1024 + gl);
            SMF_W(acc[g], a, ks);
        }
    asm volatile("s_nop 7" : "+v"(acc[0]), "+v"(acc[1]), "+v"(acc[2]), "+v"(acc[3]));
#pragma unroll
    for (int g = 0; g < 4; ++g) {
        const u32 v = pk2(acc[g].x + acc[g].y, acc[g].z + acc[g].w);
        unsigned char* o = lds + L_OA + (8 * g + 2 * gq) * 272 + (wave * 16 + n) * 2;
        *(u16*)o = (u16)v; *(u16*)(o + 272) = (u16)(v >> 16);
    }
    BAR();
    {
        const v4u av = *(const v4u*)(lds + L_OA + otok * 272 + oc * 16);
        const float g = *(const float*)(lds + L_OG + otok * 4), og = rbf(1.f - g);
        v4u o;
#pragma unroll
        for (int e = 0; e < 4; ++e)
            o[e] = pk2(rbf(g * bf2f(av[e] & 0xffffu)) + rbf(og * bf2f(bv[e] & 0xffffu)), rbf(g * bfhi(av[e])) + rbf(og * bfhi(bv[e])));
        *(v4u*)(p.out + (s * 32 + otok) * DM + cgp * 128 + oc * 8) = o;
    }
}

DEV unsigned char* arena_base(unsigned char* ws) { return (unsigned char*)(((size_t)ws + CTRL_BYTES + (2u << 20) - 1) & ~(size_t)((2u << 20) - 1)); }

constexpr int LI_V = 0, LI_O = 40960, LI_TOTAL = LI_O + 3 * 32768;
__global__ void __launch_bounds__(576) gm_init(const u16* Wv, const u16* Wo, unsigned char* ws) {
    extern __shared__ __attribute__((aligned(16))) unsigned char lds[];
    const int b = blockIdx.x, wave = rfl(threadIdx.x >> 6), lane = threadIdx.x & 63;
    const rsrc_t ar = mkrs(arena_base(ws), ARENA_BYTES);
    if (wave == 8) {
        if (b == 0) {
            u32 mn[2] = {100000u, 100000u};
            for (int it = 0; it < 4; ++it)
#pragma unroll
                for (int q = 0; q < 2; ++q) {
                    const u32 c0 = now32();
                    st32(0u, ar, lane * 4, (4 << 20) + it * 256 + q * 8192);
                    VMCNT(0);
                    mn[q] = min(mn[q], now32() - c0);
                }
            if (lane < 32) ((u32*)ws)[CW_PAR + lane] = mn[1] < mn[0];
        } else if (b == 1) {
            for (int i = lane; i < CTRL_BYTES / 16; i += 64) if (i < CW_PAR / 4 || i >= CW_PAR / 4 + 8) ((v4u*)ws)[i] = (v4u){0, 0, 0, 0};
        }
        const v4u sv = {SENT, SENT, SENT, SENT};
#pragma unroll
        for (int k = 0; k < 16; ++k) st128(sv, ar, lane * 16, b * 16384 + k * 1024);
        return;
    }
    const int h = 4 * (b & 7) + (b >> 6), g0 = 5 * ((b >> 4) & 3), ch = (b >> 3) & 1;
    const int i = lane & 15, gq = lane >> 4;
    const u32 lds0 = (u32)(size_t)(LAS unsigned char*)lds;
    {
        const int row = wave * 4 + gq, f = (row & 13) | ((row >> 4) << 1);
        const rsrc_t rv = mkrs(Wv, DM * HD * 2);
#pragma unroll
        for (int g = 0; g < 5; ++g) dma16(rfl(lds0 + LI_V + g * 8192 + wave * 1024), row * (HD * 2) + ((i ^ f) * 16), rv, rfl(((g0 + g) * 32 * HD + h * DH) * 2));
    }
    {
        const rsrc_t ro = mkrs(Wo, DM * HD * 2);
        const int tp = lane >> 3, q4 = (lane >> 1) & 3, hf = lane & 1;
#pragma unroll
        for (int cg = 0; cg < 3; ++cg)
#pragma unroll
            for (int k = 0; k < 4; ++k)
                dma16(rfl(lds0 + LI_O + cg * 32768 + (wave * 4 + k) * 1024), q4 * (DM * 2) + (((tp ^ (k >> 1)) & (cg == 2 ? 3 : 7)) * 16 + hf * 8) * 2, ro,
                      rfl(((h * DH + wave * 16 + k * 4) * DM + ch * 320 + cg * 128) * 2));
    }
    const int rd0 = LI_O + (2 * gq) * 1024 + (i >> 2) * 32 + (i & 3) * 8 + (wave ^ (gq & 1)) * 128;
    const int dl = 16 * (i >> 3) + 8 * ((i >> 1) & 1) + 4 * ((i >> 2) & 1) + (i & 1);
    const int fl = (dl & 13) | ((dl >> 4) << 1);
#pragma unroll
    for (int cg = 0; cg < 3; ++cg) {
        if (cg == 0) VMCNT(8); else if (cg == 1) VMCNT(9); else VMCNT(10);
        BAR();
        if (cg == 2 && wave >= 4) break;
        v4u w[4];
#pragma unroll
        for (int ss = 0; ss < 4; ++ss) {
            const v2u t0 = __builtin_bit_cast(v2u, __builtin_amdgcn_ds_read_tr16_b64_v4i16((LAS s16x4*)(lds + rd0 + cg * 32768 + ss * 8192)));
            const v2u t1 = __builtin_bit_cast(v2u, __builtin_amdgcn_ds_read_tr16_b64_v4i16((LAS s16x4*)(lds + rd0 + cg * 32768 + ss * 8192 + 1024)));
            w[ss] = (v4u){t0.x, t0.y, t1.x, t1.y};
        }
#pragma unroll
        for (int g = 0; g < 5; ++g) {
            f32x4 d0 = {0, 0, 0, 0}, d1 = d0;
#pragma unroll
            for (int ss = 0; ss < 4; ++ss) {
                d0 = mfma16(*(const v4u*)(lds + LI_V + g * 8192 + dl * 256 + (((4 * ss + gq) ^ fl) * 16)), w[ss], d0);
                d1 = mfma16(*(const v4u*)(lds + LI_V + g * 8192 + (dl + 2) * 256 + (((4 * ss + gq) ^ fl) * 16)), w[ss], d1);
            }
            st128((v4u){pk2(d0.x, d0.y), pk2(d0.z, d0.w), pk2(d1.x, d1.y), pk2(d1.z, d1.w)}, ar, lane * 16, VO_P + ((h * 20 + g0 + g) * 40 + ch * 20 + cg * 8 + wave) * 1024);
        }
    }
}

__global__ void __launch_bounds__(512, 2) gm_kernel(Params p) {
    extern __shared__ __attribute__((aligned(16))) unsigned char lds[];
    const int b = blockIdx.x;
    const int wave = rfl(threadIdx.x >> 6);
    Ctx cx;
    cx.ctl = (GAS u32*)p.ws;
    cx.ar = mkrs(arena_base(p.ws), ARENA_BYTES);
    cx.par = rfl(cx.ctl[CW_PAR]) & 1;
    EVT(0);
    if ((b & 7) >= 4) {
        const int u = (b >> 3) * 4 + (b & 7) - 4, j = u >> 2;
        const int h = 8 * (u & 3) + (j >> 2), type = (j >> 1) & 1, second = j & 1;
        if (type) l_worker<1>(p, cx, lds, h, second, wave); else l_worker<0>(p, cx, lds, h, second, wave);
        EVT(3);
        if (!second) return;
        ssm_task(p, cx, lds, type * 32 + h, wave, cx.par ^ 1);
        EVT(4);
        const int nb = type ? 12 : 10, gi = h / nb;
        if (gi >= (type ? 2 : 3)) return;
        const int cgp = type ? 2 + gi : (gi == 2 ? 4 : gi);
        if (wave == 0) { wait_ge(cx.ctl + CW_SSM + 64 * cgp, 16u, cx.ctl); for (int k = 0; k < h % nb; ++k) __builtin_amdgcn_s_sleep(14); }
        int ready = 0;
#pragma unroll 1
        for (int s = h % nb; s < NSEG; s += nb) out_task(p, cx, lds, s, cgp, wave, ready);
        EVT(5);
        return;
    }
    const int w = (b >> 3) * 4 + (b & 7);
    if (w < 32) {
        if (wave < 2) a_role<0>(p, cx, lds, w, wave);
        else if (wave < 6) a_role<1>(p, cx, lds, w, wave);
        else a_role<2>(p, cx, lds, w, wave);
    } else if (w < 112) { if (wave == 1) r_role<true>(p, cx, lds, w - 32, wave); else r_role<false>(p, cx, lds, w - 32, wave); }
    else {
        ssm_task(p, cx, lds, w - 48, wave, cx.par ^ 1);
        if (w == 112 && wave == 0) {
            const int lane = lane_id();
            for (int s = 2; s < NSEG; ++s) {
                const int so = rfl(amap(s * SLOT + TS_O, cx.par));
                u32 spins = 0;
                for (;;) {
                    asm volatile("" ::: "memory");
                    if (!__any(ld32(cx.ar, (lane & 31) * 64, so) == SENT)) break;
                    __builtin_amdgcn_s_sleep(12);
                    if (spin_fail(spins, cx.ctl)) break;
                }
                if (lane == 0) __hip_atomic_store(cx.ctl + CW_S + 16 * (s - 2), 1u, RLX_AGENT);
                if (spins) __builtin_amdgcn_s_sleep(44);
            }
        }
    }
    EVT(5);
}

static const int g_dA = 1200, g_dB = 1090, g_dR = 1150, g_dZ = 5;
static const int g_dbg = 0;

extern "C" void kernel_launch(void* const* d_in, const int* in_sizes, int n_in,
                              void* d_out, int out_size, void* d_ws, size_t ws_size,
                              hipStream_t stream) {
    Params p;
    p.x = (const u16*)d_in[0]; p.mem0 = (const u16*)d_in[1]; p.Wq = (const u16*)d_in[2]; p.Wk = (const u16*)d_in[3]; p.Wv = (const u16*)d_in[4]; p.Wo = (const u16*)d_in[5];
    p.Are = (const u16*)d_in[8]; p.Aim = (const u16*)d_in[9]; p.Bre = (const u16*)d_in[10]; p.Bim = (const u16*)d_in[11]; p.Cre = (const u16*)d_in[12]; p.Cim = (const u16*)d_in[13];
    p.logdt = (const u16*)d_in[14]; p.dskip = (const u16*)d_in[15]; p.gw = (const u16*)d_in[16];
    p.out = (u16*)d_out; p.ws = (unsigned char*)d_ws;
    p.c3 = g_dA; p.g1 = g_dB; p.gmax = g_dR; p.maxrel = g_dZ; p.cp = g_dB - 90; p.cmL = g_dbg ? g_dbg : 560; p.glate = 2350; p.pad = 150;
    (void)hipFuncSetAttribute((const void*)gm_init, hipFuncAttributeMaxDynamicSharedMemorySize, LI_TOTAL);
    gm_init<<<256, 576, LI_TOTAL, stream>>>(p.Wv, p.Wo, (unsigned char*)d_ws);
    (void)hipFuncSetAttribute((const void*)gm_kernel, hipFuncAttributeMaxDynamicSharedMemorySize, L_TOTAL);
    gm_kernel<<<256, 512, L_TOTAL, stream>>>(p);
}
```

```cpp
#include <hip/hip_runtime.h>
#include <hip/hip_bf16.h>
#include <stdint.h>

typedef unsigned short u16;
typedef unsigned int u32;
typedef u32 v4u __attribute__((ext_vector_type(4)));
typedef u32 v2u __attribute__((ext_vector_type(2)));
typedef u32 v8u __attribute__((ext_vector_type(8)));
typedef float f32x4 __attribute__((ext_vector_type(4)));
typedef __bf16 bf16x8 __attribute__((ext_vector_type(8)));
typedef __bf16 bf16x2 __attribute__((ext_vector_type(2)));
typedef short s16x4 __attribute__((ext_vector_type(4)));
typedef __amdgpu_buffer_rsrc_t rsrc_t;
#define GAS __attribute__((address_space(1)))
#define LAS __attribute__((address_space(3)))
#define RLX_AGENT __ATOMIC_RELAXED, __HIP_MEMORY_SCOPE_AGENT
#define DEV __device__ __forceinline__

constexpr int S = 2048, DM = 640, H = 32, DH = 128, NSEG = 64, HD = H * DH, NS = 128;
constexpr int SC1 = 16;
constexpr u32 SENT = 0xFFFFFFFFu;

DEV float bf2f(u32 b) { return __uint_as_float(b << 16); }
DEV float bfhi(u32 b) { return __uint_as_float(b & 0xFFFF0000u); }
DEV float rbf(float f) { return (float)(__bf16)f; }
DEV u32 pk2(float lo, float hi) { bf16x2 v = {(__bf16)lo, (__bf16)hi}; return __builtin_bit_cast(u32, v); }
DEV u32 pk2s(float lo, float hi) { return min(pk2(lo, hi), 0xFFFFFFFEu); }
DEV int rfl(int v) { return __builtin_amdgcn_readfirstlane(v); }
DEV int lane_id() { int l; asm volatile("v_mbcnt_lo_u32_b32 %0, -1, 0\n\tv_mbcnt_hi_u32_b32 %0, -1, %0" : "=v"(l)); return l; }

constexpr int CTRL_BYTES = 21504;
constexpr int ARENA_BYTES = 220 << 20;
constexpr int SLOT = 32768, MEM_O = 0, PX_O = 10240, PM_O = PX_O + 16384, TS_O = PM_O + 4096;
constexpr int QX_L = 3 << 20, KX_L = 19 << 20, ZM_L = 35 << 20, ZM_STEP = 80 * 4096;
constexpr int PT_L = 3 << 20, PT_STEP = 81920, BS_L = 8 << 20;
constexpr int VO_P = 112 << 20, ZX_P = 140 << 20, ZX_STEP = 80 * 16384;
constexpr int CW_TMO = 0, CW_PAR = 64, CW_SSM = 128  , CW_S = 512, CW_FIN = 1536, CW_P = 3328;

DEV int amap(int o, int sel) { return ((o >> 13) << 14) | (o & 8191) | ((((o >> 20) ^ sel) & 1) << 13); }
DEV u32 max4(v4u v) { return max(max(v.x, v.y), max(v.z, v.w)); }
DEV v8u cat8(v4u a, v4u b) { return __builtin_shufflevector(a, b, 0, 1, 2, 3, 4, 5, 6, 7); }
DEV rsrc_t mkrs(const void* p, int bytes) { return __builtin_amdgcn_make_buffer_rsrc((void*)p, 0, bytes, 0x00020000); }
DEV v4u ld128(rsrc_t r, int off, int so) { return __builtin_amdgcn_raw_buffer_load_b128(r, off, so, SC1); }
DEV u32 ld32(rsrc_t r, int off, int so) { return __builtin_amdgcn_raw_buffer_load_b32(r, off, so, SC1); }
DEV void st128(v4u v, rsrc_t r, int off, int so) { __builtin_amdgcn_raw_buffer_store_b128(v, r, off, so, SC1); }
DEV void st32(u32 v, rsrc_t r, int off, int so) { __builtin_amdgcn_raw_buffer_store_b32(v, r, off, so, SC1); }
#define VMCNT(n) asm volatile("s_waitcnt vmcnt(%0)" :: "i"(n) : "memory")
#define BAR() do { asm volatile("s_waitcnt lgkmcnt(0)" ::: "memory"); __builtin_amdgcn_s_barrier(); asm volatile("" ::: "memory"); } while (0)
template <int CTRL> DEV float dppf(float v) { return __builtin_bit_cast(float, __builtin_amdgcn_update_dpp(0, __builtin_bit_cast(int, v), CTRL, 0xf, 0xf, true)); }
DEV float rowsum16(float v) { v += dppf<0x128>(v); v += dppf<0x124>(v); v += dppf<0x122>(v); v += dppf<0x121>(v); return v; }
DEV void dma16(u32 ldsaddr, int voff, rsrc_t rs, int soff) {
    u32 keep;
    asm volatile("s_nop 4\n\ts_mov_b32 %0, m0\n\ts_mov_b32 m0, %1\n\ts_nop 0\n\tbuffer_load_dwordx4 %2, %3, %4 offen lds\n\ts_mov_b32 m0, %0"
                 : "=&s"(keep) : "s"(ldsaddr), "v"(voff), "s"(rs), "s"(soff) : "memory");
}

constexpr u32 SPINCAP = 1u << 20;
constexpr float A_CYCLES = 1570.f;
#define EVT(i)

#define DV(slot, v)
#define DFLUSH()
#define DT(name)
struct Params {
    const u16 *x, *mem0, *Wq, *Wk, *Wv, *Wo, *Are, *Aim, *Bre, *Bim, *Cre, *Cim, *logdt, *dskip, *gw;
    u16* out;
    unsigned char* ws;
    int g1, c3, gmax, cp, cmL, maxrel, glate, pad;
};
struct Ctx { GAS u32* ctl; rsrc_t ar; int par; };

constexpr int L_MEMF = 0, L_QF = 10240, L_KMF = 12288, L_KXF = 16384  , L_RED = 10240, L_ZT = 16384, L_STAGE = 24576, L_DUMP = 32768, L_TOTAL = L_STAGE + 131072;

DEV bool spin_fail(u32& spins, GAS u32* ctl) {
    if (++spins > SPINCAP) { __hip_atomic_store(ctl + CW_TMO, 1u, RLX_AGENT); return true; }
    return (spins & 1023u) == 0 && rfl(__hip_atomic_load(ctl + CW_TMO, RLX_AGENT)) != 0;
}
DEV u32 wait_ge(GAS u32* w, u32 want, GAS u32* ctl) {
    u32 spins = 0, v;
    for (;;) { v = rfl(__hip_atomic_load(w, RLX_AGENT)); if (v >= want) break; __builtin_amdgcn_s_sleep(8); if (spin_fail(spins, ctl)) break; }
    asm volatile("" ::: "memory");
    return v;
}
template <int MODE> DEV int prog_load(int h, int lane, GAS u32* ctl) { return (int)__hip_atomic_load(ctl + CW_P + 16 * (MODE == 0 ? 2 * h + (lane & 1) : 64 + lane), RLX_AGENT); }
template <int MODE>
DEV int wait_upto(int v, int need, int h, int lane, GAS u32* ctl) {
    u32 spins = 0; int r;
    for (;;) {
        v = max(v, lane & 1);
        v = min(v, __builtin_amdgcn_update_dpp(64, v, 0x128, 0xf, 0xf, false)); v = min(v, __builtin_amdgcn_update_dpp(64, v, 0x124, 0xf, 0xf, false));
        v = min(v, __builtin_amdgcn_update_dpp(64, v, 0x122, 0xf, 0xf, false)); v = min(v, __builtin_amdgcn_update_dpp(64, v, 0x121, 0xf, 0xf, false));
        r = min(min(__builtin_amdgcn_readlane(v, 0), __builtin_amdgcn_readlane(v, 16)), min(__builtin_amdgcn_readlane(v, 32), __builtin_amdgcn_readlane(v, 48)));
        if (r > need) break;
        __builtin_amdgcn_s_sleep(8);
        if (spin_fail(spins, ctl)) { r = 64; break; }
        v = prog_load<MODE>(h, lane, ctl);
    }
    asm volatile("" ::: "memory");
    return r;
}
DEV u32 now32() { unsigned long long t; asm volatile("s_memtime %0\n s_waitcnt lgkmcnt(0)" : "=s"(t) :: "memory"); return (u32)t; }
DEV u32 gnow() { unsigned long long t; asm volatile("s_memrealtime %0\n s_waitcnt lgkmcnt(0)" : "=s"(t) :: "memory"); return (u32)t * 24u + 48u; }
DEV int wait_global(u32 T, u32& off) {
    u32 e, d, n, g, c;
    asm volatile(
        "s_mov_b32 %3, 0\n"
        "1:\n"
        "s_memrealtime vcc\n s_memtime s[92:93]\n s_add_u32 %3, %3, 1\n s_waitcnt lgkmcnt(0)\n"
        "s_mul_i32 %1, vcc_lo, 24\n s_sub_u32 %1, %1, s92\n"
        "s_sub_u32 %2, %1, %0\n s_cmp_gt_i32 %2, 0\n s_cselect_b32 %0, %1, %0\n"
        "s_sub_u32 %2, %4, s92\n s_sub_u32 %2, %2, %0\n"
        "s_cmp_gt_i32 %2, 800\n s_cbranch_scc0 2f\n s_cmp_gt_i32 %2, 0x40000\n s_cbranch_scc1 4f\n s_mov_b32 %3, 0\n s_sleep 2\n s_branch 1b\n"
        "2:\n s_cmp_gt_i32 %2, 230\n s_cbranch_scc1 1b\n"
        "3:\n s_sub_u32 %2, %2, 32\n s_cmp_gt_i32 %2, 120\n s_cbranch_scc1 3b\n"
        "4:\n"
        : "+s"(off), "=&s"(e), "=&s"(d), "=&s"(n) : "s"(T) : "memory", "scc", "vcc", "s92", "s93");
    (void)g; (void)c;
    return (int)n;
}
#define RAWBAR() do { __builtin_amdgcn_s_barrier(); asm volatile("" ::: "memory"); } while (0)
DEV v2u ts_load(const Ctx& cx, int ad, int so) { return __builtin_amdgcn_raw_buffer_load_b64(cx.ar, ad, so, SC1); }
DEV int rowmax32(int d) {
    d = max(d, __builtin_amdgcn_update_dpp(0, d, 0x128, 0xf, 0xf, true)); d = max(d, __builtin_amdgcn_update_dpp(0, d, 0x124, 0xf, 0xf, true));
    d = max(d, __builtin_amdgcn_update_dpp(0, d, 0x122, 0xf, 0xf, true)); d = max(d, __builtin_amdgcn_update_dpp(0, d, 0x121, 0xf, 0xf, true));
    return max(__builtin_amdgcn_readlane(d, 0), __builtin_amdgcn_readlane(d, 16));
}
DEV u32 read_ref(v2u& v, const Ctx& cx, int ad, int so) {
    u32 spins = 0;
    while (__builtin_expect(__any(v.x == SENT || v.y == SENT), 0)) {
        if (spin_fail(spins, cx.ctl)) { v.y = 1600; break; }
        __builtin_amdgcn_s_sleep(2);
        asm volatile("" ::: "memory");
        v = ts_load(cx, ad, so);
    }
    const u32 b = rfl(v.x);
    return b + (u32)rowmax32((int)(v.x - b));
}

struct Pace { u32 p1; int c2, dur, x2, x3, relax, fails; bool sched; };
DEV void pace_next(Pace& q, v2u& tv, const Ctx& cx, int ad, int so, int s, const Params& p) {
    const int per = q.c2 + p.c3 + q.x3 + (q.dur >> 2);
    if (__builtin_expect(s > 0 && !__any(tv.x == SENT || tv.y == SENT || (int)(tv.x - q.p1) > p.gmax), 1)) {
        q.fails = max(q.fails - 1, 0);
        q.p1 += per;
        q.sched = true;
    } else {
        const u32 tref = read_ref(tv, cx, ad, so);
        const int g = (int)(tref - q.p1);
        const bool late = s == 0 || g > p.gmax;
        if (late && q.sched && s > 6) {
            q.fails += 2;
            if (q.fails >= 3 && q.relax < p.maxrel) { if (g > p.gmax + 650) q.x2 += 48; else q.x3 += 56; ++q.relax; q.fails = 0; }
        } else if (!late) q.fails = max(q.fails - 1, 0);
        q.p1 = late ? tref + per - (g < p.gmax + 200 ? 750 : p.cmL) : q.p1 + per;
        q.sched = !late;
    }
    q.c2 = p.g1 + q.x2 + q.dur;
}
DEV void pace_dur(Pace& q, v2u tv) { q.dur = rowmax32((int)tv.y); }

#define SMF_A(acc, a, w) asm volatile("v_smfmac_f32_16x16x64_bf16 %0, %1, %2, %3" : "+v"(acc) : "v"(a), "a"(w), "v"(sidx))
#define SMF_V(acc, a, w) asm volatile("v_smfmac_f32_16x16x64_bf16 %0, %1, %2, %3" : "+v"(acc) : "v"(a), "v"(w), "v"(sidx))
#define SMF_W(acc, a, i) do { if ((i) < 16) SMF_A(acc, a, wr[i]); else SMF_V(acc, a, wr[i]); } while (0)
#define MF_WA(acc, i, b) do { if ((i) < 32) asm volatile("v_mfma_f32_16x16x32_bf16 %0, %1, %2, %0" : "+v"(acc) : "a"(w4[i]), "v"(b)); \
                              else asm volatile("v_mfma_f32_16x16x32_bf16 %0, %1, %2, %0" : "+v"(acc) : "v"(w4[i]), "v"(b)); } while (0)
#define MF_WB(acc, a, i) do { if ((i) < 32) asm volatile("v_mfma_f32_16x16x32_bf16 %0, %1, %2, %0" : "+v"(acc) : "v"(a), "a"(w4[i])); \
                              else asm volatile("v_mfma_f32_16x16x32_bf16 %0, %1, %2, %0" : "+v"(acc) : "v"(a), "v"(w4[i])); } while (0)
#define LDS_RD128(dst, addr, off) asm volatile("ds_read_b128 %0, %1 offset:%2" : "=v"(dst) : "v"(addr), "i"(off) : "memory")
#define LGKM_WAIT(n, x) asm volatile("s_waitcnt lgkmcnt(%1)" : "+v"(x) : "i"(n) : "memory")
#define LGKM_WAIT2(n, x, y) asm volatile("s_waitcnt lgkmcnt(%2)" : "+v"(x), "+v"(y) : "i"(n) : "memory")
#define SIDX_DECL int sidx = (lane & 1) ? 0xEEEE : 0x4444; asm volatile("s_nop 1" : "+v"(sidx))
DEV float dmax16(float v) {
    asm volatile("s_nop 1\n v_max_f32_dpp %0, %0, %0 row_ror:8 row_mask:0xf bank_mask:0xf\n s_nop 1\n v_max_f32_dpp %0, %0, %0 row_ror:4 row_mask:0xf bank_mask:0xf\n"
                 "s_nop 1\n v_max_f32_dpp %0, %0, %0 row_ror:2 row_mask:0xf bank_mask:0xf\n s_nop 1\n v_max_f32_dpp %0, %0, %0 row_ror:1 row_mask:0xf bank_mask:0xf" : "+v"(v));
    return v;
}
DEV float max3f(float a, float b, float c) { float r; asm("v_max3_f32 %0, %1, %2, %3" : "=v"(r) : "v"(a), "v"(b), "v"(c)); return r; }
DEV f32x4 mfma16(v4u a, v4u b, f32x4 c) { return __builtin_amdgcn_mfma_f32_16x16x32_bf16(__builtin_bit_cast(bf16x8, a), __builtin_bit_cast(bf16x8, b), c, 0, 0, 0); }

template <int MODE, class Sink>
DEV void load_units(const Params& p, int h, unsigned char* lds, int wave, int lane, Sink sink) {
    constexpr int NU = MODE == 3 ? 10 : 20, NR = (NU + 3) / 4;
    const u32 lds0 = (u32)(size_t)(LAS unsigned char*)lds;
    const int tp = lane >> 3, q4 = (lane >> 1) & 3, hf = lane & 1;
    auto issue = [&](int r) {
        const int u = r * 4 + (wave >> 1), hh = wave & 1;
        if (u >= NU) return;
        bool second; int base, stride, map;
        if (MODE <= 1) { second = u & 1; base = (64 * (u >> 1) * HD + h * DH) * 2; stride = HD * 2; map = MODE; }
        else if (MODE == 2 && u < 10) { second = false; base = (64 * u * HD + h * DH) * 2; stride = HD * 2; map = 1; }
        else { const int uo = MODE == 2 ? u - 10 : u; second = true; base = ((h * DH + 64 * (uo & 1)) * DM + (uo >> 1) * 128) * 2; stride = DM * 2; map = MODE == 2 ? 2 : 1; }
        const int gq = map == 0 ? 8 * (q4 >> 1) + (q4 & 1) : q4;
        const rsrc_t rs = mkrs(second ? (MODE <= 1 ? p.Wk : p.Wo) : (MODE <= 1 ? p.Wq : p.Wv), DM * HD * 2);
#pragma unroll
        for (int i = 0; i < 8; ++i) {
            const int gB = i >> 1, i4 = i & 1;
            const int f = 32 * hh + (map == 0 ? 16 * (gB >> 1) + 4 * (gB & 1) + 2 * i4 : map == 1 ? 8 * gB + 4 * i4 : 16 * i4 + 4 * gB);
            const int voff = gq * stride + ((tp ^ (gB & 1)) * 16 + hf * 8) * 2;
            const u32 la = rfl(lds0 + L_STAGE + (r & 1) * 65536 + (wave >> 1) * 16384 + (hh * 8 + i) * 1024);
            dma16(la, voff, rs, rfl(base + f * stride));
        }
    };
    const int gB = lane >> 4;
    const int rd0 = (2 * gB) * 1024 + ((lane & 15) >> 2) * 32 + (lane & 3) * 8;
    issue(0);
#pragma unroll
    for (int r = 0; r < NR; ++r) {
        if (r + 1 < NR) { issue(r + 1); if ((r + 1) * 4 + 3 < NU) VMCNT(8); else VMCNT(0); } else if (MODE != 3) VMCNT(0);
        BAR();
#pragma unroll
        for (int k = 0; k < 4; ++k) {
            int uu, tl;
            if (MODE == 1) { uu = (k >> 1) * 2 + (wave >> 2); tl = 2 * (wave & 3) + (k & 1); } else { uu = k; tl = wave; }
            if (MODE != 1 && r * 4 + k >= NU) continue;
            v2u t[4];
#pragma unroll
            for (int j = 0; j < 4; ++j) {
                s16x4 v = __builtin_amdgcn_ds_read_tr16_b64_v4i16((LAS s16x4*)(lds + L_STAGE + (r & 1) * 65536 + uu * 16384 + ((j >> 1) * 8 + (j & 1)) * 1024 + rd0 + (tl ^ (gB & 1)) * 128));
                t[j] = __builtin_bit_cast(v2u, v);
            }
            sink(r * 4 + k, t);
        }
        BAR();
    }
}

constexpr u32 POLLCAP = 1u << 17;
#define POLL_COUNT "s_add_u32 %[cnt], %[cnt], 1\n s_cmp_lt_u32 %[cnt], %[cap]\n s_cselect_b32 %[key], %[key], -1\n"
DEV void gather_mem(const Ctx& cx, int gl, int s0, int s1, u32 ldst, u32& key) {
    u32 cnt = 0;
    asm volatile(
        "s_barrier\n"
        "1:\n"
        "buffer_load_dwordx4 v[116:119], %[gl], %[rs], %[s0] offen sc1\n"
        "buffer_load_dwordx4 v[120:123], %[gl], %[rs], %[s1] offen sc1\n"
        POLL_COUNT
        "s_waitcnt vmcnt(1)\n"
        "ds_write_b128 %[ld], v[116:119]\n"
        "v_max3_u32 v124, v116, v117, v118\n"
        "s_waitcnt vmcnt(0)\n"
        "ds_write_b128 %[ld], v[120:123] offset:1024\n"
        "v_max3_u32 v124, v124, v119, v120\n"
        "v_max3_u32 v124, v124, v121, v122\n"
        "v_max_u32 v124, v124, v123\n"
        "v_cmp_lt_u32 vcc, %[key], v124\n"
        "s_waitcnt lgkmcnt(0)\n"
        "s_cbranch_vccnz 1b\n"
        "s_barrier\n"
        : [cnt] "+s"(cnt), [key] "+s"(key)
        : [gl] "v"(gl), [rs] "s"(cx.ar), [s0] "s"(s0), [s1] "s"(s1), [ld] "v"(ldst), [cap] "s"(POLLCAP)
        : "memory", "scc", "vcc", "v116", "v117", "v118", "v119", "v120", "v121", "v122", "v123", "v124");
}
DEV void gather_mem4(const Ctx& cx, int gl, int s0, int s1, int s2, unsigned long long half, u32 ldst, u32 ldst2, u32& key) {
    u32 cnt = 0;
    asm volatile(
        "v_mov_b32 v112, 0\n v_mov_b32 v113, 0\n v_mov_b32 v114, 0\n v_mov_b32 v115, 0\n"
        "s_barrier\n"
        "1:\n"
        "buffer_load_dwordx4 v[116:119], %[gl], %[rs], %[s0] offen sc1\n"
        "buffer_load_dwordx4 v[120:123], %[gl], %[rs], %[s1] offen sc1\n"
        "s_mov_b64 exec, %[hf]\n"
        "buffer_load_dwordx4 v[112:115], %[gl], %[rs], %[s2] offen sc1\n"
        "s_mov_b64 exec, -1\n"
        POLL_COUNT
        "s_waitcnt vmcnt(2)\n"
        "ds_write_b128 %[ld], v[116:119]\n"
        "v_max3_u32 v124, v116, v117, v118\n"
        "s_waitcnt vmcnt(1)\n"
        "ds_write_b128 %[ld], v[120:123] offset:1024\n"
        "v_max3_u32 v124, v124, v119, v120\n"
        "v_max3_u32 v124, v124, v121, v122\n"
        "s_waitcnt vmcnt(0)\n"
        "ds_write_b128 %[ld2], v[112:115]\n"
        "v_max3_u32 v124, v124, v123, v112\n"
        "v_max3_u32 v124, v124, v113, v114\n"
        "v_max_u32 v124, v124, v115\n"
        "v_cmp_lt_u32 vcc, %[key], v124\n"
        "s_waitcnt lgkmcnt(0)\n"
        "s_cbranch_vccnz 1b\n"
        "s_barrier\n"
        : [cnt] "+s"(cnt), [key] "+s"(key)
        : [gl] "v"(gl), [rs] "s"(cx.ar), [s0] "s"(s0), [s1] "s"(s1), [s2] "s"(s2), [hf] "s"(half), [ld] "v"(ldst), [ld2] "v"(ldst2), [cap] "s"(POLLCAP)
        : "memory", "scc", "vcc", "v112", "v113", "v114", "v115", "v116", "v117", "v118", "v119", "v120", "v121", "v122", "v123", "v124");
}
DEV void load_mem0(const u16* mem0, unsigned char* lds, int tid) {
    for (int i = tid; i < 640; i += 512) { const int m = i / 80, c8 = i % 80; *(v4u*)(lds + L_MEMF + (c8 >> 1) * 256 + (2 * m + (c8 & 1)) * 16) = *(const v4u*)(mem0 + m * DM + c8 * 8); }
}

template <int CLS>
DEV void a_role(const Params& p, const Ctx& cx, unsigned char* lds, int h, int wave) {
    v8u wr[20];
    load_units<0>(p, h, lds, wave, lane_id(), [&](int slot, v2u (&t)[4]) { wr[slot] = (v8u){t[0].x, t[0].y, t[1].x, t[1].y, t[2].x, t[2].y, t[3].x, t[3].y}; });
    load_mem0(p.mem0, lds, wave * 64 + lane_id());
    for (int i = wave * 64 + lane_id(); i < 1024; i += 512) *(u32*)(lds + L_KMF + i * 4) = 0;
    const int par = cx.par, far = par ^ 1;
    EVT(2);
    const int lane = lane_id(), n = lane & 15, gq = lane >> 4;
    SIDX_DECL;
    const u32 lbase = (u32)(size_t)(LAS unsigned char*)lds + lane * 16;
    const int gl = lane * 16;
    const int qw = L_QF + (wave >> 2) * 1024 + (2 * ((wave >> 1) & 1) + (n >> 3)) * 256 + (2 * gq) * 32 + ((n >> 1) & 1) * 16 + (4 * ((n >> 2) & 1) + 2 * (wave & 1) + (n & 1)) * 2;
    const int kw = L_KMF + (wave >> 1) * 1024 + (16 * (n >> 2) + gq) * 16 + (4 * (wave & 1) + (n & 3)) * 2;
    const int pair = 8 * (n >> 3) + 2 * (n & 3) + ((n >> 2) & 1);
    const int tk0 = 2 * pair, tk1 = 2 * pair + 1;
    const u32 lq = (u32)(size_t)(LAS unsigned char*)lds + gq * 256 + (4 * (wave & 1) + (n >> 2)) * 32 + (n & 1) * 16;
    const int adpx = (n >> 3) * 256 + gq * 32 + (n & 7) * 4, upx = PX_O + h * 512 + (wave & 1) * 128;
    const int adpm = gq * 16 + (n & 3) * 4, upm = PM_O + h * 128 + (wave & 1) * 64;
    const int g8 = (wave - 2) & 3;
    const int t0 = 8 * g8 + gq;
    const int ql = gq * 256 + (((n >> 1) & 1) * 4 + (n >> 2)) * 32 + (n & 1) * 16;
    const int adtx = (n >> 3) * 256 + gq * 32 + (n & 7) * 4, utx = (g8 * 32 + h) * 512;
    const int adtm = gq * 16 + (n & 1) * 8 + ((n >> 1) & 1) * 4, utm = 65536 + (g8 * 32 + h) * 128;
    const float SC = 0.08838834764831845f * 1.4426950408889634f;
    const float NEGR = -1.0e9f / 0.08838834764831845f;
    const float pad0 = (n < 8) ? 0.f : -__builtin_inff();
    const bool pmlane = n < 4;
    const int adts = (lane & 31) * 64;
    int ready = 0;
    v4u qx0, qx1, qn0, qn1;
    f32x4 s1, s2;
    auto tok_load = [&](int sn) {
        const int qb = rfl(amap(QX_L + (sn * H + h) * 8192 + g8 * 2048, par));
        qn0 = ld128(cx.ar, ql, qb); qn1 = ld128(cx.ar, ql + 1024, qb);
    };
    auto tok_prep = [&](int sn) {
        qx0 = qn0; qx1 = qn1;
        v4u kx[8];
#pragma unroll
        for (int i = 0; i < 8; ++i) kx[i] = *(const v4u*)(lds + L_KXF + (sn & 1) * 8192 + i * 1024 + lane * 16);
        s1 = (f32x4){0, 0, 0, 0}; s2 = s1;
        asm volatile("s_nop 1" : "+v"(s1), "+v"(s2), "+v"(qx0), "+v"(qx1));
        SMF_V(s1, qx0, cat8(kx[0], kx[1]));
        SMF_V(s2, qx0, cat8(kx[4], kx[5]));
        SMF_V(s1, qx1, cat8(kx[2], kx[3]));
        SMF_V(s2, qx1, cat8(kx[6], kx[7]));
        asm volatile("s_nop 7" : "+v"(s1), "+v"(s2));
        s1.x = (tk0 <= t0) ? s1.x + s1.y : NEGR; s1.z = (tk0 <= t0 + 4) ? s1.z + s1.w : NEGR;
        s1.y = (tk1 <= t0) ? s2.x + s2.y : NEGR; s1.w = (tk1 <= t0 + 4) ? s2.z + s2.w : NEGR;
    };
    v4u kt[8], k1 = {0, 0, 0, 0};
    auto help_load = [&](int sn) {
        const int kb = rfl(amap(KX_L + (sn * H + h) * 8192, par));
#pragma unroll
        for (int i = 0; i < 8; ++i) kt[i] = ld128(cx.ar, gl + (i & 3) * 1024, kb + (i >> 2) * 4096);
    };
    auto help_store = [&](int sn) {
#pragma unroll
        for (int i = 0; i < 8; ++i) *(v4u*)(lds + L_KXF + (sn & 1) * 8192 + i * 1024 + lane * 16) = kt[i];
    };
    if (CLS == 2) {
        ready = wait_upto<0>(prog_load<0>(h, lane, cx.ctl), wave - 6, h, lane, cx.ctl);
        help_load(wave - 6); help_store(wave - 6);
    }
    BAR();
    if (CLS == 1) { tok_load(0); tok_prep(0); }
    Pace pc = {0u, p.g1 + 1640, 1640, 0, 0, 0, 0, false};
    u32 off = gnow() - now32() - 100u, key = 0xFFFFFFFEu;
    u32 dur = 1640, offp = 0, tp = 0;
    float slow = 0.04f;
    bool okoff = false, okp = false;
    const int gw = wave < 2 ? wave : wave - 4;
    const u32 gdst = (u32)(size_t)(LAS unsigned char*)lds + L_MEMF + gw * 2048 + gq * 256 + (n & 7) * 32 + (n >> 3) * 16;
    const u32 gdst2 = ((lane >> 5) == (gw & 1)) ? gdst - gw * 2048 + 8192 + (gw >> 1) * 1024 : (u32)(size_t)(LAS unsigned char*)lds + L_DUMP + lane * 16;
    for (int s = 0; s < NSEG; ++s) {
        const int sb = s * SLOT;
        f32x4 aq = {0, 0, 0, 0}, ak = {0, 0, 0, 0};
        asm volatile("" : "+v"(aq), "+v"(ak));
        const int xpx = rfl(amap(sb + upx, par)), xpm = rfl(amap(sb + upm, par)), xts = rfl(amap(sb + TS_O, par));
        const int xk1 = rfl(amap(KX_L + ((s == 0 ? 0 : min(s + 2, NSEG - 1)) * H + h) * 8192 + wave * 1024, par));
        if (s > 0) {
            const int x0 = rfl(amap(sb + MEM_O + gw * 2048, par)), x2 = rfl(amap(sb + MEM_O + 8192 + (gw >> 1) * 1024, par));
            asm volatile("s_waitcnt lgkmcnt(0)" :: "s"(x0), "s"(x2), "s"(xpx), "s"(xpm), "s"(xts), "s"(xk1) : "memory");
            DT(d_arr); DV(7, d_arr);
            asm volatile("s_waitcnt lgkmcnt(0)" ::: "memory");
            if (CLS == 2 && wave == 7) {
                off -= 40;
                okoff = wait_global(pc.p1, off) >= 4;
            }
            if (CLS != 1) gather_mem4(cx, gl, x0, x0 + 1024, x2, (gw & 1) ? 0xFFFFFFFF00000000ull : 0xFFFFFFFFull, gdst, gdst2, key);
            else { RAWBAR(); RAWBAR(); }
        } else BAR();
        if (CLS == 2 && wave == 7) { const u32 ts = min(now32() + off, 0xFFFFFFFEu); if (lane == 0) __builtin_amdgcn_raw_buffer_store_b64((v2u){ts, dur}, cx.ar, h * 64, xts, SC1); }
        {
            v4u a_[5];
#pragma unroll
            for (int ks = 0; ks < 5; ++ks) LDS_RD128(a_[ks], lbase, L_MEMF + ks * 1024);
#pragma unroll
            for (int ks = 0; ks < 5; ++ks) {
                if (ks == (wave < 4 ? 3 : 0)) {
                    if (CLS == 1) tok_load(min(s + 1, NSEG - 1));
                    k1 = ld128(cx.ar, gl, xk1);
                }
                LGKM_WAIT(4, a_[ks]); SMF_W(aq, a_[ks], 2 * ks); SMF_W(ak, a_[ks], 2 * ks + 1); LDS_RD128(a_[ks], lbase, L_MEMF + (ks + 5) * 1024);
            }
#pragma unroll
            for (int ks = 5; ks < 10; ++ks) { LGKM_WAIT(9 - ks, a_[ks - 5]); SMF_W(aq, a_[ks - 5], 2 * ks); SMF_W(ak, a_[ks - 5], 2 * ks + 1); }
        }
        v4u kx[8];
        if (CLS == 0) {
            const u32 lk = lbase + (s & 1) * 8192;
#pragma unroll
            for (int i = 0; i < 8; ++i) LDS_RD128(kx[i], lk, L_KXF + i * 1024);
        } else asm volatile("s_nop 7");
        asm volatile("" : "+v"(aq), "+v"(ak));
        {
            const u32 q2 = pk2(aq.x + aq.y, aq.z + aq.w), k2 = pk2(ak.x + ak.y, ak.z + ak.w);
            *(u16*)(lds + qw) = (u16)q2; *(u16*)(lds + qw + 32) = (u16)(q2 >> 16);
            *(u16*)(lds + kw) = (u16)k2; *(u16*)(lds + kw + 64) = (u16)(k2 >> 16);
        }
        f32x4 s0 = {pad0, 0, pad0, 0};
        v2u tv7 = {0, 0};
        if (CLS == 0) { s1 = (f32x4){0, 0, 0, 0}; s2 = s1; asm volatile("" : "+v"(s0), "+v"(s1), "+v"(s2) : "s"(xpx), "s"(xpm)); }
        if (CLS == 1) asm volatile("" : "+v"(s0));
        BAR();
        if (CLS == 0) {
            v4u a0, a1, km[4];
            LDS_RD128(a0, lq, L_QF); LDS_RD128(a1, lq, L_QF + 1024);
#pragma unroll
            for (int i = 0; i < 4; ++i) LDS_RD128(km[i], lbase, L_KMF + i * 1024);
            LGKM_WAIT(5, a0);
            SMF_V(s1, a0, cat8(kx[0], kx[1]));
            SMF_V(s2, a0, cat8(kx[4], kx[5]));
            LGKM_WAIT(4, a1);
            SMF_V(s1, a1, cat8(kx[2], kx[3]));
            SMF_V(s2, a1, cat8(kx[6], kx[7]));
            LGKM_WAIT2(2, km[0], km[1]);
            SMF_V(s0, a0, cat8(km[0], km[1]));
            LGKM_WAIT2(0, km[2], km[3]);
            SMF_V(s0, a1, cat8(km[2], km[3]));
            asm volatile("s_nop 4" : "+v"(s1), "+v"(s2));
            float x1 = s1.x + s1.y, x2 = s2.x + s2.y;
            asm volatile("s_nop 0" : "+v"(s0), "+v"(x1), "+v"(x2));
            const float x0 = s0.x + s0.y;
            const float mx = -SC * dmax16(max3f(x0, x1, x2));
            const float e0 = __builtin_amdgcn_exp2f(__builtin_fmaf(x0, SC, mx)), e1 = __builtin_amdgcn_exp2f(__builtin_fmaf(x1, SC, mx)), e2 = __builtin_amdgcn_exp2f(__builtin_fmaf(x2, SC, mx));
            const float inv = __builtin_amdgcn_rcpf(rowsum16(e0 + e1 + e2));
            const float p0 = e0 * inv;
            st32(pk2s(e1 * inv, e2 * inv), cx.ar, adpx, xpx);
            const u32 pm = pk2s(p0, dppf<0x104>(p0));
            if (pmlane) st32(pm, cx.ar, adpm, xpm);
            DT(d_pub); DV(3, d_pub);
        } else if (CLS == 1) {
            __builtin_amdgcn_s_sleep(4);
            if (s > 0) *(v4u*)(lds + L_KXF + (s & 1) * 8192 + wave * 1024 + lane * 16) = k1;
            asm volatile("" : "+v"(qn0), "+v"(qn1) :: "memory");
            v4u km[4];
#pragma unroll
            for (int i = 0; i < 4; ++i) km[i] = *(const v4u*)(lds + L_KMF + i * 1024 + lane * 16);
            SMF_V(s0, qx0, cat8(km[0], km[1]));
            SMF_V(s0, qx1, cat8(km[2], km[3]));
            asm volatile("s_nop 7" : "+v"(s0));
            const int xtx = rfl(amap(PT_L + s * PT_STEP + utx, far)), xtm = rfl(amap(PT_L + s * PT_STEP + utm, far));
#pragma unroll
            for (int rr = 0; rr < 2; ++rr) {
                const float x0 = rr ? s0.z + s0.w : s0.x + s0.y, x1 = rr ? s1.z : s1.x, x2 = rr ? s1.w : s1.y;
                const float mx = -SC * dmax16(max3f(x0, x1, x2));
                const float e0 = __builtin_amdgcn_exp2f(__builtin_fmaf(x0, SC, mx)), e1 = __builtin_amdgcn_exp2f(__builtin_fmaf(x1, SC, mx)), e2 = __builtin_amdgcn_exp2f(__builtin_fmaf(x2, SC, mx));
                const float inv = __builtin_amdgcn_rcpf(rowsum16(e0 + e1 + e2));
                const float p0 = e0 * inv;
                st32(pk2(e1 * inv, e2 * inv), cx.ar, adtx + rr * 128, xtx);
                const u32 pm = pk2(p0, dppf<0x104>(p0));
                if (pmlane) st32(pm, cx.ar, adtm + rr * 64, xtm);
            }
            if (s + 1 < NSEG) tok_prep(s + 1);
        } else {
            if (wave == 6) {
                if (s == 0) { ready = wait_upto<0>(prog_load<0>(h, lane, cx.ctl), 2, h, lane, cx.ctl); help_load(2); help_store(2); }
                if (s + 3 >= ready && ready < NSEG) ready = wait_upto<0>(prog_load<0>(h, lane, cx.ctl), min(s + 3, NSEG - 1), h, lane, cx.ctl);
            } else tv7 = ts_load(cx, adts, xts);
        }
        if (CLS != 1 && s > 0) *(v4u*)(lds + L_KXF + (s & 1) * 8192 + wave * 1024 + lane * 16) = k1;
        if (CLS == 2 && wave == 7) {
            if (okoff) {
                if (okp) slow = 0.75f * slow + 0.25f * fminf(fmaxf((float)(int)(off - offp) / (float)(int)(pc.p1 - tp), -0.02f), 0.4f);
                offp = off; tp = pc.p1; okp = true;
                dur = rfl((int)(A_CYCLES / (1.f - slow)));
            }
            pace_next(pc, tv7, cx, adts, xts, s, p);
            pace_dur(pc, tv7);
        }
    }
    if (key == SENT) __hip_atomic_store(cx.ctl + CW_TMO, 1u, RLX_AGENT);
    VMCNT(0);
    BAR();
    if (CLS == 2 && wave == 7 && lane == 0) __hip_atomic_store(cx.ctl + CW_FIN + 16 * h, 1u, RLX_AGENT);
    DFLUSH();
}

template <bool TW>
DEV void r_role(const Params& p, const Ctx& cx, unsigned char* lds, int c8, int wave) {
    const int lane = lane_id(), n = lane & 15, gq = lane >> 4;
    const int par = cx.par;
    v8u wr[20];
    {
        const int vb = (((n >> 3) * 20) * 40) * 1024 + (16 * gq + (n & 7)) * 16;
        const int vs = VO_P + ((4 * wave * 20) * 40 + (c8 >> 1)) * 1024 + (c8 & 1) * 128;
#pragma unroll
        for (int i = 0; i < 20; ++i) {
            const int o = vs + (i & 1) * (2 * 20 * 40960) + (i >> 1) * (2 * 40960);
            wr[i] = cat8(__builtin_amdgcn_raw_buffer_load_b128(cx.ar, vb, o, 0), __builtin_amdgcn_raw_buffer_load_b128(cx.ar, vb, o + 40960, 0));
        }
    }
    load_mem0(p.mem0, lds, wave * 64 + lane);
    EVT(3);
    SIDX_DECL;
    const u32 lbase = (u32)(size_t)(LAS unsigned char*)lds + lane * 16;
    const int gl = lane * 16;
    const int zl = gq * 128 + (n & 7) * 16;
    const u32 ztw = (u32)(size_t)(LAS unsigned char*)lds + L_ZT + wave * 512 + (n >> 3) * 128 + (n & 7) * 16 + gq * 4, ztr = (u32)(size_t)(LAS unsigned char*)lds + L_ZT + wave * 512 + zl;
    const int pml = gq * 128 + (n >> 1) * 16;
    const u32 redw = (u32)(size_t)(LAS unsigned char*)lds + L_RED + (n >> 3) * 2560 + ((2 * gq) * 4 + ((n & 7) >> 1)) * 80 + wave * 8 + (n & 1) * 4;
    const u32 gdst = (u32)(size_t)(LAS unsigned char*)lds + L_MEMF + wave * 2048 + gq * 256 + (n & 7) * 32 + (n >> 3) * 16;
    u32 key = 0xFFFFFFFEu;
    const int redr = L_RED + (lane & 31) * 80;
    const int adts = (lane & 31) * 64;
    int ready = 0;
    if (!TW && wave == 6) ready = wait_upto<1>(prog_load<1>(0, lane, cx.ctl), 1, 0, lane, cx.ctl);
    BAR();
    v4u zn0 = ld128(cx.ar, gl, ZX_P + c8 * 16384 + wave * 2048), zn1 = ld128(cx.ar, gl + 1024, ZX_P + c8 * 16384 + wave * 2048);
    Pace pc = {0u, p.g1 + 1640, 1640, 0, 0, 0, 0, false};
    v2u tv = {0, 1640};
    u32 off = gnow() - now32() - 100u;
    for (int s = 0; s < NSEG; ++s) {
        const int sb = s * SLOT;
        f32x4 z0 = {0, 0, 0, 0}, z1 = z0;
        asm volatile("" : "+v"(z0), "+v"(z1));
        const int x0 = rfl(amap(sb + MEM_O + wave * 2048, par)), x1 = rfl(amap(sb + MEM_O + wave * 2048 + 1024, par));
        const int xzx = ZX_P + min(s + 1, NSEG - 1) * ZX_STEP + c8 * 16384 + wave * 2048;
        v8u zb0 = cat8(zn0, zn1), zb1;
#pragma unroll
        for (int i = 0; i < 8; ++i) zb1[i] = __builtin_amdgcn_update_dpp(0, zb0[i], 0x108, 0xf, 0xf, true);
        asm volatile("" : "+v"(zb0), "+v"(zb1) : "s"(x0), "s"(x1), "s"(xzx));
        if (TW && s > 0) wait_global(pc.p1 + p.pad, off);
        int pv = 64;
        if (s > 0 && wave < 5) gather_mem(cx, gl, x0, x1, gdst, key);
        else {
            BAR();
            if (!TW && wave == 6 && s + 2 >= ready) { __builtin_amdgcn_s_sleep(4); pv = prog_load<1>(0, lane, cx.ctl); }
            RAWBAR();
        }
        const int xts = rfl(amap(sb + TS_O, par));
        bool late = false;
        DT(d_b1);
        {
            v4u a_[5];
#pragma unroll
            for (int ks = 0; ks < 5; ++ks) LDS_RD128(a_[ks], lbase, L_MEMF + ks * 1024);
            zn0 = ld128(cx.ar, gl, xzx); zn1 = ld128(cx.ar, gl + 1024, xzx);
#pragma unroll
            for (int ks = 0; ks < 5; ++ks) { LGKM_WAIT(4, a_[ks]); SMF_W(z0, a_[ks], 2 * ks); SMF_W(z1, a_[ks], 2 * ks + 1); LDS_RD128(a_[ks], lbase, L_MEMF + (ks + 5) * 1024); }
#pragma unroll
            for (int ks = 5; ks < 10; ++ks) { LGKM_WAIT(9 - ks, a_[ks - 5]); SMF_W(z0, a_[ks - 5], 2 * ks); SMF_W(z1, a_[ks - 5], 2 * ks + 1); }
        }
        const int xzm = rfl(amap(ZM_L + s * ZM_STEP + c8 * 4096 + wave * 512, par));
        const int xpub = rfl(amap(sb + SLOT + MEM_O + c8 * 128, par));
        const int xx0 = rfl(amap(sb + PX_O + wave * 2048, par)), xx1 = rfl(amap(sb + PX_O + wave * 2048 + 1024, par)), xm = rfl(amap(sb + PM_O + wave * 512, par));
        asm volatile("s_nop 7" : "+v"(z0), "+v"(z1) : "s"(xx0), "s"(xx1), "s"(xm), "s"(xts), "s"(xpub), "s"(xzm));
        const u32 zt0 = pk2(z0.x + z0.y, z0.z + z0.w), zt1 = pk2(z1.x + z1.y, z1.z + z1.w);
        if (s + 1 == NSEG) {
            *(LAS u32*)(size_t)ztw = zt0; *(LAS u32*)(size_t)(ztw + 256) = zt1;
            asm volatile("s_waitcnt lgkmcnt(0)" ::: "memory");
            if (n < 8) st128(*(const LAS v4u*)(size_t)ztr, cx.ar, zl, xzm);
            break;
        }
        DT(d_zp);
        u32 dsp = 0;
        DV(6, pc.p1);
        DT(d_arr); DV(7, d_arr);
        if (TW) {
            late = s == 0 || (int)(gnow() - pc.p1) > p.glate;
            pace_dur(pc, tv);
            tv = ts_load(cx, adts, xts);
            if (!late) {
                off -= 40;
                wait_global(pc.p1 + pc.c2 - 220, off);
                if ((__builtin_amdgcn_s_getreg(7 | (31 << 11)) & 0x00C0000F) == 0) late = __any(tv.x == SENT || (int)(tv.x - pc.p1) > p.g1 - 30);
            }
            wait_global(late ? read_ref(tv, cx, adts, xts) + pc.c2 - p.cp : pc.p1 + pc.c2, off);
        }
        {
            u32 cnt = 0;
            asm volatile(
                "v_mov_b32 v116, 0\n v_mov_b32 v117, 0\n v_mov_b32 v118, 0\n v_mov_b32 v119, 0\n"
                "s_barrier\n"
                "1:\n"
                "buffer_load_dwordx4 v[108:111], %[gl], %[rs], %[x0] offen sc1\n"
                "buffer_load_dwordx4 v[112:115], %[gl], %[rs], %[x1] offen sc1\n"
                "s_mov_b64 exec, %[ev]\n"
                "buffer_load_dwordx4 v[116:119], %[pml], %[rs], %[xm] offen sc1\n"
                "s_mov_b64 exec, -1\n"
                "ds_write2st64_b32 %[zw], %[zt0], %[zt1] offset1:1\n"
                POLL_COUNT
                "s_waitcnt lgkmcnt(0)\n"
                "ds_read_b128 v[104:107], %[zr]\n"
                "v_mov_b32 v120, 0\n v_mov_b32 v121, 0\n v_mov_b32 v122, 0\n v_mov_b32 v123, 0\n"
                "s_mov_b64 exec, %[lo8]\n"
                "s_waitcnt lgkmcnt(0)\n"
                "buffer_store_dwordx4 v[104:107], %[zl], %[rs], %[xz] offen sc1\n"
                "s_mov_b64 exec, -1\n"
                "s_waitcnt vmcnt(3)\n"
                "v_smfmac_f32_16x16x64_bf16 v[120:123], v[108:111], %[zb0], %[si]\n"
                "v_max3_u32 v124, v108, v109, v110\n"
                "s_waitcnt vmcnt(2)\n"
                "v_smfmac_f32_16x16x64_bf16 v[120:123], v[112:115], %[zb1], %[si]\n"
                "v_max3_u32 v124, v124, v111, v112\n"
                "v_max3_u32 v124, v124, v113, v114\n"
                "s_waitcnt vmcnt(1)\n"
                "v_mfma_f32_16x16x32_bf16 v[120:123], v[116:119], v[104:107], v[120:123]\n"
                "v_max3_u32 v124, v124, v115, v116\n"
                "v_max3_u32 v124, v124, v117, v118\n"
                "v_max_u32 v124, v124, v119\n"
                "v_cmp_lt_u32 vcc, %[key], v124\n"
                "s_nop 2\n"
                "s_cbranch_vccnz 1b\n"
                "v_add_f32 v124, v120, v121\n"
                "v_add_f32 v125, v122, v123\n"
                "ds_write2_b32 %[rw], v124, v125 offset1:80\n"
                "s_waitcnt lgkmcnt(0)\n"
                "s_barrier\n"
                : [cnt] "+s"(cnt), [key] "+s"(key)
                : [gl] "v"(gl), [pml] "v"(pml), [rs] "s"(cx.ar), [x0] "s"(xx0), [x1] "s"(xx1), [xm] "s"(xm), [xz] "s"(xzm), [ev] "s"(0x5555555555555555ull), [lo8] "s"(0x00FF00FF00FF00FFull),
                  [cap] "s"(POLLCAP), [zb0] "v"(zb0), [zb1] "v"(zb1), [si] "v"(sidx), [rw] "v"(redw), [zw] "v"(ztw), [zr] "v"(ztr), [zl] "v"(zl), [zt0] "v"(zt0), [zt1] "v"(zt1)
                : "memory", "scc", "vcc", "v104", "v105", "v106", "v107", "v108", "v109", "v110", "v111", "v112", "v113", "v114", "v115", "v116", "v117", "v118", "v119", "v120", "v121", "v122", "v123", "v124", "v125");
            dsp = cnt - 1;
        }
        if (!TW && wave == 7) {
            const unsigned char* rb = lds + redr;
            f32x4 r0 = *(const f32x4*)(rb), r1 = *(const f32x4*)(rb + 16), r2 = *(const f32x4*)(rb + 32), r3 = *(const f32x4*)(rb + 48);
            r0 = (r0 + r1) + (r2 + r3);
            if (lane < 32) st32(pk2s(r0.x + r0.z, r0.y + r0.w), cx.ar, lane * 4, xpub);
        }
        DT(d_pub);
        DV(0, d_b1); DV(1, d_zp); DV(2, d_pub); DV(3, d_pub); DV(4, late); DV(5, dsp);
        if (TW) pace_next(pc, tv, cx, adts, xts, s, p);
        if (!TW && wave == 6 && s + 2 >= ready && ready < NSEG) ready = wait_upto<1>(pv, min(s + 2, NSEG - 1), 0, lane, cx.ctl);
    }
    if (key == SENT) __hip_atomic_store(cx.ctl + CW_TMO, 1u, RLX_AGENT);
    VMCNT(0);
    BAR();
    if (!TW && wave == 6 && lane == 0) __hip_atomic_store(cx.ctl + CW_FIN + 16 * (32 + c8), 1u, RLX_AGENT);
    DFLUSH();
}

constexpr int L_XT = L_STAGE, L_VI = 0;
template <int TYPE>
DEV void l_worker(const Params& p, const Ctx& cx, unsigned char* lds, int h, int second, int wave) {
    constexpr int K1 = TYPE == 0 ? 3 : 14;
    v4u w4[40];
    if (TYPE == 0) load_units<1>(p, h, lds, wave, lane_id(), [&](int slot, v2u (&t)[4]) {
        const int ks = slot >> 1, tau = slot & 1;
        w4[(2 * ks) * 2 + tau] = (v4u){t[0].x, t[0].y, t[1].x, t[1].y}; w4[(2 * ks + 1) * 2 + tau] = (v4u){t[2].x, t[2].y, t[3].x, t[3].y}; });
    else load_units<2>(p, h, lds, wave, lane_id(), [&](int slot, v2u (&t)[4]) {
        w4[2 * slot] = (v4u){t[0].x, t[0].y, t[1].x, t[1].y}; w4[2 * slot + 1] = (v4u){t[2].x, t[2].y, t[3].x, t[3].y}; });
    EVT(1);
    const int lane = lane_id();
    const u32 lds0 = (u32)(size_t)(LAS unsigned char*)lds;
    const int i = lane & 15, gq = lane >> 4;
    const rsrc_t rxin = mkrs(p.x, S * DM * 2);
    const int xrow = (wave & 3) * 8 + (lane >> 3);
    const int xvo = xrow * (DM * 2) + (((lane & 7) ^ ((xrow >> 1) & 7)) * 16) + (5 * (wave >> 2)) * 128;
    const u32 xld = lds0 + L_XT + (5 * (wave >> 2)) * 4096 + (wave & 3) * 1024;
    auto issue1 = [&](int s, int buf, int k) { dma16(rfl(xld + buf * 40960 + k * 4096), xvo, rxin, rfl(min(s, NSEG - 1) * (32 * DM * 2) + k * 128)); };
    const int nit = second ? K1 : NSEG - K1;
    auto seg_of = [&](int it) { return second ? (it < K1 ? 2 * it + 1 : NSEG) : it + min(it, K1); };
#pragma unroll
    for (int k = 0; k < 5; ++k) issue1(seg_of(0), 0, k);
#pragma unroll
    for (int k = 0; k < 5; ++k) issue1(seg_of(1), 1, k);
    const int sel = cx.par;
    const u32 xb0 = lds0 + L_XT + i * 128 + ((gq ^ (i >> 1)) * 16), xb1 = lds0 + L_XT + i * 128 + (((4 + gq) ^ (i >> 1)) * 16);
    int ost0, ost1, ubase;
    if (TYPE == 0) {
        const int hp = wave & 3;
        if (wave < 4) { ost0 = (i >> 3) * 2048 + (hp >> 1) * 1024 + (16 * (2 * (hp & 1) + (gq >> 1)) + 2 * (i & 7) + (gq & 1)) * 16; ost1 = ost0 + 4096; ubase = QX_L + h * 8192; }
        else { const int ip = i >> 1; ost0 = (i & 1) * 4096 + hp * 1024 + (16 * gq + 4 * (ip & 1) + ((ip >> 1) & 3)) * 16; ost1 = ost0 + 128; ubase = KX_L + h * 8192; }
    } else { ost0 = (i >> 3) * 16384 + (16 * gq + (i & 7)) * 16; ost1 = 0; ubase = ZX_P + wave * 32768 + (h >> 2) * 2048 + (h & 1) * 1024 + ((h >> 1) & 1) * 128; }
    const int viw = L_VI + i * 256 + (((4 * (wave >> 1) + gq) ^ i) * 16) + (wave & 1) * 8;
    const int vtok = 8 * (i >> 2) + (i & 3);
    auto signal = [&](int it) { if (wave == 0 && lane == 0) __hip_atomic_store(cx.ctl + CW_P + 16 * (64 * TYPE + 2 * h + second), (u32)seg_of(it + 1), RLX_AGENT); };
    auto phase1 = [&](int it, int buf) {
        const int bn = buf == 0 ? 2 : buf - 1, sn = seg_of(it + 2);
        const u32 x0 = xb0 + buf * 40960, x1 = xb1 + buf * 40960;
        f32x4 d00 = {0, 0, 0, 0}, d01 = d00, d10 = d00, d11 = d00;
        asm volatile("" : "+v"(d00), "+v"(d01), "+v"(d10), "+v"(d11));
        v4u b[8];
#define XRD(slot, ss) do { LDS_RD128(b[2 * (slot)], ((ss) & 1) ? x1 : x0, ((ss) >> 1) * 4096); LDS_RD128(b[2 * (slot) + 1], ((ss) & 1) ? x1 : x0, ((ss) >> 1) * 4096 + 2048); } while (0)
#pragma unroll
        for (int ss = 0; ss < 4; ++ss) XRD(ss, ss);
#pragma unroll
        for (int ss = 0; ss < 20; ++ss) {
            const int sl = ss & 3;
            if (ss < 17) LGKM_WAIT2(6, b[2 * sl], b[2 * sl + 1]); else LGKM_WAIT2(2 * (19 - ss), b[2 * sl], b[2 * sl + 1]);
            if (TYPE == 0) { MF_WA(d00, 2 * ss, b[2 * sl]); MF_WA(d01, 2 * ss, b[2 * sl + 1]); MF_WA(d10, 2 * ss + 1, b[2 * sl]); MF_WA(d11, 2 * ss + 1, b[2 * sl + 1]); }
            else { MF_WA(d00, ss, b[2 * sl]); MF_WA(d01, ss, b[2 * sl + 1]); }
            if (ss + 4 < 20) XRD(sl, ss + 4);
            if (TYPE == 0 && ss == 8) { VMCNT(0); RAWBAR(); if (it > 0) signal(it - 1); }
            if (TYPE == 0 && ss >= 9 && ss < 19 && (ss & 1)) issue1(sn, bn, (ss - 9) >> 1);
            if (TYPE == 1 && ss % 4 == 1) issue1(sn, bn, ss >> 2);
        }
#undef XRD
        asm volatile("s_nop 7" : "+v"(d00), "+v"(d01), "+v"(d10), "+v"(d11));
        if (TYPE == 0) {
            const int so = rfl(amap(ubase + seg_of(it) * (H * 8192), sel));
            u32 a0 = pk2(d00.x, d00.y), a1 = pk2(d00.z, d00.w), a2 = pk2(d10.x, d10.y), a3 = pk2(d10.z, d10.w);
            u32 c0 = pk2(d01.x, d01.y), c1 = pk2(d01.z, d01.w), c2 = pk2(d11.x, d11.y), c3 = pk2(d11.z, d11.w);
            if (wave < 4) {
                auto sa = __builtin_amdgcn_permlane16_swap(a0, a1, false, false), sb = __builtin_amdgcn_permlane16_swap(a2, a3, false, false);
                auto sc = __builtin_amdgcn_permlane16_swap(c0, c1, false, false), sd = __builtin_amdgcn_permlane16_swap(c2, c3, false, false);
                st128((v4u){sa[0], sb[0], sa[1], sb[1]}, cx.ar, ost0, so);
                st128((v4u){sc[0], sd[0], sc[1], sd[1]}, cx.ar, ost1, so);
            } else {
                st128((v4u){a0, a1, a2, a3}, cx.ar, ost0, so);
                st128((v4u){c0, c1, c2, c3}, cx.ar, ost1, so);
            }
        } else {
            *(v2u*)(lds + viw + (it & 1) * 8192) = (v2u){pk2(d00.x, d00.y), pk2(d00.z, d00.w)};
            *(v2u*)(lds + viw + (it & 1) * 8192 + 4096) = (v2u){pk2(d01.x, d01.y), pk2(d01.z, d01.w)};
        }
    };
    auto phase2 = [&](int it, bool mid) {
        const int ub = ubase + seg_of(it) * ZX_STEP;
        v4u a[8];
#pragma unroll
        for (int k = 0; k < 8; ++k) { const int tok = vtok + 4 * (k >> 2); a[k] = *(const v4u*)(lds + L_VI + (it & 1) * 8192 + tok * 256 + (((4 * (k & 3) + gq) ^ (tok & 15)) * 16)); }
        f32x4 d0[5], d1[5];
#pragma unroll
        for (int cg = 0; cg < 5; ++cg) { d0[cg] = (f32x4){0, 0, 0, 0}; d1[cg] = d0[cg]; asm volatile("" : "+v"(d0[cg]), "+v"(d1[cg])); }
        asm volatile("s_nop 1");
#pragma unroll
        for (int cg = 0; cg < 6; ++cg) {
            if (cg < 5) {
#pragma unroll
                for (int ss = 0; ss < 4; ++ss) { MF_WB(d0[cg], a[ss], 20 + cg * 4 + ss); MF_WB(d1[cg], a[4 + ss], 20 + cg * 4 + ss); }
            } else asm volatile("s_nop 7");
            if (cg == 0 && mid) { VMCNT(5); BAR(); if (it > 0) signal(it - 1); }
            if (cg > 0) {
                asm volatile("" : "+v"(d0[cg - 1]), "+v"(d1[cg - 1]));
                st128((v4u){pk2(d0[cg - 1].x, d0[cg - 1].y), pk2(d0[cg - 1].z, d0[cg - 1].w), pk2(d1[cg - 1].x, d1[cg - 1].y), pk2(d1[cg - 1].z, d1[cg - 1].w)},
                      cx.ar, ost0, ub + (cg - 1) * (8 * 32768));
            }
        }
    };
    VMCNT(0);
    BAR();
    if (TYPE == 0) {
        int buf = 0;
        for (int it = 0; it < nit; ++it) {
            phase1(it, buf);
            buf = buf == 2 ? 0 : buf + 1;
            if (it == 0) { VMCNT(0); BAR(); signal(0); EVT(2); }
        }
    } else {
        phase1(0, 0);
        BAR();
        phase2(0, false);
        VMCNT(0); BAR(); signal(0);
        EVT(2);
        phase1(1, 1);
        VMCNT(5); BAR();
        int buf = 2;
        for (int k = 1; k < nit; ++k) {
            phase1(k + 1, buf);
            phase2(k, true);
            buf = buf == 2 ? 0 : buf + 1;
        }
    }
    VMCNT(0);
    BAR();
    signal(nit - 1);
}

constexpr int SS_K = 33 * 144, SS_U = 4288, SS_CH = 2 * SS_K + 2 * SS_U + 32;
static_assert(SS_CH % 16 == 0 && 8 * SS_CH <= L_TOTAL, "ssm lds");
DEV void ssm_task(const Params& p, const Ctx& cx, unsigned char* lds, int task, int wave, int far) {
    const int lane = lane_id(), tid = wave * 64 + lane;
    const int d = task * 8 + wave;
    unsigned char* ch = lds + wave * SS_CH;
    unsigned char* tmpb = ch + 2 * SS_K;
    const int i = lane & 15, gq = lane >> 4;
    v4u xr[2][3];
    {
        const float dt = rbf(__expf(bf2f(p.logdt[d])));
#pragma unroll
        for (int k = 0; k < 2; ++k) {
            const int nn = lane + 64 * k, o = d * NS + nn;
            const float br = dt * bf2f(p.Bre[o]), bi = dt * bf2f(p.Bim[o]), cr = bf2f(p.Cre[o]), ci = bf2f(p.Cim[o]);
            *(f32x4*)(tmpb + nn * 16) = (f32x4){dt * bf2f(p.Are[o]) * 1.4426950408889634f, dt * bf2f(p.Aim[o]) * 0.15915494309189535f, cr * br - ci * bi, cr * bi + ci * br};
        }
        asm volatile("s_waitcnt lgkmcnt(0)" ::: "memory");
        {
            const rsrc_t rxin = mkrs(p.x, S * DM * 2);
#pragma unroll
            for (int k = 0; k < 2; ++k)
#pragma unroll
                for (int j = 0; j < 3; ++j) xr[k][j] = __builtin_amdgcn_raw_buffer_load_b128(rxin, ((2 * (tid + 512 * k) + j) * DM + task * 8) * 2, 0, 0);
            __builtin_amdgcn_sched_barrier(0);
        }
        const float lu = 16.f * i;
        const float lv0 = (float)i;
        const float lv1 = i < 8 ? 256.f + 2.f * i : 512.f + 256.f * ((i - 8) >> 2) + 4.f * (i & 3);
        const float lv2 = i < 8 ? 1024.f + 256.f * (i >> 1) + 8.f * (i & 1) : 2048.f;
        f32x4 k0 = {0, 0, 0, 0}, k1 = k0, k2 = k0;
#pragma unroll 2
        for (int it = 0; it < 32; ++it) {
            const f32x4 q = *(const f32x4*)(tmpb + (4 * it + gq) * 16);
            auto cexp = [&](float l, float& re, float& im) {
                const float e = __builtin_amdgcn_exp2f(q.x * l), th = __builtin_amdgcn_fractf(q.y * l);
                re = e * __builtin_amdgcn_cosf(th); im = e * __builtin_amdgcn_sinf(th);
            };
            float er, ei, r0, i0, r1, i1, r2, i2;
            cexp(lu, er, ei); cexp(lv0, r0, i0); cexp(lv1, r1, i1); cexp(lv2, r2, i2);
            const float ur = q.z * er - q.w * ei, nui = -(q.z * ei + q.w * er);
            k0 = __builtin_amdgcn_mfma_f32_16x16x4f32(ur, r0, k0, 0, 0, 0); k1 = __builtin_amdgcn_mfma_f32_16x16x4f32(ur, r1, k1, 0, 0, 0); k2 = __builtin_amdgcn_mfma_f32_16x16x4f32(ur, r2, k2, 0, 0, 0);
            k0 = __builtin_amdgcn_mfma_f32_16x16x4f32(nui, i0, k0, 0, 0, 0); k1 = __builtin_amdgcn_mfma_f32_16x16x4f32(nui, i1, k1, 0, 0, 0); k2 = __builtin_amdgcn_mfma_f32_16x16x4f32(nui, i2, k2, 0, 0, 0);
        }
        float* tab = (float*)(tmpb + 2048);
        *(f32x4*)(tab + i * 20 + 4 * gq) = k0; *(f32x4*)(tab + (16 + i) * 20 + 4 * gq) = k1; *(f32x4*)(tab + (32 + i) * 20 + 4 * gq) = k2;
        asm volatile("s_waitcnt lgkmcnt(0)" ::: "memory");
        if (lane < 36) { *(u32*)(ch + lane * 4) = 0; *(u32*)(ch + SS_K + lane * 4) = 0; }
#pragma unroll 2
        for (int it = 0; it < 16; ++it) {
            const int l0 = it * 128 + lane * 2;
            float kv[2];
#pragma unroll
            for (int e = 0; e < 2; ++e) {
                const int lr = (int)rbf((float)(l0 + e));
                const int a = (lr >> 4) & 15, j = lr & 15, hi = lr >> 8;
                int col;
                if (hi == 0) col = j; else if (hi == 1) col = 16 + (j >> 1); else if (hi < 4) col = 24 + 4 * (hi - 2) + (j >> 2); else if (hi < 8) col = 32 + 2 * (hi - 4) + (j >> 3); else col = 40;
                kv[e] = tab[col * 20 + a];
            }
            const float h0 = rbf(kv[0]), h1 = rbf(kv[1]);
            const int o = ((l0 >> 6) + 1) * 144 + ((l0 & 56) + 6 - (l0 & 6)) * 2;
            *(u32*)(ch + o) = pk2(h1, h0);
            *(u32*)(ch + SS_K + o) = pk2(kv[1] - h1, kv[0] - h0);
        }
    }
    EVT(8);
    BAR();
    {
        if (lane < 24) { *(u32*)(ch + 2 * SS_K + lane * 4) = 0; if (lane < 23) *(u32*)(ch + 2 * SS_K + SS_U + lane * 4) = 0; }
#pragma unroll
        for (int k = 0; k < 2; ++k) {
            const int l = 2 * (tid + 512 * k);
#pragma unroll
            for (int c = 0; c < 8; ++c) {
                const u32 sel = (c & 1) ? 0x07060302u : 0x05040100u;
                unsigned char* ub = lds + c * SS_CH + 2 * SS_K;
                *(u32*)(ub + (l + 48) * 2) = __builtin_amdgcn_perm(xr[k][1][c >> 1], xr[k][0][c >> 1], sel);
                *(u32*)(ub + SS_U + (l + 48) * 2) = __builtin_amdgcn_perm(xr[k][2][c >> 1], xr[k][1][c >> 1], sel);
                if (k == 0 && tid == 0) *(u32*)(ub + SS_U + 46 * 2) = __builtin_amdgcn_perm(xr[0][0][c >> 1], 0u, sel);
            }
        }
    }
    BAR();
    EVT(9);
    f32x4 acc[2][4];
#pragma unroll
    for (int I = 0; I < 2; ++I)
#pragma unroll
        for (int rt = 0; rt < 4; ++rt) acc[I][rt] = (f32x4){0, 0, 0, 0};
    {
        const int st0 = i - 8 * gq - 7 + 48;
        const unsigned char* ua = ch + 2 * SS_K + ((st0 & 1) ? SS_U + (st0 - 1) * 2 : st0 * 2);
        auto afrag = [&](int o) { const u32* q = (const u32*)(ua + o * 32); return (v4u){q[0], q[1], q[2], q[3]}; };
        v4u am2 = {0, 0, 0, 0};
        v4u am1 = afrag(-1);
        const int kb = gq * 16;
        auto step = [&](int t, int I, const v4u& a0, const v4u& a1, const v4u& a2, const v4u& a3) {
            const int blk = max(16 * I + i - t + 1, 0);
            const unsigned char* kp = ch + blk * 144 + kb;
#pragma unroll
            for (int hl = 0; hl < 2; ++hl) {
                const v4u b0 = *(const v4u*)(kp + hl * SS_K), b1 = *(const v4u*)(kp + hl * SS_K + 64);
                acc[I][0] = mfma16(a0, b0, acc[I][0]); acc[I][1] = mfma16(a1, b0, acc[I][1]); acc[I][2] = mfma16(a2, b0, acc[I][2]); acc[I][3] = mfma16(a3, b0, acc[I][3]);
                acc[I][0] = mfma16(am2, b1, acc[I][0]); acc[I][1] = mfma16(am1, b1, acc[I][1]); acc[I][2] = mfma16(a0, b1, acc[I][2]); acc[I][3] = mfma16(a1, b1, acc[I][3]);
            }
        };
#pragma unroll 1
        for (int t = 0; t < 16; ++t) {
            const v4u a0 = afrag(4 * t), a1 = afrag(4 * t + 1), a2 = afrag(4 * t + 2), a3 = afrag(4 * t + 3);
            step(t, 0, a0, a1, a2, a3); step(t, 1, a0, a1, a2, a3);
            am2 = a2; am1 = a3;
        }
#pragma unroll 1
        for (int t = 16; t < 32; ++t) {
            const v4u a0 = afrag(4 * t), a1 = afrag(4 * t + 1), a2 = afrag(4 * t + 2), a3 = afrag(4 * t + 3);
            step(t, 1, a0, a1, a2, a3);
            am2 = a2; am1 = a3;
        }
    }
    EVT(10);
    {
        const float dsk = bf2f(p.dskip[d]);
        asm volatile("s_waitcnt lgkmcnt(0)" ::: "memory");
#pragma unroll
        for (int I = 0; I < 2; ++I)
#pragma unroll
            for (int rt = 0; rt < 4; ++rt) {
                const int l = 64 * (16 * I + i) + 16 * rt + 4 * gq;
                const v2u uu = *(const v2u*)(ch + 2 * SS_K + (l + 48) * 2);
                const f32x4 y = acc[I][rt];
                const float b0 = rbf(y.x) + rbf(bf2f(uu.x & 0xffffu) * dsk), b1 = rbf(y.y) + rbf(bfhi(uu.x) * dsk), b2 = rbf(y.z) + rbf(bf2f(uu.y & 0xffffu) * dsk), b3 = rbf(y.w) + rbf(bfhi(uu.y) * dsk);
                *(v2u*)(ch + (16 * I + i) * 136 + (16 * rt + 4 * gq) * 2) = (v2u){pk2(b0, b1), pk2(b2, b3)};
            }
    }
    BAR();
#pragma unroll
    for (int k = 0; k < 2; ++k) {
        const int l = 2 * (tid + 512 * k);
        u32 e[8];
#pragma unroll
        for (int c = 0; c < 8; ++c) e[c] = *(const u32*)(lds + c * SS_CH + (l >> 6) * 136 + (l & 63) * 2);
        v4u lo, hi;
#pragma unroll
        for (int c = 0; c < 4; ++c) { lo[c] = __builtin_amdgcn_perm(e[2 * c + 1], e[2 * c], 0x05040100u); hi[c] = __builtin_amdgcn_perm(e[2 * c + 1], e[2 * c], 0x07060302u); }
        const int so = rfl(amap(BS_L + task * 32768 + k * 16384 + wave * 2048, far));
        st128(lo, cx.ar, lane * 32, so); st128(hi, cx.ar, lane * 32 + 16, so);
    }
    VMCNT(0);
    BAR();
    if (tid == 0) __hip_atomic_fetch_add(cx.ctl + CW_SSM + 64 * (task >> 4), 1u, RLX_AGENT);
}

constexpr int L_OP = 0, L_OA = 81920, L_OG = L_OA + 32 * 272;
DEV void out_task(const Params& p, const Ctx& cx, unsigned char* lds, int s, int cgp, int wave, int& ready) {
    const int lane = lane_id(), tid = wave * 64 + lane, far = cx.par ^ 1;
    const int n = lane & 15, gq = lane >> 4, gl = lane * 16;
    SIDX_DECL;
    const int ct = 8 * cgp + wave;
    const int zxl = (n >> 3) * 16384 + (16 * gq + (n & 7)) * 16, zml = (n >> 3) * 4096 + gq * 128 + (n & 7) * 16;
    if (wave == 0 && s >= ready) ready = wait_upto<1>(prog_load<1>(0, lane, cx.ctl), s, 0, lane, cx.ctl);
    BAR();
    v8u wr[20];
#pragma unroll
    for (int k = 0; k < 16; ++k) {
        const int so = ZX_P + s * ZX_STEP + ct * 32768 + (k >> 1) * 2048;
        wr[k] = cat8(ld128(cx.ar, zxl + (k & 1) * 128, so), ld128(cx.ar, zxl + (k & 1) * 128 + 1024, so));
    }
    const int otok = tid >> 4, oc = tid & 15;
    const v4u bv = ld128(cx.ar, amap(BS_L + (16 * cgp + oc) * 32768 + (s * 32 + otok) * 16, far), 0);
    {
        const rsrc_t rxin = mkrs(p.x, S * DM * 2), rg = mkrs(p.gw, DM * 2);
        const int tok = wave * 4 + gq;
        float sum = 0.f;
#pragma unroll
        for (int k = 0; k < 5; ++k) {
            const v4u xv = __builtin_amdgcn_raw_buffer_load_b128(rxin, ((s * 32 + tok) * DM + k * 128 + n * 8) * 2, 0, 0);
            const v4u gv = __builtin_amdgcn_raw_buffer_load_b128(rg, (k * 128 + n * 8) * 2, 0, 0);
#pragma unroll
            for (int e = 0; e < 4; ++e) sum += rbf(bf2f(xv[e] & 0xffffu) * bf2f(gv[e] & 0xffffu)) + rbf(bfhi(xv[e]) * bfhi(gv[e]));
        }
        sum = rbf(rowsum16(sum));
        const float g = rbf(1.f / (1.f + __expf(-sum)));
        if (n == 0) *(float*)(lds + L_OG + tok * 4) = g;
    }
    if (wave == 0) {
        if (s < NSEG - 2) wait_ge(cx.ctl + CW_S + 16 * s, 1u, cx.ctl);
        else {
            u32 spins = 0;
            for (;;) {
                const u32 a = __hip_atomic_load(cx.ctl + CW_FIN + 16 * lane, RLX_AGENT), c = __hip_atomic_load(cx.ctl + CW_FIN + 16 * (lane < 48 ? lane + 64 : lane), RLX_AGENT);
                if (__all(a && c)) break;
                __builtin_amdgcn_s_sleep(2);
                if (spin_fail(spins, cx.ctl)) break;
            }
        }
    }
    BAR();
    {
        v4u t[10];
#pragma unroll
        for (int k = 0; k < 10; ++k) t[k] = ld128(cx.ar, gl, rfl(amap(PT_L + s * PT_STEP + (k < 8 ? wave * 8 + k : 48 + wave * 2 + 8 + k) * 1024, far)));
        const int so = rfl(amap(ZM_L + s * ZM_STEP + ct * 8192, cx.par));
#pragma unroll
        for (int k = 0; k < 4; ++k) wr[16 + k] = cat8(ld128(cx.ar, zml + k * 1024, so), ld128(cx.ar, zml + k * 1024 + 512, so));
#pragma unroll
        for (int k = 0; k < 8; ++k) *(v4u*)(lds + L_OP + (wave * 8 + k) * 1024 + gl) = t[k];
#pragma unroll
        for (int k = 8; k < 10; ++k) {
            unsigned char* d = lds + L_OP + (56 + wave * 2 + k) * 1024 + gq * 256 + (lane & 7) * 32 + ((lane >> 3) & 1) * 8;
            *(v2u*)d = (v2u){t[k].x, t[k].y}; *(v2u*)(d + 16) = (v2u){t[k].z, t[k].w};
        }
    }
    BAR();
    f32x4 acc[4];
#pragma unroll
    for (int g = 0; g < 4; ++g) acc[g] = (f32x4){0, 0, 0, 0};
    asm volatile("s_nop 1" : "+v"(acc[0]), "+v"(acc[1]), "+v"(acc[2]), "+v"(acc[3]));
#pragma unroll
    for (int ks = 0; ks < 20; ++ks)
#pragma unroll
        for (int g = 0; g < 4; ++g) {
            const v4u a = *(const v4u*)(lds + L_OP + (ks < 16 ? g * 16 + ks : 64 + g * 4 + ks - 16) * 1024 + gl);
            SMF_W(acc[g], a, ks);
        }
    asm volatile("s_nop 7" : "+v"(acc[0]), "+v"(acc[1]), "+v"(acc[2]), "+v"(acc[3]));
#pragma unroll
    for (int g = 0; g < 4; ++g) {
        const u32 v = pk2(acc[g].x + acc[g].y, acc[g].z + acc[g].w);
        unsigned char* o = lds + L_OA + (8 * g + 2 * gq) * 272 + (wave * 16 + n) * 2;
        *(u16*)o = (u16)v; *(u16*)(o + 272) = (u16)(v >> 16);
    }
    BAR();
    {
        const v4u av = *(const v4u*)(lds + L_OA + otok * 272 + oc * 16);
        const float g = *(const float*)(lds + L_OG + otok * 4), og = rbf(1.f - g);
        v4u o;
#pragma unroll
        for (int e = 0; e < 4; ++e)
            o[e] = pk2(rbf(g * bf2f(av[e] & 0xffffu)) + rbf(og * bf2f(bv[e] & 0xffffu)), rbf(g * bfhi(av[e])) + rbf(og * bfhi(bv[e])));
        *(v4u*)(p.out + (s * 32 + otok) * DM + cgp * 128 + oc * 8) = o;
    }
}

DEV unsigned char* arena_base(unsigned char* ws) { return (unsigned char*)(((size_t)ws + CTRL_BYTES + (2u << 20) - 1) & ~(size_t)((2u << 20) - 1)); }

constexpr int LI_V = 0, LI_O = 40960, LI_TOTAL = LI_O + 3 * 32768;
__global__ void __launch_bounds__(576) gm_init(const u16* Wv, const u16* Wo, unsigned char* ws) {
    extern __shared__ __attribute__((aligned(16))) unsigned char lds[];
    const int b = blockIdx.x, wave = rfl(threadIdx.x >> 6), lane = threadIdx.x & 63;
    const rsrc_t ar = mkrs(arena_base(ws), ARENA_BYTES);
    if (wave == 8) {
        if (b == 0) {
            u32 mn[2] = {100000u, 100000u};
            for (int it = 0; it < 4; ++it)
#pragma unroll
                for (int q = 0; q < 2; ++q) {
                    const u32 c0 = now32();
                    st32(0u, ar, lane * 4, (4 << 20) + it * 256 + q * 8192);
                    VMCNT(0);
                    mn[q] = min(mn[q], now32() - c0);
                }
            if (lane < 32) ((u32*)ws)[CW_PAR + lane] = lane == 1 ? mn[0] : lane == 2 ? mn[1] : (u32)(mn[1] < mn[0]);
        } else if (b == 1) {
            for (int i = lane; i < CTRL_BYTES / 16; i += 64) if (i < CW_PAR / 4 || i >= CW_PAR / 4 + 8) ((v4u*)ws)[i] = (v4u){0, 0, 0, 0};
        }
        const v4u sv = {SENT, SENT, SENT, SENT};
#pragma unroll
        for (int k = 0; k < 16; ++k) st128(sv, ar, lane * 16, b * 16384 + k * 1024);
        return;
    }
    const int h = 4 * (b & 7) + (b >> 6), g0 = 5 * ((b >> 4) & 3), ch = (b >> 3) & 1;
    const int i = lane & 15, gq = lane >> 4;
    const u32 lds0 = (u32)(size_t)(LAS unsigned char*)lds;
    {
        const int row = wave * 4 + gq, f = (row & 13) | ((row >> 4) << 1);
        const rsrc_t rv = mkrs(Wv, DM * HD * 2);
#pragma unroll
        for (int g = 0; g < 5; ++g) dma16(rfl(lds0 + LI_V + g * 8192 + wave * 1024), row * (HD * 2) + ((i ^ f) * 16), rv, rfl(((g0 + g) * 32 * HD + h * DH) * 2));
    }
    {
        const rsrc_t ro = mkrs(Wo, DM * HD * 2);
        const int tp = lane >> 3, q4 = (lane >> 1) & 3, hf = lane & 1;
#pragma unroll
        for (int cg = 0; cg < 3; ++cg)
#pragma unroll
            for (int k = 0; k < 4; ++k)
                dma16(rfl(lds0 + LI_O + cg * 32768 + (wave * 4 + k) * 1024), q4 * (DM * 2) + (((tp ^ (k >> 1)) & (cg == 2 ? 3 : 7)) * 16 + hf * 8) * 2, ro,
                      rfl(((h * DH + wave * 16 + k * 4) * DM + ch * 320 + cg * 128) * 2));
    }
    const int rd0 = LI_O + (2 * gq) * 1024 + (i >> 2) * 32 + (i & 3) * 8 + (wave ^ (gq & 1)) * 128;
    const int dl = 16 * (i >> 3) + 8 * ((i >> 1) & 1) + 4 * ((i >> 2) & 1) + (i & 1);
    const int fl = (dl & 13) | ((dl >> 4) << 1);
#pragma unroll
    for (int cg = 0; cg < 3; ++cg) {
        if (cg == 0) VMCNT(8); else if (cg == 1) VMCNT(9); else VMCNT(10);
        BAR();
        if (cg == 2 && wave >= 4) break;
        v4u w[4];
#pragma unroll
        for (int ss = 0; ss < 4; ++ss) {
            const v2u t0 = __builtin_bit_cast(v2u, __builtin_amdgcn_ds_read_tr16_b64_v4i16((LAS s16x4*)(lds + rd0 + cg * 32768 + ss * 8192)));
            const v2u t1 = __builtin_bit_cast(v2u, __builtin_amdgcn_ds_read_tr16_b64_v4i16((LAS s16x4*)(lds + rd0 + cg * 32768 + ss * 8192 + 1024)));
            w[ss] = (v4u){t0.x, t0.y, t1.x, t1.y};
        }
#pragma unroll
        for (int g = 0; g < 5; ++g) {
            f32x4 d0 = {0, 0, 0, 0}, d1 = d0;
#pragma unroll
            for (int ss = 0; ss < 4; ++ss) {
                d0 = mfma16(*(const v4u*)(lds + LI_V + g * 8192 + dl * 256 + (((4 * ss + gq) ^ fl) * 16)), w[ss], d0);
                d1 = mfma16(*(const v4u*)(lds + LI_V + g * 8192 + (dl + 2) * 256 + (((4 * ss + gq) ^ fl) * 16)), w[ss], d1);
            }
            st128((v4u){pk2(d0.x, d0.y), pk2(d0.z, d0.w), pk2(d1.x, d1.y), pk2(d1.z, d1.w)}, ar, lane * 16, VO_P + ((h * 20 + g0 + g) * 40 + ch * 20 + cg * 8 + wave) * 1024);
        }
    }
}

__global__ void __launch_bounds__(512, 2) gm_kernel(Params p) {
    extern __shared__ __attribute__((aligned(16))) unsigned char lds[];
    const int b = blockIdx.x;
    const int wave = rfl(threadIdx.x >> 6);
    Ctx cx;
    cx.ctl = (GAS u32*)p.ws;
    cx.ar = mkrs(arena_base(p.ws), ARENA_BYTES);
    cx.par = rfl(cx.ctl[CW_PAR]) & 1;
    EVT(0);
    if ((b & 7) >= 4) {
        const int u = (b >> 3) * 4 + (b & 7) - 4, j = u >> 2;
        const int h = 8 * (u & 3) + (j >> 2), type = (j >> 1) & 1, second = j & 1;
        if (type) l_worker<1>(p, cx, lds, h, second, wave); else l_worker<0>(p, cx, lds, h, second, wave);
        EVT(3);
        if (!second) return;
        ssm_task(p, cx, lds, type * 32 + h, wave, cx.par ^ 1);
        EVT(4);
        const int nb = type ? 12 : 10, gi = h / nb;
        if (gi >= (type ? 2 : 3)) return;
        const int cgp = type ? 2 + gi : (gi == 2 ? 4 : gi);
        if (wave == 0) { wait_ge(cx.ctl + CW_SSM + 64 * cgp, 16u, cx.ctl); for (int k = 0; k < h % nb; ++k) __builtin_amdgcn_s_sleep(14); }
        int ready = 0;
#pragma unroll 1
        for (int s = h % nb; s < NSEG; s += nb) out_task(p, cx, lds, s, cgp, wave, ready);
        EVT(5);
        return;
    }
    const int w = (b >> 3) * 4 + (b & 7);
    if (w < 32) {
        if (wave < 2) a_role<0>(p, cx, lds, w, wave);
        else if (wave < 6) a_role<1>(p, cx, lds, w, wave);
        else a_role<2>(p, cx, lds, w, wave);
    } else if (w < 112) { if (wave == 1) r_role<true>(p, cx, lds, w - 32, wave); else r_role<false>(p, cx, lds, w - 32, wave); }
    else {
        ssm_task(p, cx, lds, w - 48, wave, cx.par ^ 1);
        if (w == 112 && wave == 0) {
            const int lane = lane_id();
            for (int s = 2; s < NSEG; ++s) {
                const int so = rfl(amap(s * SLOT + TS_O, cx.par));
                u32 spins = 0;
                for (;;) {
                    asm volatile("" ::: "memory");
                    if (!__any(ld32(cx.ar, (lane & 31) * 64, so) == SENT)) break;
                    __builtin_amdgcn_s_sleep(12);
                    if (spin_fail(spins, cx.ctl)) break;
                }
                if (lane == 0) __hip_atomic_store(cx.ctl + CW_S + 16 * (s - 2), 1u, RLX_AGENT);
                if (spins) __builtin_amdgcn_s_sleep(44);
            }
        }
    }
    EVT(5);
}

static const int g_dA = 1250, g_dB = 1160, g_dR = 1200, g_dZ = 5;
static const int g_dbg = 0;

extern "C" void kernel_launch(void* const* d_in, const int* in_sizes, int n_in,
                              void* d_out, int out_size, void* d_ws, size_t ws_size,
                              hipStream_t stream) {
    Params p;
    p.x = (const u16*)d_in[0]; p.mem0 = (const u16*)d_in[1]; p.Wq = (const u16*)d_in[2]; p.Wk = (const u16*)d_in[3]; p.Wv = (const u16*)d_in[4]; p.Wo = (const u16*)d_in[5];
    p.Are = (const u16*)d_in[8]; p.Aim = (const u16*)d_in[9]; p.Bre = (const u16*)d_in[10]; p.Bim = (const u16*)d_in[11]; p.Cre = (const u16*)d_in[12]; p.Cim = (const u16*)d_in[13];
    p.logdt = (const u16*)d_in[14]; p.dskip = (const u16*)d_in[15]; p.gw = (const u16*)d_in[16];
    p.out = (u16*)d_out; p.ws = (unsigned char*)d_ws;
    p.c3 = g_dA; p.g1 = g_dB; p.gmax = g_dR; p.maxrel = g_dZ; p.cp = g_dB - 90; p.cmL = g_dbg ? g_dbg : 300; p.glate = 2350; p.pad = 150;
    (void)hipFuncSetAttribute((const void*)gm_init, hipFuncAttributeMaxDynamicSharedMemorySize, LI_TOTAL);
    gm_init<<<256, 576, LI_TOTAL, stream>>>(p.Wv, p.Wo, (unsigned char*)d_ws);
    (void)hipFuncSetAttribute((const void*)gm_kernel, hipFuncAttributeMaxDynamicSharedMemorySize, L_TOTAL);
    gm_kernel<<<256, 512, L_TOTAL, stream>>>(p);
}
```

```cpp
#include <hip/hip_runtime.h>
#include <hip/hip_bf16.h>
#include <stdint.h>

typedef unsigned short u16;
typedef unsigned int u32;
typedef u32 v4u __attribute__((ext_vector_type(4)));
typedef u32 v2u __attribute__((ext_vector_type(2)));
typedef u32 v8u __attribute__((ext_vector_type(8)));
typedef float f32x4 __attribute__((ext_vector_type(4)));
typedef __bf16 bf16x8 __attribute__((ext_vector_type(8)));
typedef __bf16 bf16x2 __attribute__((ext_vector_type(2)));
typedef short s16x4 __attribute__((ext_vector_type(4)));
typedef __amdgpu_buffer_rsrc_t rsrc_t;
#define GAS __attribute__((address_space(1)))
#define LAS __attribute__((address_space(3)))
#define RLX_AGENT __ATOMIC_RELAXED, __HIP_MEMORY_SCOPE_AGENT
#define DEV __device__ __forceinline__

constexpr int S = 2048, DM = 640, H = 32, DH = 128, NSEG = 64, HD = H * DH, NS = 128;
constexpr int SC1 = 16;
constexpr u32 SENT = 0xFFFFFFFFu;

DEV float bf2f(u32 b) { return __uint_as_float(b << 16); }
DEV float bfhi(u32 b) { return __uint_as_float(b & 0xFFFF0000u); }
DEV float rbf(float f) { return (float)(__bf16)f; }
DEV u32 pk2(float lo, float hi) { bf16x2 v = {(__bf16)lo, (__bf16)hi}; return __builtin_bit_cast(u32, v); }
DEV u32 pk2s(float lo, float hi) { return min(pk2(lo, hi), 0xFFFFFFFEu); }
DEV int rfl(int v) { return __builtin_amdgcn_readfirstlane(v); }
DEV int lane_id() { int l; asm volatile("v_mbcnt_lo_u32_b32 %0, -1, 0\n\tv_mbcnt_hi_u32_b32 %0, -1, %0" : "=v"(l)); return l; }

constexpr int CTRL_BYTES = 21504;
constexpr int ARENA_BYTES = 208 << 20;
constexpr int SLOT = 32768, MEM_O = 0, PX_O = 10240, PM_O = PX_O + 16384, TS_O = PM_O + 4096;
constexpr int QX_L = 3 << 20, KX_L = 19 << 20, ZM_L = 35 << 20, ZM_STEP = 40 * 8192;
constexpr int PT_L = 56 << 20, PT_STEP = 81920, BS_L = 61 << 20;
constexpr int ZX_P = 128 << 20, ZX_STEP = 40 * 32768;
constexpr int CW_TMO = 0, CW_PAR = 64, CW_SSM = 128  , CW_OQ = 448, CW_S = 512, CW_FIN = 1536, CW_P = 3328;

DEV int amap(int o, int sel) { return ((o >> 13) << 14) | (o & 8191) | ((((o >> 20) ^ sel) & 1) << 13); }
DEV v8u cat8(v4u a, v4u b) { return __builtin_shufflevector(a, b, 0, 1, 2, 3, 4, 5, 6, 7); }
DEV rsrc_t mkrs(const void* p, int bytes) { return __builtin_amdgcn_make_buffer_rsrc((void*)p, 0, bytes, 0x00020000); }
DEV v4u ld128(rsrc_t r, int off, int so) { return __builtin_amdgcn_raw_buffer_load_b128(r, off, so, SC1); }
DEV u32 ld32(rsrc_t r, int off, int so) { return __builtin_amdgcn_raw_buffer_load_b32(r, off, so, SC1); }
DEV void st128(v4u v, rsrc_t r, int off, int so) { __builtin_amdgcn_raw_buffer_store_b128(v, r, off, so, SC1); }
DEV void st32(u32 v, rsrc_t r, int off, int so) { __builtin_amdgcn_raw_buffer_store_b32(v, r, off, so, SC1); }
#define VMCNT(n) asm volatile("s_waitcnt vmcnt(%0)" :: "i"(n) : "memory")
#define BAR() do { asm volatile("s_waitcnt lgkmcnt(0)" ::: "memory"); __builtin_amdgcn_s_barrier(); asm volatile("" ::: "memory"); } while (0)
template <int CTRL> DEV float dppf(float v) { return __builtin_bit_cast(float, __builtin_amdgcn_update_dpp(0, __builtin_bit_cast(int, v), CTRL, 0xf, 0xf, true)); }
DEV float rowsum16(float v) { v += dppf<0x128>(v); v += dppf<0x124>(v); v += dppf<0x122>(v); v += dppf<0x121>(v); return v; }
DEV void dma16(u32 ldsaddr, int voff, rsrc_t rs, int soff) {
    u32 keep;
    asm volatile("s_nop 4\n\ts_mov_b32 %0, m0\n\ts_mov_b32 m0, %1\n\ts_nop 0\n\tbuffer_load_dwordx4 %2, %3, %4 offen lds\n\ts_mov_b32 m0, %0"
                 : "=&s"(keep) : "s"(ldsaddr), "v"(voff), "s"(rs), "s"(soff) : "memory");
}

constexpr u32 SPINCAP = 1u << 20;

struct Params {
    const u16 *x, *mem0, *Wq, *Wk, *Wv, *Wo, *Are, *Aim, *Bre, *Bim, *Cre, *Cim, *logdt, *dskip, *gw;
    u16* out;
    unsigned char* ws;
    int c3, c2, gmax, cmL;
};
struct Ctx { GAS u32* ctl; rsrc_t ar; int par; };

constexpr int L_MEMF = 0, L_QF = 10240, L_KMF = 12288, L_KXF = 16384  , L_STAGE = 24576, L_DUMP = 32768, L_TOTAL = L_STAGE + 131072;

DEV bool spin_fail(u32& spins, GAS u32* ctl) {
    if (++spins > SPINCAP) { __hip_atomic_store(ctl + CW_TMO, 1u, RLX_AGENT); return true; }
    return (spins & 1023u) == 0 && rfl(__hip_atomic_load(ctl + CW_TMO, RLX_AGENT)) != 0;
}
DEV u32 wait_ge(GAS u32* w, u32 want, GAS u32* ctl) {
    u32 spins = 0, v;
    for (;;) { v = rfl(__hip_atomic_load(w, RLX_AGENT)); if (v >= want) break; __builtin_amdgcn_s_sleep(8); if (spin_fail(spins, ctl)) break; }
    asm volatile("" ::: "memory");
    return v;
}
template <int MODE> DEV int prog_load(int h, int lane, GAS u32* ctl) { return (int)__hip_atomic_load(ctl + CW_P + 16 * (MODE == 0 ? 2 * h + (lane & 1) : 64 + lane), RLX_AGENT); }
template <int MODE>
DEV int wait_upto(int v, int need, int h, int lane, GAS u32* ctl) {
    u32 spins = 0; int r;
    for (;;) {
        v = max(v, lane & 1);
        v = min(v, __builtin_amdgcn_update_dpp(64, v, 0x128, 0xf, 0xf, false)); v = min(v, __builtin_amdgcn_update_dpp(64, v, 0x124, 0xf, 0xf, false));
        v = min(v, __builtin_amdgcn_update_dpp(64, v, 0x122, 0xf, 0xf, false)); v = min(v, __builtin_amdgcn_update_dpp(64, v, 0x121, 0xf, 0xf, false));
        r = min(min(__builtin_amdgcn_readlane(v, 0), __builtin_amdgcn_readlane(v, 16)), min(__builtin_amdgcn_readlane(v, 32), __builtin_amdgcn_readlane(v, 48)));
        if (r > need) break;
        __builtin_amdgcn_s_sleep(8);
        if (spin_fail(spins, ctl)) { r = 64; break; }
        v = prog_load<MODE>(h, lane, ctl);
    }
    asm volatile("" ::: "memory");
    return r;
}
DEV u32 now32() { unsigned long long t; asm volatile("s_memtime %0\n s_waitcnt lgkmcnt(0)" : "=s"(t) :: "memory"); return (u32)t; }
DEV u32 gnow() { unsigned long long t; asm volatile("s_memrealtime %0\n s_waitcnt lgkmcnt(0)" : "=s"(t) :: "memory"); return (u32)t * 24u + 48u; }
DEV u32 wait_global(u32 T, int* slack = nullptr) {
    u32 off, e, d, d0;
    asm volatile(
        "s_memrealtime vcc\n s_memtime s[92:93]\n s_waitcnt lgkmcnt(0)\n"
        "s_mul_i32 %0, vcc_lo, 24\n s_sub_u32 %3, %4, %0\n s_sub_u32 %0, %0, s92\n s_cmp_gt_i32 %3, 150\n s_cbranch_scc0 4f\n"
        "1:\n"
        "s_memrealtime vcc\n s_memtime s[92:93]\n s_waitcnt lgkmcnt(0)\n"
        "s_mul_i32 %1, vcc_lo, 24\n s_sub_u32 %1, %1, s92\n"
        "s_sub_u32 %2, %1, %0\n s_cmp_gt_i32 %2, 0\n s_cselect_b32 %0, %1, %0\n"
        "s_sub_u32 %2, %4, s92\n s_sub_u32 %2, %2, %0\n"
        "s_cmp_gt_i32 %2, 700\n s_cbranch_scc0 2f\n s_cmp_gt_i32 %2, 0x40000\n s_cbranch_scc1 4f\n s_sleep 2\n s_branch 1b\n"
        "2:\n s_cmp_gt_i32 %2, 230\n s_cbranch_scc1 1b\n"
        "3:\n s_sub_u32 %2, %2, 32\n s_cmp_gt_i32 %2, 120\n s_cbranch_scc1 3b\n"
        "4:\n"
        : "=&s"(off), "=&s"(e), "=&s"(d), "=&s"(d0) : "s"(T) : "memory", "scc", "vcc", "s92", "s93");
    if (slack) *slack = (int)d0;
    return off;
}
#define RAWBAR() do { __builtin_amdgcn_s_barrier(); asm volatile("" ::: "memory"); } while (0)
DEV int rowmax32(int d) {
    d = max(d, __builtin_amdgcn_update_dpp(0, d, 0x128, 0xf, 0xf, true)); d = max(d, __builtin_amdgcn_update_dpp(0, d, 0x124, 0xf, 0xf, true));
    d = max(d, __builtin_amdgcn_update_dpp(0, d, 0x122, 0xf, 0xf, true)); d = max(d, __builtin_amdgcn_update_dpp(0, d, 0x121, 0xf, 0xf, true));
    return max(__builtin_amdgcn_readlane(d, 0), __builtin_amdgcn_readlane(d, 16));
}
DEV u32 read_ref(u32& v, const Ctx& cx, int ad, int so, u32 near) {
    u32 spins = 0;
    while (__builtin_expect(__any(v == SENT), 0)) {
        if (spin_fail(spins, cx.ctl)) break;
        __builtin_amdgcn_s_sleep(2);
        asm volatile("" ::: "memory");
        v = ld32(cx.ar, ad, so);
    }
    return near + (u32)(rowmax32((int)((v & ~63u) - (near << 4)) >> 4) );
}

struct Pace { u32 p1; int c2, x2, x3, relax, fails, slow; bool sched; };
DEV void pace_next(Pace& q, u32 tref, int s, const Params& p) {
    const int g = (int)(tref - q.p1);
    const bool late = s == 0 || g > p.gmax;
    if (q.sched && s > 6) {
        q.fails = late ? q.fails + 2 : max(q.fails - 1, 0);
        if (q.fails >= 3 && q.relax < 5) { if (g > p.gmax + 650) q.x2 += 48; else q.x3 += 56; ++q.relax; q.fails = 0; }
    }
    const int per = q.c2 + p.c3 + q.x3 + 2 * q.slow;
    q.p1 = late ? tref + per - p.cmL : q.p1 + per;
    q.c2 = p.c2 + q.x2 + 7 * q.slow;
    q.sched = !late;
}

#define SMF_A(acc, a, w) asm volatile("v_smfmac_f32_16x16x64_bf16 %0, %1, %2, %3" : "+v"(acc) : "v"(a), "a"(w), "v"(sidx))
#define SMF_V(acc, a, w) asm volatile("v_smfmac_f32_16x16x64_bf16 %0, %1, %2, %3" : "+v"(acc) : "v"(a), "v"(w), "v"(sidx))
#define SMF_W(acc, a, i) do { if ((i) < 16) SMF_A(acc, a, wr[i]); else SMF_V(acc, a, wr[i]); } while (0)
#define MF_WA(acc, i, b) do { if ((i) < 32) asm volatile("v_mfma_f32_16x16x32_bf16 %0, %1, %2, %0" : "+v"(acc) : "a"(w4[i]), "v"(b)); \
                              else asm volatile("v_mfma_f32_16x16x32_bf16 %0, %1, %2, %0" : "+v"(acc) : "v"(w4[i]), "v"(b)); } while (0)
#define MF_WB(acc, a, i) do { if ((i) < 32) asm volatile("v_mfma_f32_16x16x32_bf16 %0, %1, %2, %0" : "+v"(acc) : "v"(a), "a"(w4[i])); \
                              else asm volatile("v_mfma_f32_16x16x32_bf16 %0, %1, %2, %0" : "+v"(acc) : "v"(a), "v"(w4[i])); } while (0)
#define LDS_RD128(dst, addr, off) asm volatile("ds_read_b128 %0, %1 offset:%2" : "=v"(dst) : "v"(addr), "i"(off) : "memory")
#define LGKM_WAIT(n, x) asm volatile("s_waitcnt lgkmcnt(%1)" : "+v"(x) : "i"(n) : "memory")
#define LGKM_WAIT2(n, x, y) asm volatile("s_waitcnt lgkmcnt(%2)" : "+v"(x), "+v"(y) : "i"(n) : "memory")
#define SIDX_DECL int sidx = (lane & 1) ? 0xEEEE : 0x4444; asm volatile("s_nop 1" : "+v"(sidx))
DEV float dmax16(float v) {
    asm volatile("s_nop 1\n v_max_f32_dpp %0, %0, %0 row_ror:8 row_mask:0xf bank_mask:0xf\n s_nop 1\n v_max_f32_dpp %0, %0, %0 row_ror:4 row_mask:0xf bank_mask:0xf\n"
                 "s_nop 1\n v_max_f32_dpp %0, %0, %0 row_ror:2 row_mask:0xf bank_mask:0xf\n s_nop 1\n v_max_f32_dpp %0, %0, %0 row_ror:1 row_mask:0xf bank_mask:0xf" : "+v"(v));
    return v;
}
DEV float max3f(float a, float b, float c) { float r; asm("v_max3_f32 %0, %1, %2, %3" : "=v"(r) : "v"(a), "v"(b), "v"(c)); return r; }
DEV f32x4 mfma16(v4u a, v4u b, f32x4 c) { return __builtin_amdgcn_mfma_f32_16x16x32_bf16(__builtin_bit_cast(bf16x8, a), __builtin_bit_cast(bf16x8, b), c, 0, 0, 0); }

struct NoHook { DEV void operator()() const {} };
template <int MODE, class Sink, class Hook = NoHook>
DEV void load_units(const Params& p, int h, unsigned char* lds, int wave, int lane, Sink sink, int ch = 0, Hook hook = Hook()) {
    constexpr int NU = MODE == 3 ? 16 : 20, NR = NU / 4;
    const u32 lds0 = (u32)(size_t)(LAS unsigned char*)lds;
    const int tp = lane >> 3, q4 = (lane >> 1) & 3, hf = lane & 1;
    auto issue = [&](int r) {
        const int u = r * 4 + (wave >> 1), hh = wave & 1;
        if (u >= NU) return;
        bool second; int base, stride, map;
        if (MODE <= 1) { second = u & 1; base = (64 * (u >> 1) * HD + h * DH) * 2; stride = HD * 2; map = MODE; }
        else if (u < 10) { second = false; base = (64 * u * HD + h * DH) * 2; stride = HD * 2; map = MODE == 2 ? 1 : 0; }
        else { const int uo = u - 10; second = true; base = ((h * DH + 64 * (uo & 1)) * DM + (uo >> 1) * 128 + ch * 320) * 2; stride = DM * 2; map = MODE == 2 ? 2 : 0; }
        const int tmask = (MODE == 3 && u >= 14) ? 3 : 7;
        const int gq = map == 0 ? 8 * (q4 >> 1) + (q4 & 1) : q4;
        const rsrc_t rs = mkrs(second ? (MODE <= 1 ? p.Wk : p.Wo) : (MODE <= 1 ? p.Wq : p.Wv), DM * HD * 2);
#pragma unroll
        for (int i = 0; i < 8; ++i) {
            const int gB = i >> 1, i4 = i & 1;
            const int f = 32 * hh + (map == 0 ? 16 * (gB >> 1) + 4 * (gB & 1) + 2 * i4 : map == 1 ? 8 * gB + 4 * i4 : 16 * i4 + 4 * gB);
            const int voff = gq * stride + (((tp ^ (gB & 1)) & tmask) * 16 + hf * 8) * 2;
            const u32 la = rfl(lds0 + L_STAGE + (r & 1) * 65536 + (wave >> 1) * 16384 + (hh * 8 + i) * 1024);
            dma16(la, voff, rs, rfl(base + f * stride));
        }
    };
    const int gB = lane >> 4;
    const int rd0 = (2 * gB) * 1024 + ((lane & 15) >> 2) * 32 + (lane & 3) * 8;
    issue(0);
#pragma unroll
    for (int r = 0; r < NR; ++r) {
        if (r + 1 < NR) { issue(r + 1); VMCNT(8); } else if (MODE == 1 || MODE == 2) { hook(); VMCNT(5); } else VMCNT(0);
        BAR();
#pragma unroll
        for (int k = 0; k < 4; ++k) {
            int uu, tl;
            if (MODE == 1) { uu = (k >> 1) * 2 + (wave >> 2); tl = 2 * (wave & 3) + (k & 1); } else { uu = k; tl = wave; }
            if (MODE != 1 && r * 4 + k >= NU) continue;
            v2u t[4];
#pragma unroll
            for (int j = 0; j < 4; ++j) {
                s16x4 v = __builtin_amdgcn_ds_read_tr16_b64_v4i16((LAS s16x4*)(lds + L_STAGE + (r & 1) * 65536 + uu * 16384 + ((j >> 1) * 8 + (j & 1)) * 1024 + rd0 + (tl ^ (gB & 1)) * 128));
                t[j] = __builtin_bit_cast(v2u, v);
            }
            sink(r * 4 + k, t);
        }
        BAR();
    }
}

constexpr u32 POLLCAP = 1u << 17;
#define POLL_COUNT "s_add_u32 %[cnt], %[cnt], 1\n s_cmp_lt_u32 %[cnt], %[cap]\n s_cselect_b32 %[key], %[key], -1\n"
DEV void gather_mem4(const Ctx& cx, int gl, int s0, int s1, int s2, unsigned long long half, u32 ldst, u32 ldst2, u32& key) {
    u32 cnt = 0;
    asm volatile(
        "v_mov_b32 v112, 0\n v_mov_b32 v113, 0\n v_mov_b32 v114, 0\n v_mov_b32 v115, 0\n"
        "s_barrier\n"
        "1:\n"
        "buffer_load_dwordx4 v[116:119], %[gl], %[rs], %[s0] offen sc1\n"
        "buffer_load_dwordx4 v[120:123], %[gl], %[rs], %[s1] offen sc1\n"
        "s_mov_b64 exec, %[hf]\n"
        "buffer_load_dwordx4 v[112:115], %[gl], %[rs], %[s2] offen sc1\n"
        "s_mov_b64 exec, -1\n"
        POLL_COUNT
        "s_waitcnt vmcnt(2)\n"
        "ds_write_b128 %[ld], v[116:119]\n"
        "v_max3_u32 v124, v116, v117, v118\n"
        "s_waitcnt vmcnt(1)\n"
        "ds_write_b128 %[ld], v[120:123] offset:1024\n"
        "v_max3_u32 v124, v124, v119, v120\n"
        "v_max3_u32 v124, v124, v121, v122\n"
        "s_waitcnt vmcnt(0)\n"
        "ds_write2_b64 %[ld2], v[112:113], v[114:115] offset1:4\n"
        "v_max3_u32 v124, v124, v123, v112\n"
        "v_max3_u32 v124, v124, v113, v114\n"
        "v_max_u32 v124, v124, v115\n"
        "v_cmp_lt_u32 vcc, %[key], v124\n"
        "s_waitcnt lgkmcnt(0)\n"
        "s_cbranch_vccnz 1b\n"
        "s_barrier\n"
        : [cnt] "+s"(cnt), [key] "+s"(key)
        : [gl] "v"(gl), [rs] "s"(cx.ar), [s0] "s"(s0), [s1] "s"(s1), [s2] "s"(s2), [hf] "s"(half), [ld] "v"(ldst), [ld2] "v"(ldst2), [cap] "s"(POLLCAP)
        : "memory", "scc", "vcc", "v112", "v113", "v114", "v115", "v116", "v117", "v118", "v119", "v120", "v121", "v122", "v123", "v124");
}
DEV void load_mem0(const u16* mem0, unsigned char* lds, int tid) {
    for (int i = tid; i < 640; i += 512) { const int m = i / 80, c8 = i % 80; *(v4u*)(lds + L_MEMF + (c8 >> 1) * 256 + (2 * m + (c8 & 1)) * 16) = *(const v4u*)(mem0 + m * DM + c8 * 8); }
}

template <int CLS>
DEV void a_role(const Params& p, const Ctx& cx, unsigned char* lds, int h, int wave) {
    v8u wr[20];
    load_units<0>(p, h, lds, wave, lane_id(), [&](int slot, v2u (&t)[4]) { wr[slot] = (v8u){t[0].x, t[0].y, t[1].x, t[1].y, t[2].x, t[2].y, t[3].x, t[3].y}; });
    load_mem0(p.mem0, lds, wave * 64 + lane_id());
    for (int i = wave * 64 + lane_id(); i < 1024; i += 512) *(u32*)(lds + L_KMF + i * 4) = 0;
    const int par = cx.par, far = par ^ 1;
    const int lane = lane_id(), n = lane & 15, gq = lane >> 4;
    SIDX_DECL;
    const u32 lbase = (u32)(size_t)(LAS unsigned char*)lds + lane * 16;
    const int gl = lane * 16;
    const int qw = L_QF + (wave >> 2) * 1024 + (2 * ((wave >> 1) & 1) + (n >> 3)) * 256 + (2 * gq) * 32 + ((n >> 1) & 1) * 16 + (4 * ((n >> 2) & 1) + 2 * (wave & 1) + (n & 1)) * 2;
    const int kw = L_KMF + (wave >> 1) * 1024 + (16 * (n >> 2) + gq) * 16 + (4 * (wave & 1) + (n & 3)) * 2;
    const int pair = 8 * (n >> 3) + 2 * (n & 3) + ((n >> 2) & 1);
    const int tk0 = 2 * pair, tk1 = 2 * pair + 1;
    const u32 lq = (u32)(size_t)(LAS unsigned char*)lds + gq * 256 + (4 * (wave & 1) + (n >> 2)) * 32 + (n & 1) * 16;
    const int adpx = (n >> 3) * 256 + gq * 32 + (n & 7) * 4, upx = PX_O + h * 512 + (wave & 1) * 128;
    const int adpm = gq * 16 + (n & 3) * 4, upm = PM_O + h * 128 + (wave & 1) * 64;
    const int g8 = (wave - 2) & 3;
    const int t0 = 8 * g8 + gq;
    const int ql = gq * 256 + (((n >> 1) & 1) * 4 + (n >> 2)) * 32 + (n & 1) * 16;
    const int adtx = (n >> 3) * 256 + gq * 32 + (n & 7) * 4, utx = (g8 * 32 + h) * 512;
    const int adtm = gq * 16 + (n & 1) * 8 + ((n >> 1) & 1) * 4, utm = 65536 + (g8 * 32 + h) * 128;
    const float SC = 0.08838834764831845f * 1.4426950408889634f;
    const float NEGR = -1.0e9f / 0.08838834764831845f;
    const float pad0 = (n < 8) ? 0.f : -__builtin_inff();
    const bool pmlane = n < 4;
    const int adts = (lane & 31) * 64;
    int ready = 0;
    v4u qx0, qx1, qn0, qn1;
    f32x4 s1, s2;
    auto tok_load = [&](int sn) {
        const int qb = rfl(amap(QX_L + (sn * H + h) * 8192 + g8 * 2048, par));
        qn0 = ld128(cx.ar, ql, qb); qn1 = ld128(cx.ar, ql + 1024, qb);
    };
    auto tok_prep = [&](int sn) {
        qx0 = qn0; qx1 = qn1;
        v4u kx[8];
#pragma unroll
        for (int i = 0; i < 8; ++i) kx[i] = *(const v4u*)(lds + L_KXF + (sn & 1) * 8192 + i * 1024 + lane * 16);
        s1 = (f32x4){0, 0, 0, 0}; s2 = s1;
        asm volatile("s_nop 1" : "+v"(s1), "+v"(s2), "+v"(qx0), "+v"(qx1));
        SMF_V(s1, qx0, cat8(kx[0], kx[1]));
        SMF_V(s2, qx0, cat8(kx[4], kx[5]));
        SMF_V(s1, qx1, cat8(kx[2], kx[3]));
        SMF_V(s2, qx1, cat8(kx[6], kx[7]));
        asm volatile("s_nop 7" : "+v"(s1), "+v"(s2));
        s1.x = (tk0 <= t0) ? s1.x + s1.y : NEGR; s1.z = (tk0 <= t0 + 4) ? s1.z + s1.w : NEGR;
        s1.y = (tk1 <= t0) ? s2.x + s2.y : NEGR; s1.w = (tk1 <= t0 + 4) ? s2.z + s2.w : NEGR;
    };
    v4u kt[8], k1 = {0, 0, 0, 0};
    auto help_load = [&](int sn) {
        const int kb = rfl(amap(KX_L + (sn * H + h) * 8192, par));
#pragma unroll
        for (int i = 0; i < 8; ++i) kt[i] = ld128(cx.ar, gl + (i & 3) * 1024, kb + (i >> 2) * 4096);
    };
    auto help_store = [&](int sn) {
#pragma unroll
        for (int i = 0; i < 8; ++i) *(v4u*)(lds + L_KXF + (sn & 1) * 8192 + i * 1024 + lane * 16) = kt[i];
    };
    if (CLS == 2) {
        ready = wait_upto<0>(prog_load<0>(h, lane, cx.ctl), wave - 6, h, lane, cx.ctl);
        help_load(wave - 6); help_store(wave - 6);
    }
    BAR();
    if (CLS == 1) { tok_load(0); tok_prep(0); }
    Pace pc = {gnow(), p.c2 + 7 * 12, 0, 0, 0, 0, 12, false};
    u32 off = gnow() - now32(), key = 0xFFFFFFFEu;
    u32 myslow = 12, offp = 0, tp = 0;
    float slow = 0.045f;
    bool okoff = false, okp = false;
    const int gw = wave < 2 ? wave : wave - 4;
    const u32 gdst = lbase + L_MEMF + gw * 2048;
    const u32 gdst2 = (u32)(size_t)(LAS unsigned char*)lds + (((lane >> 5) == (gw & 1)) ? L_MEMF + 8192 + (gw >> 1) * 1024 : L_DUMP) + gq * 256 + (n & 3) * 64 + (n >> 2) * 8;
    for (int s = 0; s < NSEG; ++s) {
        const int sb = s * SLOT;
        f32x4 aq = {0, 0, 0, 0}, ak = {0, 0, 0, 0};
        asm volatile("" : "+v"(aq), "+v"(ak));
        const int xpx = rfl(amap(sb + upx, par)), xpm = rfl(amap(sb + upm, par)), xts = rfl(amap(sb + TS_O, par));
        const int xk1 = rfl(amap(KX_L + ((s == 0 ? 0 : min(s + 2, NSEG - 1)) * H + h) * 8192 + wave * 1024, par));
        const int xqn = rfl(amap(QX_L + (min(s + 1, NSEG - 1) * H + h) * 8192 + g8 * 2048, par));
        if (s > 0) {
            const int x0 = rfl(amap(sb + MEM_O + gw * 2048, par)), x2 = rfl(amap(sb + MEM_O + 8192 + (gw >> 1) * 1024, par));
            asm volatile("s_waitcnt lgkmcnt(0)" :: "s"(x0), "s"(x2), "s"(xpx), "s"(xpm), "s"(xts), "s"(xk1), "s"(xqn) : "memory");
            asm volatile("s_waitcnt lgkmcnt(0)" ::: "memory");
            if (CLS == 2 && wave == 7) { int slack; off = wait_global(pc.p1, &slack); okoff = slack > 500; }
            if (CLS != 1) gather_mem4(cx, gl, x0, x0 + 1024, x2, (gw & 1) ? 0xFFFFFFFF00000000ull : 0xFFFFFFFFull, gdst, gdst2, key);
            else { RAWBAR(); RAWBAR(); }
        } else BAR();
        if (CLS == 2 && wave == 7) { const u32 ts = (((now32() + off) << 4) & ~63u) | myslow; if (lane == 0) st32(ts, cx.ar, h * 64, xts); }
        {
            v4u a_[5];
#pragma unroll
            for (int ks = 0; ks < 5; ++ks) LDS_RD128(a_[ks], lbase, L_MEMF + ks * 1024);
            if (CLS == 1) { qn0 = ld128(cx.ar, ql, xqn); qn1 = ld128(cx.ar, ql + 1024, xqn); }
            k1 = ld128(cx.ar, gl, xk1);
#pragma unroll
            for (int ks = 0; ks < 5; ++ks) { LGKM_WAIT(4, a_[ks]); SMF_W(aq, a_[ks], 2 * ks); SMF_W(ak, a_[ks], 2 * ks + 1); LDS_RD128(a_[ks], lbase, L_MEMF + (ks + 5) * 1024); }
#pragma unroll
            for (int ks = 5; ks < 10; ++ks) { LGKM_WAIT(9 - ks, a_[ks - 5]); SMF_W(aq, a_[ks - 5], 2 * ks); SMF_W(ak, a_[ks - 5], 2 * ks + 1); }
        }
        v4u kx[8];
        if (CLS == 0) {
            const u32 lk = lbase + (s & 1) * 8192;
#pragma unroll
            for (int i = 0; i < 8; ++i) LDS_RD128(kx[i], lk, L_KXF + i * 1024);
        } else asm volatile("s_nop 7");
        asm volatile("" : "+v"(aq), "+v"(ak));
        {
            const u32 q2 = pk2(aq.x + aq.y, aq.z + aq.w), k2 = pk2(ak.x + ak.y, ak.z + ak.w);
            *(u16*)(lds + qw) = (u16)q2; *(u16*)(lds + qw + 32) = (u16)(q2 >> 16);
            *(u16*)(lds + kw) = (u16)k2; *(u16*)(lds + kw + 64) = (u16)(k2 >> 16);
        }
        f32x4 s0 = {pad0, 0, pad0, 0};
        u32 tv7 = 0;
        if (CLS == 0) { s1 = (f32x4){0, 0, 0, 0}; s2 = s1; asm volatile("" : "+v"(s0), "+v"(s1), "+v"(s2) : "s"(xpx), "s"(xpm)); }
        if (CLS == 1) asm volatile("" : "+v"(s0));
        BAR();
        if (CLS == 0) {
            v4u a0, a1, km[4];
            LDS_RD128(a0, lq, L_QF); LDS_RD128(a1, lq, L_QF + 1024);
#pragma unroll
            for (int i = 0; i < 4; ++i) LDS_RD128(km[i], lbase, L_KMF + i * 1024);
            LGKM_WAIT(5, a0);
            SMF_V(s1, a0, cat8(kx[0], kx[1]));
            SMF_V(s2, a0, cat8(kx[4], kx[5]));
            LGKM_WAIT(4, a1);
            SMF_V(s1, a1, cat8(kx[2], kx[3]));
            SMF_V(s2, a1, cat8(kx[6], kx[7]));
            LGKM_WAIT2(2, km[0], km[1]);
            SMF_V(s0, a0, cat8(km[0], km[1]));
            LGKM_WAIT2(0, km[2], km[3]);
            SMF_V(s0, a1, cat8(km[2], km[3]));
            asm volatile("s_nop 4" : "+v"(s1), "+v"(s2));
            float x1 = s1.x + s1.y, x2 = s2.x + s2.y;
            asm volatile("s_nop 0" : "+v"(s0), "+v"(x1), "+v"(x2));
            const float x0 = s0.x + s0.y;
            const float mx = dmax16(max3f(x0, x1, x2));
            const float e0 = __builtin_amdgcn_exp2f((x0 - mx) * SC), e1 = __builtin_amdgcn_exp2f((x1 - mx) * SC), e2 = __builtin_amdgcn_exp2f((x2 - mx) * SC);
            const float inv = __builtin_amdgcn_rcpf(rowsum16(e0 + e1 + e2));
            const float p0 = e0 * inv;
            st32(pk2s(e1 * inv, e2 * inv), cx.ar, adpx, xpx);
            const u32 pm = pk2s(p0, dppf<0x104>(p0));
            if (pmlane) st32(pm, cx.ar, adpm, xpm);
        } else if (CLS == 1) {
            __builtin_amdgcn_s_sleep(5);
            if (s > 0) *(v4u*)(lds + L_KXF + (s & 1) * 8192 + wave * 1024 + lane * 16) = k1;
            asm volatile("" : "+v"(qn0), "+v"(qn1) :: "memory");
            v4u km[4];
#pragma unroll
            for (int i = 0; i < 4; ++i) km[i] = *(const v4u*)(lds + L_KMF + i * 1024 + lane * 16);
            SMF_V(s0, qx0, cat8(km[0], km[1]));
            SMF_V(s0, qx1, cat8(km[2], km[3]));
            asm volatile("s_nop 7" : "+v"(s0));
            const int xtx = rfl(amap(PT_L + s * PT_STEP + utx, far)), xtm = rfl(amap(PT_L + s * PT_STEP + utm, far));
#pragma unroll
            for (int rr = 0; rr < 2; ++rr) {
                const float x0 = rr ? s0.z + s0.w : s0.x + s0.y, x1 = rr ? s1.z : s1.x, x2 = rr ? s1.w : s1.y;
                const float mx = dmax16(max3f(x0, x1, x2));
                const float e0 = __builtin_amdgcn_exp2f((x0 - mx) * SC), e1 = __builtin_amdgcn_exp2f((x1 - mx) * SC), e2 = __builtin_amdgcn_exp2f((x2 - mx) * SC);
                const float inv = __builtin_amdgcn_rcpf(rowsum16(e0 + e1 + e2));
                const float p0 = e0 * inv;
                st32(pk2(e1 * inv, e2 * inv), cx.ar, adtx + rr * 128, xtx);
                const u32 pm = pk2(p0, dppf<0x104>(p0));
                if (pmlane) st32(pm, cx.ar, adtm + rr * 64, xtm);
            }
            if (s + 1 < NSEG) tok_prep(s + 1);
        } else {
            if (wave == 6) {
                if (s == 0) { ready = wait_upto<0>(prog_load<0>(h, lane, cx.ctl), 2, h, lane, cx.ctl); help_load(2); help_store(2); }
                if (s + 3 >= ready && ready < NSEG) ready = wait_upto<0>(prog_load<0>(h, lane, cx.ctl), min(s + 3, NSEG - 1), h, lane, cx.ctl);
            } else tv7 = ld32(cx.ar, adts, xts);
        }
        if (CLS != 1 && s > 0) *(v4u*)(lds + L_KXF + (s & 1) * 8192 + wave * 1024 + lane * 16) = k1;
        if (CLS == 2 && wave == 7) {
            if (okoff) {
                if (okp) slow = 0.75f * slow + 0.25f * (float)(int)(off - offp) / (float)(int)(pc.p1 - tp);
                offp = off; tp = pc.p1; okp = true;
                myslow = rfl(min(max((int)(slow * 256.f + 0.5f), 0), 62));
            }
            pace_next(pc, read_ref(tv7, cx, adts, xts, pc.p1), s, p);
            pc.slow = rowmax32((int)(tv7 & 63u));
        }
    }
    if (key == SENT) __hip_atomic_store(cx.ctl + CW_TMO, 1u, RLX_AGENT);
    VMCNT(0);
    BAR();
    if (CLS == 2 && wave == 7 && lane == 0) __hip_atomic_store(cx.ctl + CW_FIN + 16 * h, 1u, RLX_AGENT);
}

DEV void b_role(const Params& p, const Ctx& cx, unsigned char* lds, int beta, int wave) {
    const int h = 8 * (beta & 3) + (beta >> 3), ch = (beta >> 2) & 1, par = cx.par;
    v8u wr[16];
    load_units<3>(p, h, lds, wave, lane_id(), [&](int slot, v2u (&t)[4]) { wr[slot] = (v8u){t[0].x, t[0].y, t[1].x, t[1].y, t[2].x, t[2].y, t[3].x, t[3].y}; }, ch);
    load_mem0(p.mem0, lds, wave * 64 + lane_id());
    const int lane = lane_id(), n = lane & 15, gq = lane >> 4;
    SIDX_DECL;
    const u32 lbase = (u32)(size_t)(LAS unsigned char*)lds + lane * 16;
    const int gl = lane * 16, gw = wave & 3;
    const u32 gdst = lbase + L_MEMF + gw * 2048;
    const u32 gdst2 = (u32)(size_t)(LAS unsigned char*)lds + (((lane >> 5) == (gw & 1)) ? L_MEMF + 8192 + (gw >> 1) * 1024 : L_DUMP) + gq * 256 + (n & 3) * 64 + (n >> 2) * 8;
    const int vw = L_QF + wave * 256 + (4 * gq + (n >> 3)) * 16 + (n & 7) * 2;
    const int zsl = ZM_L + (20 * ch + 8 * gq + wave) * 8192 + h * 256 + n * 16;
    const bool zlane = gq < 2 || (gq == 2 && wave < 4);
    const int adts = (lane & 31) * 64;
    u32 key = 0xFFFFFFFEu, sink = 0;
    const int wr0 = 8 * (beta & 3) + (beta >> 3), wl0 = ((beta >> 2) & 1) * 160 + (wave & 1) * 80 + n;
    Pace pc = {gnow(), p.c2 + 7 * 12, 0, 0, 0, 0, 12, false};
    for (int s = 0; s < NSEG; ++s) {
        const int sb = s * SLOT;
        f32x4 av = {0, 0, 0, 0}, z0 = av, z1 = av, z2 = av;
        int zo = amap(zsl + s * ZM_STEP, par);
        asm volatile("" : "+v"(av), "+v"(zo), "+v"(z0), "+v"(z1), "+v"(z2));
        const int xts = rfl(amap(sb + TS_O, par));
        if (s > 0) {
            const int x0 = rfl(amap(sb + MEM_O + gw * 2048, par)), x2 = rfl(amap(sb + MEM_O + 8192 + (gw >> 1) * 1024, par));
            asm volatile("" :: "s"(x0), "s"(x2), "s"(xts));
            if (wave == 7) wait_global(pc.p1);
            if (wave < 4) gather_mem4(cx, gl, x0, x0 + 1024, x2, (gw & 1) ? 0xFFFFFFFF00000000ull : 0xFFFFFFFFull, gdst, gdst2, key);
            else { RAWBAR(); if (wave < 6) VMCNT(5 - 2); else VMCNT(0); RAWBAR(); }
        } else BAR();
        const bool warmer = wave == 4 || wave == 5;
        const int wso = ZX_P + min(s + 2, NSEG - 1) * ZX_STEP;
        auto warm = [&](int k) {
            const int L = wl0 + 16 * k;
            const int o = L < 256 ? wr0 * 32768 + L * 128 : (32 + (wr0 >> 2)) * 32768 + (wr0 & 3) * 8192 + (L - 256) * 128;
            asm volatile("buffer_load_dword %0, %1, %2, %3 offen sc1" : "+v"(sink) : "v"(o), "s"(cx.ar), "s"(wso) : "memory");
        };
        {
            v4u a_[10];
#pragma unroll
            for (int ks = 0; ks < 10; ++ks) LDS_RD128(a_[ks], lbase, L_MEMF + ks * 1024);
            if (warmer) {
#pragma unroll
                for (int k = 0; k < 2; ++k) warm(k);
            }
#pragma unroll
            for (int ks = 0; ks < 10; ++ks) { LGKM_WAIT(9 - ks, a_[ks]); SMF_A(av, a_[ks], wr[ks]); }
        }
        asm volatile("s_nop 7" : "+v"(av));
        const u32 v2 = pk2(av.x + av.y, av.z + av.w);
        *(u16*)(lds + vw) = (u16)v2; *(u16*)(lds + vw + 32) = (u16)(v2 >> 16);
        BAR();
        v4u a0, a1;
        LDS_RD128(a0, lbase, L_QF); LDS_RD128(a1, lbase, L_QF + 1024);
        LGKM_WAIT(1, a0);
        SMF_A(z0, a0, wr[10]); SMF_A(z1, a0, wr[12]);
        if (wave < 4) SMF_A(z2, a0, wr[14]);
        LGKM_WAIT(0, a1);
        SMF_A(z0, a1, wr[11]); SMF_A(z1, a1, wr[13]);
        if (wave < 4) SMF_A(z2, a1, wr[15]);
        asm volatile("s_nop 7" : "+v"(z0), "+v"(z1), "+v"(z2));
        const u32 r0 = pk2s(z0.x + z0.y, z0.z + z0.w), r1 = pk2s(z1.x + z1.y, z1.z + z1.w), r2 = pk2s(z2.x + z2.y, z2.z + z2.w);
        auto s02 = __builtin_amdgcn_permlane32_swap(r0, r2, false, false);
        auto s13 = __builtin_amdgcn_permlane32_swap(r1, r1, false, false);
        auto t01 = __builtin_amdgcn_permlane16_swap(s02[0], s13[0], false, false);
        auto t23 = __builtin_amdgcn_permlane16_swap(s02[1], s13[1], false, false);
        if (zlane) st128((v4u){t01[0], t01[1], t23[0], t23[1]}, cx.ar, zo, 0);
        u32 tv = 0;
        if (wave == 7) tv = ld32(cx.ar, adts, xts);
        if (wave == 7 && s + 1 < NSEG) {
            pace_next(pc, read_ref(tv, cx, adts, xts, pc.p1), s, p);
            pc.slow = rowmax32((int)(tv & 63u));
        } else if (!warmer) __builtin_amdgcn_s_sleep(18);
        if (s + 3 < NSEG && zlane) st128((v4u){SENT, SENT, SENT, SENT}, cx.ar, amap(zsl + (s + 2) * ZM_STEP, par), 0);
        if (warmer) {
#pragma unroll
            for (int k = 2; k < 5; ++k) { warm(k); __builtin_amdgcn_s_sleep(7); }
        }
    }
    if (key == SENT) __hip_atomic_store(cx.ctl + CW_TMO, 1u, RLX_AGENT);
    VMCNT(0);
    asm volatile("" :: "v"(sink));
    BAR();
    if (wave == 6 && lane == 0) __hip_atomic_store(cx.ctl + CW_FIN + 16 * (32 + beta), 1u, RLX_AGENT);
}

DEV void r_role(const Params& p, const Ctx& cx, unsigned char* lds, int rho, int wave) {
    const int r = 8 * (rho & 3) + (rho >> 2), par = cx.par;
    const int te = 32 + (r >> 2), qe = r & 3;
    const int lane = lane_id(), n = lane & 15, gq = lane >> 4;
    SIDX_DECL;
    const u32 lds0 = (u32)(size_t)(LAS unsigned char*)lds;
    const int gl = lane * 16;
    const int pml = gq * 128 + (n >> 1) * 16;
    const unsigned long long emask = 0x000F000F000F000Full;
    const bool elane = n < 4;
    const u32 redw = lds0 + ((2 * gq) * 8 + (n >> 1)) * 80 + wave * 8 + (n & 1) * 4;
    const u32 redx = lds0 + (elane ? 5120 : 8192) + ((2 * gq) * 2 + ((n >> 1) & 1)) * 80 + wave * 8 + (n & 1) * 4;
    const int adts = (lane & 31) * 64;
    u32 key = 0xFFFFFFFEu;
    int ready = 0;
    if (wave == 5) ready = wait_upto<1>(prog_load<1>(0, lane, cx.ctl), 0, 0, lane, cx.ctl);
    BAR();
    Pace pc = {gnow(), p.c2 + 7 * 12, 0, 0, 0, 0, 12, false};
    for (int s = 0; s + 1 < NSEG; ++s) {
        const int sb = s * SLOT;
        if (wave < 6) { __builtin_amdgcn_s_sleep(4); if (wave != 1) __builtin_amdgcn_s_sleep(2); }
        v8u zb0, zb1, ze0, ze1;
        {
            const int xz = ZX_P + s * ZX_STEP + r * 32768 + wave * 4096;
            const v4u e = ld128(cx.ar, gl, ZX_P + s * ZX_STEP + te * 32768 + qe * 8192 + wave * 1024);
            zb0 = cat8(ld128(cx.ar, gl, xz), ld128(cx.ar, gl + 1024, xz)); zb1 = cat8(ld128(cx.ar, gl + 2048, xz), ld128(cx.ar, gl + 3072, xz));
            int pv = 64;
            if (wave == 5 && s + 1 >= ready) pv = prog_load<1>(0, lane, cx.ctl);
#pragma unroll
            for (int i = 0; i < 4; ++i) {
                ze0[i] = e[i]; ze0[4 + i] = __builtin_amdgcn_update_dpp(0, e[i], 0x104, 0xf, 0xf, true);
                ze1[i] = __builtin_amdgcn_update_dpp(0, e[i], 0x108, 0xf, 0xf, true); ze1[4 + i] = __builtin_amdgcn_update_dpp(0, e[i], 0x10C, 0xf, 0xf, true);
            }
            if (wave == 5 && s + 1 >= ready) ready = wait_upto<1>(pv, s + 1, 0, lane, cx.ctl);
        }
        const int xts = rfl(amap(sb + TS_O, par));
        const int xpub = rfl(amap(sb + SLOT + MEM_O + (wave == 7 ? r * 256 : te * 256 + qe * 64), par));
        const int xx0 = rfl(amap(sb + PX_O + wave * 2048, par)), xx1 = rfl(amap(sb + PX_O + wave * 2048 + 1024, par)), xm = rfl(amap(sb + PM_O + wave * 512, par));
        const int xzm = rfl(amap(ZM_L + s * ZM_STEP + r * 8192 + wave * 1024, par)), xze = rfl(amap(ZM_L + s * ZM_STEP + te * 8192 + wave * 1024 + qe * 64, par));
        asm volatile("" :: "s"(xx0), "s"(xx1), "s"(xm), "s"(xts), "s"(xpub), "s"(xzm), "s"(xze));
        u32 tv = 0, tref = 0;
        if (wave == 1) {
            if (s > 0) wait_global(pc.p1 + 1500);
            tv = ld32(cx.ar, adts, xts);
            u32 t2 = pc.p1 + pc.c2;
            if (s == 0 || __any(tv == SENT || (int)((tv & ~63u) - (pc.p1 << 4)) > 16 * p.gmax)) t2 = read_ref(tv, cx, adts, xts, pc.p1) + pc.c2 - 800;
            wait_global(t2 - 240);
        }
        asm volatile("" : "+v"(zb0), "+v"(zb1), "+v"(ze0), "+v"(ze1));
        {
            u32 cnt = 0;
            asm volatile(
                "v_mov_b32 v116, 0\n v_mov_b32 v117, 0\n v_mov_b32 v118, 0\n v_mov_b32 v119, 0\n"
                "v_mov_b32 v100, 0\n v_mov_b32 v101, 0\n v_mov_b32 v102, 0\n v_mov_b32 v103, 0\n"
                "s_barrier\n"
                "1:\n"
                "buffer_load_dwordx4 v[104:107], %[gl], %[rs], %[xz] offen sc1\n"
                "s_mov_b64 exec, %[em]\n"
                "buffer_load_dwordx4 v[100:103], %[gl], %[rs], %[xe] offen sc1\n"
                "s_mov_b64 exec, -1\n"
                "s_sleep " "3" "\n"
                "buffer_load_dwordx4 v[108:111], %[gl], %[rs], %[x0] offen sc1\n"
                "buffer_load_dwordx4 v[112:115], %[gl], %[rs], %[x1] offen sc1\n"
                "s_mov_b64 exec, %[ev]\n"
                "buffer_load_dwordx4 v[116:119], %[pml], %[rs], %[xm] offen sc1\n"
                "s_mov_b64 exec, -1\n"
                POLL_COUNT
                "v_mov_b32 v120, 0\n v_mov_b32 v121, 0\n v_mov_b32 v122, 0\n v_mov_b32 v123, 0\n"
                "v_mov_b32 v96, 0\n v_mov_b32 v97, 0\n v_mov_b32 v98, 0\n v_mov_b32 v99, 0\n"
                "s_waitcnt vmcnt(3)\n"
                "v_max3_u32 v124, v104, v105, v106\n"
                "v_max3_u32 v124, v124, v107, v100\n"
                "v_max3_u32 v124, v124, v101, v102\n"
                "s_waitcnt vmcnt(2)\n"
                "v_smfmac_f32_16x16x64_bf16 v[120:123], v[108:111], %[zb0], %[si]\n"
                "v_smfmac_f32_16x16x64_bf16 v[96:99], v[108:111], %[ze0], %[si]\n"
                "v_max3_u32 v124, v124, v103, v108\n"
                "v_max3_u32 v124, v124, v109, v110\n"
                "s_waitcnt vmcnt(1)\n"
                "v_smfmac_f32_16x16x64_bf16 v[120:123], v[112:115], %[zb1], %[si]\n"
                "v_smfmac_f32_16x16x64_bf16 v[96:99], v[112:115], %[ze1], %[si]\n"
                "v_max3_u32 v124, v124, v111, v112\n"
                "v_max3_u32 v124, v124, v113, v114\n"
                "s_waitcnt vmcnt(0)\n"
                "v_mfma_f32_16x16x32_bf16 v[120:123], v[116:119], v[104:107], v[120:123]\n"
                "v_mfma_f32_16x16x32_bf16 v[96:99], v[116:119], v[100:103], v[96:99]\n"
                "v_max3_u32 v124, v124, v115, v116\n"
                "v_max3_u32 v124, v124, v117, v118\n"
                "v_max_u32 v124, v124, v119\n"
                "v_cmp_lt_u32 vcc, %[key], v124\n"
                "s_nop 1\n"
                "s_cbranch_vccnz 1b\n"
                "v_add_f32 v124, v120, v121\n"
                "v_add_f32 v125, v122, v123\n"
                "ds_write2_b32 %[rw], v124, v125 offset1:160\n"
                "v_add_f32 v124, v96, v97\n"
                "v_add_f32 v125, v98, v99\n"
                "ds_write2_b32 %[rx], v124, v125 offset1:40\n"
                "s_waitcnt lgkmcnt(0)\n"
                "s_barrier\n"
                : [cnt] "+s"(cnt), [key] "+s"(key)
                : [gl] "v"(gl), [pml] "v"(pml), [rs] "s"(cx.ar), [x0] "s"(xx0), [x1] "s"(xx1), [xm] "s"(xm), [xz] "s"(xzm), [xe] "s"(xze), [ev] "s"(0x5555555555555555ull), [em] "s"(emask),
                  [cap] "s"(POLLCAP), [zb0] "v"(zb0), [zb1] "v"(zb1), [ze0] "v"(ze0), [ze1] "v"(ze1), [si] "v"(sidx), [rw] "v"(redw), [rx] "v"(redx)
                : "memory", "scc", "vcc", "v96", "v97", "v98", "v99", "v100", "v101", "v102", "v103", "v104", "v105", "v106", "v107", "v108", "v109", "v110", "v111", "v112", "v113", "v114", "v115",
                  "v116", "v117", "v118", "v119", "v120", "v121", "v122", "v123", "v124", "v125");
        }
        if (wave >= 6) {
            const unsigned char* rb = lds + (wave == 7 ? lane : 64 + (lane & 15)) * 80;
            f32x4 r0 = *(const f32x4*)(rb), r1 = *(const f32x4*)(rb + 16), r2 = *(const f32x4*)(rb + 32), r3 = *(const f32x4*)(rb + 48);
            r0 = (r0 + r1) + (r2 + r3);
            if (wave == 7 || lane < 16) st32(pk2s(r0.x + r0.z, r0.y + r0.w), cx.ar, lane * 4, xpub);
        }
        if (wave == 1) { tref = read_ref(tv, cx, adts, xts, pc.p1); pace_next(pc, tref, s, p); pc.slow = rowmax32((int)(tv & 63u)); }
        if (rho == 0 && wave == 1 && s >= 2) { __builtin_amdgcn_s_sleep(5); if (lane == 0) __hip_atomic_store(cx.ctl + CW_S + 16 * (s - 2), 1u, RLX_AGENT); }
    }
    if (key == SENT) __hip_atomic_store(cx.ctl + CW_TMO, 1u, RLX_AGENT);
    if (rho == 0 && wave == 1) {
        u32 tv = ld32(cx.ar, adts, rfl(amap((NSEG - 1) * SLOT + TS_O, par)));
        read_ref(tv, cx, adts, rfl(amap((NSEG - 1) * SLOT + TS_O, par)), pc.p1);
        if (lane == 0) __hip_atomic_store(cx.ctl + CW_S + 16 * (NSEG - 3), 1u, RLX_AGENT);
    }
}

constexpr int L_XT = L_STAGE, L_VI = 0;
template <int TYPE>
DEV void l_worker(const Params& p, const Ctx& cx, unsigned char* lds, int h, int second, int wave) {
    constexpr int K1 = TYPE == 0 ? 3 : 12;
    const int lane = lane_id();
    const u32 lds0 = (u32)(size_t)(LAS unsigned char*)lds;
    const rsrc_t rxin = mkrs(p.x, S * DM * 2);
    const int xrow = (wave & 3) * 8 + (lane >> 3);
    const int xvo = xrow * (DM * 2) + (((lane & 7) ^ ((xrow >> 1) & 7)) * 16) + (5 * (wave >> 2)) * 128;
    const u32 xld = lds0 + L_XT + (5 * (wave >> 2)) * 4096 + (wave & 3) * 1024;
    auto issue1 = [&](int s, int buf, int k) { dma16(rfl(xld + buf * 40960 + k * 4096), xvo, rxin, rfl(min(s, NSEG - 1) * (32 * DM * 2) + k * 128)); };
    const int nit = second ? K1 : NSEG - K1;
    auto seg_of = [&](int it) { return second ? (it < K1 ? 2 * it + 1 : NSEG) : it + min(it, K1); };
    auto first_x = [&]() {
#pragma unroll
        for (int k = 0; k < 5; ++k) issue1(seg_of(0), 2, k);
    };
    v4u w4[40];
    if (TYPE == 0) load_units<1>(p, h, lds, wave, lane, [&](int slot, v2u (&t)[4]) {
        const int ks = slot >> 1, tau = slot & 1;
        w4[(2 * ks) * 2 + tau] = (v4u){t[0].x, t[0].y, t[1].x, t[1].y}; w4[(2 * ks + 1) * 2 + tau] = (v4u){t[2].x, t[2].y, t[3].x, t[3].y}; }, 0, first_x);
    else load_units<2>(p, h, lds, wave, lane, [&](int slot, v2u (&t)[4]) {
        w4[2 * slot] = (v4u){t[0].x, t[0].y, t[1].x, t[1].y}; w4[2 * slot + 1] = (v4u){t[2].x, t[2].y, t[3].x, t[3].y}; }, 0, first_x);
    const int i = lane & 15, gq = lane >> 4;
#pragma unroll
    for (int k = 0; k < 5; ++k) issue1(seg_of(1), 0, k);
    const int sel = cx.par;
    const u32 xb0 = lds0 + L_XT + i * 128 + ((gq ^ (i >> 1)) * 16), xb1 = lds0 + L_XT + i * 128 + (((4 + gq) ^ (i >> 1)) * 16);
    int ost0, ost1, ubase;
    if (TYPE == 0) {
        const int hp = wave & 3;
        if (wave < 4) { ost0 = (i >> 3) * 2048 + (hp >> 1) * 1024 + (16 * (2 * (hp & 1) + (gq >> 1)) + 2 * (i & 7) + (gq & 1)) * 16; ost1 = ost0 + 4096; ubase = QX_L + h * 8192; }
        else { const int ip = i >> 1; ost0 = (i & 1) * 4096 + hp * 1024 + (16 * gq + 4 * (ip & 1) + ((ip >> 1) & 3)) * 16; ost1 = ost0 + 128; ubase = KX_L + h * 8192; }
    } else { ost0 = lane * 16; ost1 = (i >> 2) * 8192 + (h >> 2) * 1024 + (16 * gq + 4 * (h & 3) + (i & 3)) * 16 - h * 1024; ubase = ZX_P + wave * 32768 + h * 1024; }
    const int viw = L_VI + i * 256 + (((4 * (wave >> 1) + gq) ^ i) * 16) + (wave & 1) * 8;
    const int vtok = 8 * (i >> 2) + (i & 3);
    auto signal = [&](int it) { if (wave == 0 && lane == 0) __hip_atomic_store(cx.ctl + CW_P + 16 * (64 * TYPE + 2 * h + second), (u32)seg_of(it + 1), RLX_AGENT); };
    auto phase1 = [&](int it, int buf) {
        const int bn = buf == 0 ? 2 : buf - 1, sn = seg_of(it + 2);
        const u32 x0 = xb0 + buf * 40960, x1 = xb1 + buf * 40960;
        f32x4 d00 = {0, 0, 0, 0}, d01 = d00, d10 = d00, d11 = d00;
        asm volatile("" : "+v"(d00), "+v"(d01), "+v"(d10), "+v"(d11));
        v4u b[8];
#define XRD(slot, ss) do { LDS_RD128(b[2 * (slot)], ((ss) & 1) ? x1 : x0, ((ss) >> 1) * 4096); LDS_RD128(b[2 * (slot) + 1], ((ss) & 1) ? x1 : x0, ((ss) >> 1) * 4096 + 2048); } while (0)
#pragma unroll
        for (int ss = 0; ss < 4; ++ss) XRD(ss, ss);
#pragma unroll
        for (int ss = 0; ss < 20; ++ss) {
            const int sl = ss & 3;
            if (ss < 17) LGKM_WAIT2(6, b[2 * sl], b[2 * sl + 1]); else LGKM_WAIT2(2 * (19 - ss), b[2 * sl], b[2 * sl + 1]);
            if (TYPE == 0) { MF_WA(d00, 2 * ss, b[2 * sl]); MF_WA(d01, 2 * ss, b[2 * sl + 1]); MF_WA(d10, 2 * ss + 1, b[2 * sl]); MF_WA(d11, 2 * ss + 1, b[2 * sl + 1]); }
            else { MF_WA(d00, ss, b[2 * sl]); MF_WA(d01, ss, b[2 * sl + 1]); }
            if (ss + 4 < 20) XRD(sl, ss + 4);
            if (TYPE == 0 && ss == 8) { VMCNT(0); RAWBAR(); if (it > 0) signal(it - 1); }
            if (TYPE == 0 && ss >= 9 && ss < 19 && (ss & 1)) issue1(sn, bn, (ss - 9) >> 1);
            if (TYPE == 1 && ss % 4 == 1) issue1(sn, bn, ss >> 2);
        }
#undef XRD
        asm volatile("s_nop 7" : "+v"(d00), "+v"(d01), "+v"(d10), "+v"(d11));
        if (TYPE == 0) {
            const int so = rfl(amap(ubase + seg_of(it) * (H * 8192), sel));
            u32 a0 = pk2(d00.x, d00.y), a1 = pk2(d00.z, d00.w), a2 = pk2(d10.x, d10.y), a3 = pk2(d10.z, d10.w);
            u32 c0 = pk2(d01.x, d01.y), c1 = pk2(d01.z, d01.w), c2 = pk2(d11.x, d11.y), c3 = pk2(d11.z, d11.w);
            if (wave < 4) {
                auto sa = __builtin_amdgcn_permlane16_swap(a0, a1, false, false), sb = __builtin_amdgcn_permlane16_swap(a2, a3, false, false);
                auto sc = __builtin_amdgcn_permlane16_swap(c0, c1, false, false), sd = __builtin_amdgcn_permlane16_swap(c2, c3, false, false);
                st128((v4u){sa[0], sb[0], sa[1], sb[1]}, cx.ar, ost0, so);
                st128((v4u){sc[0], sd[0], sc[1], sd[1]}, cx.ar, ost1, so);
            } else {
                st128((v4u){a0, a1, a2, a3}, cx.ar, ost0, so);
                st128((v4u){c0, c1, c2, c3}, cx.ar, ost1, so);
            }
        } else {
            *(v2u*)(lds + viw + (it & 1) * 8192) = (v2u){pk2(d00.x, d00.y), pk2(d00.z, d00.w)};
            *(v2u*)(lds + viw + (it & 1) * 8192 + 4096) = (v2u){pk2(d01.x, d01.y), pk2(d01.z, d01.w)};
        }
    };
    auto phase2 = [&](int it, bool mid) {
        const int ub = ubase + seg_of(it) * ZX_STEP;
        v4u a[8];
#pragma unroll
        for (int k = 0; k < 8; ++k) { const int tok = vtok + 4 * (k >> 2); a[k] = *(const v4u*)(lds + L_VI + (it & 1) * 8192 + tok * 256 + (((4 * (k & 3) + gq) ^ (tok & 15)) * 16)); }
        f32x4 d0[5], d1[5];
#pragma unroll
        for (int cg = 0; cg < 5; ++cg) { d0[cg] = (f32x4){0, 0, 0, 0}; d1[cg] = d0[cg]; asm volatile("" : "+v"(d0[cg]), "+v"(d1[cg])); }
        asm volatile("s_nop 1");
#pragma unroll
        for (int cg = 0; cg < 6; ++cg) {
            if (cg < 5) {
#pragma unroll
                for (int ss = 0; ss < 4; ++ss) { MF_WB(d0[cg], a[ss], 20 + cg * 4 + ss); MF_WB(d1[cg], a[4 + ss], 20 + cg * 4 + ss); }
            } else asm volatile("s_nop 7");
            if (cg == 0 && mid) { VMCNT(5); BAR(); if (it > 0) signal(it - 1); }
            if (cg > 0) {
                asm volatile("" : "+v"(d0[cg - 1]), "+v"(d1[cg - 1]));
                st128((v4u){pk2(d0[cg - 1].x, d0[cg - 1].y), pk2(d0[cg - 1].z, d0[cg - 1].w), pk2(d1[cg - 1].x, d1[cg - 1].y), pk2(d1[cg - 1].z, d1[cg - 1].w)},
                      cx.ar, cg == 5 ? ost1 + 32768 : ost0, ub + (cg - 1) * (8 * 32768) - (cg == 5 ? 32768 : 0));
            }
        }
    };
    VMCNT(5);
    BAR();
    if (TYPE == 0) {
        int buf = 2;
        for (int it = 0; it < nit; ++it) {
            phase1(it, buf);
            buf = buf == 2 ? 0 : buf + 1;
            if (it < 1) { VMCNT(0); BAR(); signal(it); }
        }
    } else {
        phase1(0, 2);
        BAR();
        phase2(0, false);
        VMCNT(0); BAR(); signal(0);
        phase1(1, 0);
        VMCNT(5); BAR();
        int buf = 1;
        for (int k = 1; k < nit; ++k) {
            phase1(k + 1, buf);
            phase2(k, true);
            if (k < 1) { VMCNT(0); BAR(); signal(k); }
            buf = buf == 2 ? 0 : buf + 1;
        }
    }
    VMCNT(0);
    BAR();
    signal(nit - 1);
}

constexpr int SS_K = 33 * 144, SS_U = 4288, SS_CH = 2 * SS_K + 2 * SS_U + 32;
static_assert(SS_CH % 16 == 0 && 8 * SS_CH <= L_TOTAL, "ssm lds");
DEV void ssm_task(const Params& p, const Ctx& cx, unsigned char* lds, int task, int wave, int far) {
    const int lane = lane_id(), tid = wave * 64 + lane;
    const int d = task * 8 + wave;
    unsigned char* ch = lds + wave * SS_CH;
    unsigned char* tmpb = ch + 2 * SS_K;
    const int i = lane & 15, gq = lane >> 4;
    v4u xr[2][3];
    {
        const float dt = rbf(__expf(bf2f(p.logdt[d])));
#pragma unroll
        for (int k = 0; k < 2; ++k) {
            const int nn = lane + 64 * k, o = d * NS + nn;
            const float br = dt * bf2f(p.Bre[o]), bi = dt * bf2f(p.Bim[o]), cr = bf2f(p.Cre[o]), ci = bf2f(p.Cim[o]);
            *(f32x4*)(tmpb + nn * 16) = (f32x4){dt * bf2f(p.Are[o]) * 1.4426950408889634f, dt * bf2f(p.Aim[o]) * 0.15915494309189535f, cr * br - ci * bi, cr * bi + ci * br};
        }
        asm volatile("s_waitcnt lgkmcnt(0)" ::: "memory");
        {
            const rsrc_t rxin = mkrs(p.x, S * DM * 2);
#pragma unroll
            for (int k = 0; k < 2; ++k)
#pragma unroll
                for (int j = 0; j < 3; ++j) xr[k][j] = __builtin_amdgcn_raw_buffer_load_b128(rxin, ((2 * (tid + 512 * k) + j) * DM + task * 8) * 2, 0, 0);
            __builtin_amdgcn_sched_barrier(0);
        }
        const float lu = 16.f * i;
        const float lv0 = (float)i;
        const float lv1 = i < 8 ? 256.f + 2.f * i : 512.f + 256.f * ((i - 8) >> 2) + 4.f * (i & 3);
        const float lv2 = i < 8 ? 1024.f + 256.f * (i >> 1) + 8.f * (i & 1) : 2048.f;
        f32x4 k0 = {0, 0, 0, 0}, k1 = k0, k2 = k0;
#pragma unroll 2
        for (int it = 0; it < 32; ++it) {
            const f32x4 q = *(const f32x4*)(tmpb + (4 * it + gq) * 16);
            auto cexp = [&](float l, float& re, float& im) {
                const float e = __builtin_amdgcn_exp2f(q.x * l), th = __builtin_amdgcn_fractf(q.y * l);
                re = e * __builtin_amdgcn_cosf(th); im = e * __builtin_amdgcn_sinf(th);
            };
            float er, ei, r0, i0, r1, i1, r2, i2;
            cexp(lu, er, ei); cexp(lv0, r0, i0); cexp(lv1, r1, i1); cexp(lv2, r2, i2);
            const float ur = q.z * er - q.w * ei, nui = -(q.z * ei + q.w * er);
            k0 = __builtin_amdgcn_mfma_f32_16x16x4f32(ur, r0, k0, 0, 0, 0); k1 = __builtin_amdgcn_mfma_f32_16x16x4f32(ur, r1, k1, 0, 0, 0); k2 = __builtin_amdgcn_mfma_f32_16x16x4f32(ur, r2, k2, 0, 0, 0);
            k0 = __builtin_amdgcn_mfma_f32_16x16x4f32(nui, i0, k0, 0, 0, 0); k1 = __builtin_amdgcn_mfma_f32_16x16x4f32(nui, i1, k1, 0, 0, 0); k2 = __builtin_amdgcn_mfma_f32_16x16x4f32(nui, i2, k2, 0, 0, 0);
        }
        float* tab = (float*)(tmpb + 2048);
        *(f32x4*)(tab + i * 20 + 4 * gq) = k0; *(f32x4*)(tab + (16 + i) * 20 + 4 * gq) = k1; *(f32x4*)(tab + (32 + i) * 20 + 4 * gq) = k2;
        asm volatile("s_waitcnt lgkmcnt(0)" ::: "memory");
        if (lane < 36) { *(u32*)(ch + lane * 4) = 0; *(u32*)(ch + SS_K + lane * 4) = 0; }
#pragma unroll 2
        for (int it = 0; it < 16; ++it) {
            const int l0 = it * 128 + lane * 2;
            float kv[2];
#pragma unroll
            for (int e = 0; e < 2; ++e) {
                const int lr = (int)rbf((float)(l0 + e));
                const int a = (lr >> 4) & 15, j = lr & 15, hi = lr >> 8;
                int col;
                if (hi == 0) col = j; else if (hi == 1) col = 16 + (j >> 1); else if (hi < 4) col = 24 + 4 * (hi - 2) + (j >> 2); else if (hi < 8) col = 32 + 2 * (hi - 4) + (j >> 3); else col = 40;
                kv[e] = tab[col * 20 + a];
            }
            const float h0 = rbf(kv[0]), h1 = rbf(kv[1]);
            const int o = ((l0 >> 6) + 1) * 144 + ((l0 & 56) + 6 - (l0 & 6)) * 2;
            *(u32*)(ch + o) = pk2(h1, h0);
            *(u32*)(ch + SS_K + o) = pk2(kv[1] - h1, kv[0] - h0);
        }
    }
    BAR();
    {
        if (lane < 24) { *(u32*)(ch + 2 * SS_K + lane * 4) = 0; if (lane < 23) *(u32*)(ch + 2 * SS_K + SS_U + lane * 4) = 0; }
#pragma unroll
        for (int k = 0; k < 2; ++k) {
            const int l = 2 * (tid + 512 * k);
#pragma unroll
            for (int c = 0; c < 8; ++c) {
                const u32 sel = (c & 1) ? 0x07060302u : 0x05040100u;
                unsigned char* ub = lds + c * SS_CH + 2 * SS_K;
                *(u32*)(ub + (l + 48) * 2) = __builtin_amdgcn_perm(xr[k][1][c >> 1], xr[k][0][c >> 1], sel);
                *(u32*)(ub + SS_U + (l + 48) * 2) = __builtin_amdgcn_perm(xr[k][2][c >> 1], xr[k][1][c >> 1], sel);
                if (k == 0 && tid == 0) *(u32*)(ub + SS_U + 46 * 2) = __builtin_amdgcn_perm(xr[0][0][c >> 1], 0u, sel);
            }
        }
    }
    BAR();
    f32x4 acc[2][4];
#pragma unroll
    for (int I = 0; I < 2; ++I)
#pragma unroll
        for (int rt = 0; rt < 4; ++rt) acc[I][rt] = (f32x4){0, 0, 0, 0};
    {
        const int st0 = i - 8 * gq - 7 + 48;
        const unsigned char* ua = ch + 2 * SS_K + ((st0 & 1) ? SS_U + (st0 - 1) * 2 : st0 * 2);
        auto afrag = [&](int o) { const u32* q = (const u32*)(ua + o * 32); return (v4u){q[0], q[1], q[2], q[3]}; };
        v4u am2 = {0, 0, 0, 0};
        v4u am1 = afrag(-1);
        const int kb = gq * 16;
        auto step = [&](int t, int I, const v4u& a0, const v4u& a1, const v4u& a2, const v4u& a3) {
            const int blk = max(16 * I + i - t + 1, 0);
            const unsigned char* kp = ch + blk * 144 + kb;
#pragma unroll
            for (int hl = 0; hl < 2; ++hl) {
                const v4u b0 = *(const v4u*)(kp + hl * SS_K), b1 = *(const v4u*)(kp + hl * SS_K + 64);
                acc[I][0] = mfma16(a0, b0, acc[I][0]); acc[I][1] = mfma16(a1, b0, acc[I][1]); acc[I][2] = mfma16(a2, b0, acc[I][2]); acc[I][3] = mfma16(a3, b0, acc[I][3]);
                acc[I][0] = mfma16(am2, b1, acc[I][0]); acc[I][1] = mfma16(am1, b1, acc[I][1]); acc[I][2] = mfma16(a0, b1, acc[I][2]); acc[I][3] = mfma16(a1, b1, acc[I][3]);
            }
        };
#pragma unroll 1
        for (int t = 0; t < 16; ++t) {
            const v4u a0 = afrag(4 * t), a1 = afrag(4 * t + 1), a2 = afrag(4 * t + 2), a3 = afrag(4 * t + 3);
            step(t, 0, a0, a1, a2, a3); step(t, 1, a0, a1, a2, a3);
            am2 = a2; am1 = a3;
        }
#pragma unroll 1
        for (int t = 16; t < 32; ++t) {
            const v4u a0 = afrag(4 * t), a1 = afrag(4 * t + 1), a2 = afrag(4 * t + 2), a3 = afrag(4 * t + 3);
            step(t, 1, a0, a1, a2, a3);
            am2 = a2; am1 = a3;
        }
    }
    {
        const float dsk = bf2f(p.dskip[d]);
        asm volatile("s_waitcnt lgkmcnt(0)" ::: "memory");
#pragma unroll
        for (int I = 0; I < 2; ++I)
#pragma unroll
            for (int rt = 0; rt < 4; ++rt) {
                const int l = 64 * (16 * I + i) + 16 * rt + 4 * gq;
                const v2u uu = *(const v2u*)(ch + 2 * SS_K + (l + 48) * 2);
                const f32x4 y = acc[I][rt];
                const float b0 = rbf(y.x) + rbf(bf2f(uu.x & 0xffffu) * dsk), b1 = rbf(y.y) + rbf(bfhi(uu.x) * dsk), b2 = rbf(y.z) + rbf(bf2f(uu.y & 0xffffu) * dsk), b3 = rbf(y.w) + rbf(bfhi(uu.y) * dsk);
                *(v2u*)(ch + (16 * I + i) * 136 + (16 * rt + 4 * gq) * 2) = (v2u){pk2(b0, b1), pk2(b2, b3)};
            }
    }
    BAR();
#pragma unroll
    for (int k = 0; k < 2; ++k) {
        const int l = 2 * (tid + 512 * k);
        u32 e[8];
#pragma unroll
        for (int c = 0; c < 8; ++c) e[c] = *(const u32*)(lds + c * SS_CH + (l >> 6) * 136 + (l & 63) * 2);
        v4u lo, hi;
#pragma unroll
        for (int c = 0; c < 4; ++c) { lo[c] = __builtin_amdgcn_perm(e[2 * c + 1], e[2 * c], 0x05040100u); hi[c] = __builtin_amdgcn_perm(e[2 * c + 1], e[2 * c], 0x07060302u); }
        const int so = rfl(amap(BS_L + task * 32768 + k * 16384 + wave * 2048, far));
        st128(lo, cx.ar, lane * 32, so); st128(hi, cx.ar, lane * 32 + 16, so);
    }
    VMCNT(0);
    BAR();
    if (tid == 0) __hip_atomic_fetch_add(cx.ctl + CW_SSM + 64 * (task >> 4), 1u, RLX_AGENT);
}

constexpr int L_OP = 0, L_OA = 81920, L_OG = L_OA + 32 * 272, L_OT = L_OG + 128;
DEV void out_task(const Params& p, const Ctx& cx, unsigned char* lds, int s, int cgp, int wave, int& ready) {
    const int lane = lane_id(), tid = wave * 64 + lane, far = cx.par ^ 1;
    const int n = lane & 15, gq = lane >> 4, gl = lane * 16;
    SIDX_DECL;
    const int ct = 8 * cgp + wave;
    const int zl = cgp < 4 ? gl : (n >> 2) * 8192 + gq * 256 + (n & 3) * 16;
    if (wave == 0 && s >= ready) ready = wait_upto<1>(prog_load<1>(0, lane, cx.ctl), s, 0, lane, cx.ctl);
    BAR();
    v8u wr[20];
#pragma unroll
    for (int k = 0; k < 16; ++k) {
        const int so = ZX_P + s * ZX_STEP + ct * 32768 + (cgp < 4 ? k * 2048 : (k >> 1) * 1024 + (k & 1) * 128);
        wr[k] = cat8(ld128(cx.ar, zl, so), ld128(cx.ar, zl, so + (cgp < 4 ? 1024 : 64)));
    }
    const int otok = tid >> 4, oc = tid & 15;
    const v4u bv = ld128(cx.ar, amap(BS_L + (16 * cgp + oc) * 32768 + (s * 32 + otok) * 16, far), 0);
    {
        const rsrc_t rxin = mkrs(p.x, S * DM * 2), rg = mkrs(p.gw, DM * 2);
        const int tok = wave * 4 + gq;
        float sum = 0.f;
#pragma unroll
        for (int k = 0; k < 5; ++k) {
            const v4u xv = __builtin_amdgcn_raw_buffer_load_b128(rxin, ((s * 32 + tok) * DM + k * 128 + n * 8) * 2, 0, 0);
            const v4u gv = __builtin_amdgcn_raw_buffer_load_b128(rg, (k * 128 + n * 8) * 2, 0, 0);
#pragma unroll
            for (int e = 0; e < 4; ++e) sum += rbf(bf2f(xv[e] & 0xffffu) * bf2f(gv[e] & 0xffffu)) + rbf(bfhi(xv[e]) * bfhi(gv[e]));
        }
        sum = rbf(rowsum16(sum));
        const float g = rbf(1.f / (1.f + __expf(-sum)));
        if (n == 0) *(float*)(lds + L_OG + tok * 4) = g;
    }
    if (wave == 0) {
        if (s < NSEG - 2) wait_ge(cx.ctl + CW_S + 16 * s, 1u, cx.ctl);
        else {
            u32 spins = 0;
            for (;;) {
                const u32 a = __hip_atomic_load(cx.ctl + CW_FIN + 16 * lane, RLX_AGENT), c = __hip_atomic_load(cx.ctl + CW_FIN + 16 * (64 + (lane & 31)), RLX_AGENT);
                if (__all(a && c)) break;
                __builtin_amdgcn_s_sleep(2);
                if (spin_fail(spins, cx.ctl)) break;
            }
        }
    }
    BAR();
    {
        v4u t[10];
#pragma unroll
        for (int k = 0; k < 10; ++k) t[k] = ld128(cx.ar, gl, rfl(amap(PT_L + s * PT_STEP + (k < 8 ? wave * 8 + k : 48 + wave * 2 + 8 + k) * 1024, far)));
        const int so = rfl(amap(ZM_L + s * ZM_STEP + ct * 8192, cx.par));
#pragma unroll
        for (int k = 0; k < 4; ++k) wr[16 + k] = cat8(ld128(cx.ar, gl, so + k * 2048), ld128(cx.ar, gl + 1024, so + k * 2048));
#pragma unroll
        for (int k = 0; k < 8; ++k) *(v4u*)(lds + L_OP + (wave * 8 + k) * 1024 + gl) = t[k];
#pragma unroll
        for (int k = 8; k < 10; ++k) {
            unsigned char* d = lds + L_OP + (56 + wave * 2 + k) * 1024 + gq * 256 + (lane & 7) * 32 + ((lane >> 3) & 1) * 8;
            *(v2u*)d = (v2u){t[k].x, t[k].y}; *(v2u*)(d + 16) = (v2u){t[k].z, t[k].w};
        }
    }
    BAR();
    f32x4 acc[4];
#pragma unroll
    for (int g = 0; g < 4; ++g) acc[g] = (f32x4){0, 0, 0, 0};
    asm volatile("s_nop 1" : "+v"(acc[0]), "+v"(acc[1]), "+v"(acc[2]), "+v"(acc[3]));
    {
        const u32 lb0 = (u32)(size_t)(LAS unsigned char*)lds + L_OP + gl, lb1 = lb0 + 65536;
        v4u a[8];
#define PRD(i) do { if ((i) < 64) LDS_RD128(a[(i) & 7], lb0, (((i) & 3) * 16 + ((i) >> 2)) * 1024); else LDS_RD128(a[(i) & 7], lb1, (((i) & 3) * 4 + (((i) - 64) >> 2)) * 1024); } while (0)
#pragma unroll
        for (int i = 0; i < 8; ++i) PRD(i);
#pragma unroll
        for (int i = 0; i < 80; ++i) {
            LGKM_WAIT(i < 73 ? 7 : 79 - i, a[i & 7]);
            SMF_W(acc[i & 3], a[i & 7], i >> 2);
            if (i + 8 < 80) PRD(i + 8);
        }
#undef PRD
    }
    asm volatile("s_nop 7" : "+v"(acc[0]), "+v"(acc[1]), "+v"(acc[2]), "+v"(acc[3]));
#pragma unroll
    for (int g = 0; g < 4; ++g) {
        const u32 v = pk2(acc[g].x + acc[g].y, acc[g].z + acc[g].w);
        unsigned char* o = lds + L_OA + (8 * g + 2 * gq) * 272 + (wave * 16 + n) * 2;
        *(u16*)o = (u16)v; *(u16*)(o + 272) = (u16)(v >> 16);
    }
    BAR();
    {
        const v4u av = *(const v4u*)(lds + L_OA + otok * 272 + oc * 16);
        const float g = *(const float*)(lds + L_OG + otok * 4), og = rbf(1.f - g);
        v4u o;
#pragma unroll
        for (int e = 0; e < 4; ++e)
            o[e] = pk2(rbf(g * bf2f(av[e] & 0xffffu)) + rbf(og * bf2f(bv[e] & 0xffffu)), rbf(g * bfhi(av[e])) + rbf(og * bfhi(bv[e])));
        *(v4u*)(p.out + (s * 32 + otok) * DM + cgp * 128 + oc * 8) = o;
    }
}

DEV unsigned char* arena_base(unsigned char* ws) { return (unsigned char*)(((size_t)ws + CTRL_BYTES + (2u << 20) - 1) & ~(size_t)((2u << 20) - 1)); }

__global__ void __launch_bounds__(512) gm_init(unsigned char* ws) {
    const int tid = threadIdx.x, b = blockIdx.x;
    const rsrc_t ar = mkrs(arena_base(ws), ARENA_BYTES);
    if (b == 0) {
        if (tid >= 64) return;
        u32 mn[2] = {100000u, 100000u};
        for (int it = 0; it < 3; ++it)
#pragma unroll
            for (int q = 0; q < 2; ++q) {
                const u32 c0 = now32();
                st32(0u, ar, tid * 4, (4 << 20) + it * 256 + q * 8192);
                VMCNT(0);
                mn[q] = min(mn[q], now32() - c0);
            }
        if (tid < 32) ((u32*)ws)[CW_PAR + tid] = mn[1] < mn[0];
    } else if (b == 1) {
        for (int i = tid; i < CTRL_BYTES / 16; i += 512) if (i < CW_PAR / 4 || i >= CW_PAR / 4 + 8) ((v4u*)ws)[i] = (v4u){0, 0, 0, 0};
    } else {
        const v4u sv = {SENT, SENT, SENT, SENT};
#pragma unroll
        for (int k = 0; k < 4; ++k) st128(sv, ar, (b < 130 ? (b - 2) * 32768 : 2 * ZM_L + (b - 130) * 32768) + k * 8192 + tid * 16, 0);
    }
}

__global__ void __launch_bounds__(512, 2) gm_kernel(Params p) {
    extern __shared__ __attribute__((aligned(16))) unsigned char lds[];
    const int b = blockIdx.x;
    const int wave = rfl(threadIdx.x >> 6);
    Ctx cx;
    cx.ctl = (GAS u32*)p.ws;
    cx.ar = mkrs(arena_base(p.ws), ARENA_BYTES);
    cx.par = rfl(cx.ctl[CW_PAR]) & 1;
    if ((b & 7) >= 4) {
        const int u = (b >> 3) * 4 + (b & 7) - 4, j = u >> 2;
        const int h = 8 * (u & 3) + (j >> 2), type = (j >> 1) & 1, second = j & 1;
        if (type) l_worker<1>(p, cx, lds, h, second, wave); else l_worker<0>(p, cx, lds, h, second, wave);
        if (!second && type) return;
        if (second) {
#pragma unroll 1
            for (int t = type * 32 + h; t < 80; t += 48 + 16 * type + (h < 16 ? 64 : 0)) ssm_task(p, cx, lds, t, wave, cx.par ^ 1);
        }
        const bool tick = !type && (!second || h >= 16);
        const int nb = type ? 16 : 8, cgp = tick ? 4 : type ? 2 + h / 16 : h / 8;
        if (wave == 0) { wait_ge(cx.ctl + CW_SSM + 64 * cgp, 16u, cx.ctl); for (int k = 0; k < h % 16; ++k) __builtin_amdgcn_s_sleep(14); }
        int ready = 0, s = h % nb - nb;
#pragma unroll 1
        for (;;) {
            s += nb;
            if (tick) {
                if (wave == 0 && lane_id() == 0) *(u32*)(lds + L_OT) = __hip_atomic_fetch_add(cx.ctl + CW_OQ, 1u, RLX_AGENT);
                BAR();
                s = rfl(*(const u32*)(lds + L_OT));
            }
            if (s >= NSEG) break;
            out_task(p, cx, lds, s, cgp, wave, ready);
        }
        return;
    }
    const int w = (b >> 3) * 4 + (b & 7);
    if (w < 32) {
        if (wave < 2) a_role<0>(p, cx, lds, w, wave);
        else if (wave < 6) a_role<1>(p, cx, lds, w, wave);
        else a_role<2>(p, cx, lds, w, wave);
    } else if (w < 96) b_role(p, cx, lds, w - 32, wave);
    else r_role(p, cx, lds, w - 96, wave);
}


extern "C" void kernel_launch(void* const* d_in, const int* in_sizes, int n_in,
                              void* d_out, int out_size, void* d_ws, size_t ws_size,
                              hipStream_t stream) {
    Params p;
    p.x = (const u16*)d_in[0]; p.mem0 = (const u16*)d_in[1]; p.Wq = (const u16*)d_in[2]; p.Wk = (const u16*)d_in[3]; p.Wv = (const u16*)d_in[4]; p.Wo = (const u16*)d_in[5];
    p.Are = (const u16*)d_in[8]; p.Aim = (const u16*)d_in[9]; p.Bre = (const u16*)d_in[10]; p.Bim = (const u16*)d_in[11]; p.Cre = (const u16*)d_in[12]; p.Cim = (const u16*)d_in[13];
    p.logdt = (const u16*)d_in[14]; p.dskip = (const u16*)d_in[15]; p.gw = (const u16*)d_in[16];
    p.out = (u16*)d_out; p.ws = (unsigned char*)d_ws;
    p.c3 = 1700; p.c2 = 2710; p.gmax = 1150; p.cmL = 650;
    gm_init<<<170, 512, 0, stream>>>((unsigned char*)d_ws);
    (void)hipFuncSetAttribute((const void*)gm_kernel, hipFuncAttributeMaxDynamicSharedMemorySize, L_TOTAL);
    gm_kernel<<<256, 512, L_TOTAL, stream>>>(p);
}
```
